```python
import torch

def _softmax(x, axis=-1):
    return torch.softmax(x, dim=axis)
def _gelu(x):
    return torch.nn.functional.gelu(x, approximate="tanh")
def _silu(x):
    return torch.nn.functional.silu(x)
def _sigmoid(x):
    return torch.sigmoid(x)
def _layernorm(x, axis=-1, eps=1e-5):
    m = x.mean(dim=axis, keepdim=True)
    v = x.var(dim=axis, keepdim=True, unbiased=False)
    return (x - m) / torch.sqrt(v + eps)
def _scatter_add(tgt, idx, src, axis=0):
    return tgt.clone().index_add_(axis, idx, src)
def _segment_sum(src, seg, n):
    out = torch.zeros((n,) + src.shape[1:], dtype=src.dtype)
    return out.index_add_(0, seg, src)
def _pad(x, pw):
    flat = tuple(v for pair in reversed(pw) for v in pair)
    return torch.nn.functional.pad(x, flat)

kernel_name = 'sequential_preLN__compressed_kv_cla__mixer_token_mlp__prompt'

def setup_inputs():

    _w = (lambda a: (lambda t: t.to(torch.bfloat16) if t.is_floating_point() else t)(torch.as_tensor(a)))
    def _A_setup():
        B, H, Sq, Sk, D, M = 1, 32, 2048, 2048, 128, 640
        Dc = 4 * D
        return {
            "x": _w((lambda *s: torch.randn(*s, dtype=torch.bfloat16) * 0.05)(B, Sq, M)),
            "c_kv": _w((lambda *s: torch.randn(*s, dtype=torch.bfloat16) * 0.2)(B, Sk, Dc)),
            "w_q": _w((lambda *s: torch.randn(*s, dtype=torch.bfloat16) * 3.2)(M, H, D) * (M ** -0.5)),
            "w_uk": _w((lambda *s: torch.randn(*s, dtype=torch.bfloat16) * 6.4)(Dc, H, D) * (Dc ** -0.5)),
            "w_uv": _w((lambda *s: torch.randn(*s, dtype=torch.bfloat16) * 0.2)(Dc, H, D) * (Dc ** -0.5)),
            "w_o": _w((lambda *s: torch.randn(*s, dtype=torch.bfloat16) * 0.2)(H, D, M) * ((H * D) ** -0.5)),
        }
    _A_in = _A_setup()

    def _B_setup():
        B, S, D, F = 1, 2048, 640, 1728
        return {
            "x": _w((lambda *s: torch.randn(*s, dtype=torch.bfloat16) * 0.05)(B, S, D)),
            "w1": _w((lambda *s: torch.randn(*s, dtype=torch.bfloat16) * 0.4)(S, F) * (S ** -0.5)),
            "b1": _w((lambda *s: torch.randn(*s, dtype=torch.bfloat16) * 3.2)(F) * 0.02),
            "w2": _w((lambda *s: torch.randn(*s, dtype=torch.bfloat16) * 0.2)(F, S) * (F ** -0.5)),
            "b2": _w((lambda *s: torch.randn(*s, dtype=torch.bfloat16) * 0.8)(S) * 0.02),
        }
    _B_in = _B_setup()

    return {
        "A_x": _A_in["x"],
        "A_c_kv": _A_in["c_kv"],
        "A_w_q": _A_in["w_q"],
        "A_w_uk": _A_in["w_uk"],
        "A_w_uv": _A_in["w_uv"],
        "A_w_o": _A_in["w_o"],
        "B_x": _B_in["x"],
        "B_w1": _B_in["w1"],
        "B_b1": _B_in["b1"],
        "B_w2": _B_in["w2"],
        "B_b2": _B_in["b2"],
    }

def reference(A_x, A_c_kv, A_w_q, A_w_uk, A_w_uv, A_w_o, B_x, B_w1, B_b1, B_w2, B_b2):
    def _A_ref(x, c_kv, w_q, w_uk, w_uv, w_o):
        B, Sq, M = x.shape[0], x.shape[1], x.shape[2]
        Sk = c_kv.shape[1]
        H, D = w_q.shape[1], w_q.shape[2]
        q = torch.einsum("bqm,mhd->bhqd", x, w_q)
        k = torch.einsum("bkc,chd->bhkd", c_kv, w_uk)
        v = torch.einsum("bkc,chd->bhkd", c_kv, w_uv)
        scores = torch.einsum("bhqd,bhkd->bhqk", q, k) * (D ** -0.5)
        off = Sk - Sq
        qi = torch.arange(Sq)[:, None]
        kj = torch.arange(Sk)[None, :]
        cmask = kj <= (qi + off)
        scores = torch.where(cmask[None, None], scores, -1e9)
        p = _softmax(scores, axis=-1)
        ctx = torch.einsum("bhqk,bhkd->bhqd", p, v)
        return torch.einsum("bhqd,hdm->bqm", ctx, w_o)

    def _B_ref(x, w1, b1, w2, b2):
        B, S, D = x.shape
        h = _layernorm(x, axis=-1)
        u = torch.einsum("bsd,sf->bdf", h, w1) + b1
        u = _gelu(u)
        y = torch.einsum("bdf,fs->bds", u, w2) + b2
        return (lambda x, *d: x.permute(*d))(y, 0, 2, 1)

    x = A_x
    h = x + _A_ref(_layernorm(x, axis=-1), A_c_kv, A_w_q, A_w_uk, A_w_uv, A_w_o)
    h2 = h + _B_ref(_layernorm(h, axis=-1), B_w1, B_b1, B_w2, B_b2)
    return h2
```

```cpp
#include <hip/hip_runtime.h>
#include <stdint.h>

#define LAS __attribute__((address_space(3)))
#define DI __device__ __forceinline__
typedef unsigned short u16;
typedef short bf16x8 __attribute__((ext_vector_type(8)));
typedef short s16x4 __attribute__((ext_vector_type(4)));
typedef float f32x2 __attribute__((ext_vector_type(2)));
typedef float f32x4 __attribute__((ext_vector_type(4)));
typedef float f32x16 __attribute__((ext_vector_type(16)));
typedef unsigned u32x2 __attribute__((ext_vector_type(2)));
typedef unsigned u32x4 __attribute__((ext_vector_type(4)));
typedef __bf16 bf16x2_t __attribute__((ext_vector_type(2)));
typedef LAS unsigned char* ldsp;
typedef LAS const unsigned char* ldscp;

constexpr int SEQ = 2048, DM = 640, DC = 512, NH = 32, HD = 128, FF = 1728;
constexpr float LN_EPS = 1e-5f;

constexpr size_t WS_FLAGS = 0;
constexpr size_t WS_XNF = 4096;
constexpr size_t WS_KF = WS_XNF + (size_t)SEQ * DM * 2;
constexpr size_t WS_VF = WS_KF + (size_t)NH * SEQ * HD * 2;
constexpr size_t WS_CTX = WS_VF + (size_t)NH * SEQ * HD * 2;
constexpr size_t WS_SLAB = WS_CTX + (size_t)NH * SEQ * HD * 2;
constexpr size_t WS_H = WS_SLAB + (size_t)4 * SEQ * DM * 4;
constexpr size_t WS_HNF = WS_H + (size_t)SEQ * DM * 2;
constexpr size_t WS_UF = WS_HNF + (size_t)SEQ * DM * 2;
constexpr size_t WS_END = WS_UF + (size_t)DM * FF * 2;

#define SBAR() __builtin_amdgcn_sched_barrier(0)
#define BAR() asm volatile("s_barrier" ::: "memory")
#define WAIT_V(n) asm volatile("s_waitcnt vmcnt(" #n ")" ::: "memory")
#define WAIT_L(n) asm volatile("s_waitcnt lgkmcnt(" #n ")" ::: "memory")
#define MFMA32(a, b, c) __builtin_amdgcn_mfma_f32_32x32x16_bf16((a), (b), (c), 0, 0, 0)
#define MFMA16(a, b, c) __builtin_amdgcn_mfma_f32_16x16x32_bf16((a), (b), (c), 0, 0, 0)

DI unsigned cvtpk(float lo, float hi) { f32x2 v = {lo, hi}; bf16x2_t b = __builtin_convertvector(v, bf16x2_t); return __builtin_bit_cast(unsigned, b); }
DI float bflo(unsigned u) { return __uint_as_float(u << 16); }
DI float bfhi(unsigned u) { return __uint_as_float(u & 0xffff0000u); }
DI unsigned rfl(unsigned x) { return __builtin_amdgcn_readfirstlane(x); }
DI void glds16(unsigned voff, const void* sbase, unsigned lds_dst) {
  unsigned keep;
  asm volatile("s_mov_b32 %0, m0\n\ts_mov_b32 m0, %3\n\ts_nop 0\n\tglobal_load_lds_dwordx4 %1, %2\n\ts_mov_b32 m0, %0"
               : "=&s"(keep) : "v"(voff), "s"(sbase), "s"(lds_dst) : "memory");
}
DI void glds16x4(unsigned voff, const void* sbase, unsigned lds_dst) {
  unsigned keep;
  asm volatile("s_mov_b32 %0, m0\n\ts_mov_b32 m0, %3\n\ts_nop 0\n\tglobal_load_lds_dwordx4 %1, %2\n\tglobal_load_lds_dwordx4 %1, %2 offset:1024\n\t"
               "global_load_lds_dwordx4 %1, %2 offset:2048\n\tglobal_load_lds_dwordx4 %1, %2 offset:3072\n\ts_mov_b32 m0, %0"
               : "=&s"(keep) : "v"(voff), "s"(sbase), "s"(lds_dst) : "memory");
}
typedef short v4i16_t __attribute__((ext_vector_type(4)));
DI s16x4 vtr(ldscp p) { return __builtin_bit_cast(s16x4, __builtin_amdgcn_ds_read_tr16_b64_v4i16((LAS v4i16_t*)p)); }
DI bf16x8 cat8(s16x4 lo, s16x4 hi) { return __builtin_shufflevector(lo, hi, 0, 1, 2, 3, 4, 5, 6, 7); }
DI bf16x8 ldsr128(ldscp p) { return *(const LAS bf16x8*)p; }
DI bf16x8 pack8(const f32x16& x, int s) {
  u32x4 w = {cvtpk(x[8 * s], x[8 * s + 1]), cvtpk(x[8 * s + 2], x[8 * s + 3]), cvtpk(x[8 * s + 4], x[8 * s + 5]), cvtpk(x[8 * s + 6], x[8 * s + 7])};
  return __builtin_bit_cast(bf16x8, w);
}
DI float wave_sum(float v) {
#pragma unroll
  for (int o = 32; o >= 1; o >>= 1) v += __shfl_xor(v, o);
  return v;
}

__global__ __launch_bounds__(512) void k0_ln(const u16* __restrict__ x, u16* __restrict__ xnf, unsigned* __restrict__ flags) {
  const int w = threadIdx.x >> 6, lane = threadIdx.x & 63;
  const int q = blockIdx.x * 8 + w;
  if (blockIdx.x == 0) { flags[threadIdx.x] = 0u; flags[512 + threadIdx.x] = 0u; }
  const u32x4* row = (const u32x4*)(x + (size_t)q * DM);
  u32x4 c0 = row[lane];
  u32x4 c1 = {0u, 0u, 0u, 0u};
  if (lane < 16) c1 = row[64 + lane];
  float v[16];
#pragma unroll
  for (int i = 0; i < 4; ++i) { v[2 * i] = bflo(c0[i]); v[2 * i + 1] = bfhi(c0[i]); v[8 + 2 * i] = bflo(c1[i]); v[8 + 2 * i + 1] = bfhi(c1[i]); }
  float s = 0.f;
#pragma unroll
  for (int i = 0; i < 16; ++i) s += v[i];
  const float mean = wave_sum(s) * (1.0f / DM);
  float ss = 0.f;
#pragma unroll
  for (int i = 0; i < 8; ++i) { const float d = v[i] - mean; ss += d * d; }
  if (lane < 16) {
#pragma unroll
    for (int i = 8; i < 16; ++i) { const float d = v[i] - mean; ss += d * d; }
  }
  const float rstd = 1.0f / sqrtf(wave_sum(ss) * (1.0f / DM) + LN_EPS);
  u32x4 o0, o1;
#pragma unroll
  for (int i = 0; i < 4; ++i) { o0[i] = cvtpk((v[2 * i] - mean) * rstd, (v[2 * i + 1] - mean) * rstd); o1[i] = cvtpk((v[8 + 2 * i] - mean) * rstd, (v[8 + 2 * i + 1] - mean) * rstd); }
  u32x4* dst = (u32x4*)xnf + (size_t)(q >> 5) * 40 * 64 + (q & 31);
  dst[(lane >> 1) * 64 + (lane & 1) * 32] = o0;
  if (lane < 16) dst[((64 + lane) >> 1) * 64 + (lane & 1) * 32] = o1;
}

DI void kv_phase(unsigned char* smem, const u16* __restrict__ ckv, const u16* __restrict__ wuk, const u16* __restrict__ wuv,
                 u16* __restrict__ Kf, u16* __restrict__ Vf, int h, int j) {
  const int tid = threadIdx.x, lane = tid & 63;
  const int wid = rfl(tid >> 6), wr = wid >> 2, wc = wid & 3;
  const int l31 = lane & 31, hh = lane >> 5;
  ldsp lds = (ldsp)smem;
  const unsigned lds0 = (unsigned)(uintptr_t)smem;
  unsigned voffC, voffW;
  { const int row = 8 * wid + (lane >> 3); voffC = (unsigned)(row * 1024 + (((lane & 7) ^ ((row >> 1) & 7)) << 4)); }
  { const int row = 8 * (wid >> 1) + ((lane >> 2) & 7); const int chq = 2 * (wid & 1) + (lane >> 5);
    const int ch = 4 * chq + ((lane & 3) ^ ((row >> 2) & 3)); voffW = (unsigned)(row * 8192 + ch * 16); }
  const char* cbase = (const char*)ckv + (size_t)(256 * j) * 1024;
  const char* wkb = (const char*)wuk + h * 256;
  const char* wvb = (const char*)wuv + h * 256;
  const unsigned dstw = lds0 + wid * 1024;
#define CBUF(b, a) (((b) * 2 + (a)) * 16384)
#define WBUF(b, a) (65536 + ((b) * 2 + (a)) * 16384)
#define STAGE_C(b, a, kt) do { const char* s_ = cbase + (a) * 131072 + (kt) * 128; glds16(voffC, s_, dstw + CBUF(b, a)); glds16(voffC, s_ + 65536, dstw + CBUF(b, a) + 8192); } while (0)
#define STAGE_W(b, a, kt) do { const char* s_ = ((a) ? wvb : wkb) + (size_t)(kt) * 64 * 8192; glds16(voffW, s_, dstw + WBUF(b, a)); glds16(voffW, s_ + 32 * 8192, dstw + WBUF(b, a) + 8192); } while (0)
  int aoff[4], boff[2];
#pragma unroll
  for (int s = 0; s < 4; ++s) aoff[s] = 128 * (wr * 64 + l31) + (((2 * s + hh) ^ ((l31 >> 1) & 7)) << 4);
  { const int blk = (lane >> 4) & 1, q = (lane & 15) >> 2, p = lane & 3;
#pragma unroll
    for (int t = 0; t < 2; ++t) boff[t] = 2048 * hh + 512 * wc + 64 * (4 * t + q) + (((2 * blk + (p >> 1)) ^ ((2 * hh + t) & 3)) << 4) + 8 * (p & 1); }
#define LDA(dst, b, a) do { _Pragma("unroll") for (int m = 0; m < 2; ++m) _Pragma("unroll") for (int s = 0; s < 4; ++s) dst[m][s] = ldsr128(lds + CBUF(b, a) + m * 4096 + aoff[s]); } while (0)
#define LDB(dst, b, a) do { _Pragma("unroll") for (int s = 0; s < 4; ++s) dst[s] = cat8(vtr(lds + WBUF(b, a) + s * 4096 + boff[0]), vtr(lds + WBUF(b, a) + s * 4096 + boff[1])); } while (0)
#define MMAK(ai, At, Bt) do { __builtin_amdgcn_s_setprio(1); _Pragma("unroll") for (int s = 0; s < 4; ++s) _Pragma("unroll") for (int m = 0; m < 2; ++m) \
    accK[ai][m] = MFMA32(Bt[s], At[m][s], accK[ai][m]); __builtin_amdgcn_s_setprio(0); } while (0)
#define MMAV(ai, At, Bt) do { __builtin_amdgcn_s_setprio(1); _Pragma("unroll") for (int s = 0; s < 4; ++s) _Pragma("unroll") for (int m = 0; m < 2; ++m) \
    accV[ai][m] = MFMA32(At[m][s], Bt[s], accV[ai][m]); __builtin_amdgcn_s_setprio(0); } while (0)
  f32x16 accK[2][2] = {}, accV[2][2] = {};
  bf16x8 At[2][4], B0[4], B1[4];
  constexpr int nt = DC / 64;
  STAGE_W(0, 0, 0); STAGE_C(0, 0, 0); STAGE_W(0, 1, 0); STAGE_C(0, 1, 0);
  if (wr == 1) BAR();
  WAIT_V(4); BAR();
  STAGE_W(1, 0, 1); STAGE_C(1, 0, 1); STAGE_W(1, 1, 1);
  WAIT_V(6); BAR();
  for (int t = 0; t < nt - 2; t += 2) {
    LDB(B0, 0, 0); SBAR(); LDA(At, 0, 0); STAGE_C(1, 1, t + 1);
    WAIT_L(8); BAR(); WAIT_L(0); MMAK(0, At, B0); BAR(); SBAR();
    LDB(B1, 0, 1); STAGE_W(0, 0, t + 2);
    BAR(); WAIT_L(0); MMAV(0, At, B1); BAR();
    LDA(At, 0, 1); STAGE_C(0, 0, t + 2);
    BAR(); WAIT_L(0); MMAK(1, At, B0); BAR(); SBAR();
    STAGE_W(0, 1, t + 2);
    WAIT_V(6); BAR(); MMAV(1, At, B1); BAR();
    LDB(B0, 1, 0); SBAR(); LDA(At, 1, 0); STAGE_C(0, 1, t + 2);
    WAIT_L(8); BAR(); WAIT_L(0); MMAK(0, At, B0); BAR(); SBAR();
    LDB(B1, 1, 1); STAGE_W(1, 0, t + 3);
    BAR(); WAIT_L(0); MMAV(0, At, B1); BAR();
    LDA(At, 1, 1); STAGE_C(1, 0, t + 3);
    BAR(); WAIT_L(0); MMAK(1, At, B0); BAR(); SBAR();
    STAGE_W(1, 1, t + 3);
    WAIT_V(6); BAR(); MMAV(1, At, B1); BAR();
  }
  { LDB(B0, 0, 0); WAIT_V(0); LDA(At, 0, 0); STAGE_C(1, 1, nt - 1);
    BAR(); WAIT_L(0); MMAK(0, At, B0); BAR();
    LDB(B1, 0, 1); BAR(); WAIT_L(0); MMAV(0, At, B1); BAR();
    LDA(At, 0, 1); WAIT_V(4); BAR(); WAIT_L(0); MMAK(1, At, B0); MMAV(1, At, B1); BAR(); }
  { LDB(B0, 1, 0); LDA(At, 1, 0); WAIT_V(2); BAR(); WAIT_L(0); MMAK(0, At, B0); BAR();
    LDB(B1, 1, 1); WAIT_V(0); BAR(); WAIT_L(0); MMAV(0, At, B1); BAR();
    LDA(At, 1, 1); BAR(); WAIT_L(0); MMAK(1, At, B0); MMAV(1, At, B1); BAR(); }
  if (wr == 0) BAR();
#pragma unroll
  for (int ai = 0; ai < 2; ++ai)
#pragma unroll
    for (int m = 0; m < 2; ++m) {
      const int kt = 8 * j + 4 * ai + 2 * wr + m;
#pragma unroll
      for (int sp = 0; sp < 2; ++sp) {
        *(bf16x8*)(Kf + ((size_t)((h * 64 + kt) * 8 + 2 * wc + sp) * 64 + lane) * 8) = pack8(accK[ai][m], sp);
        *(bf16x8*)(Vf + ((size_t)((h * 128 + 2 * kt + sp) * 4 + wc) * 64 + lane) * 8) = pack8(accV[ai][m], sp);
      }
    }
#undef CBUF
#undef WBUF
#undef STAGE_C
#undef STAGE_W
#undef LDA
#undef LDB
#undef MMAK
#undef MMAV
}

__global__ __launch_bounds__(512, 2) void kA_kv(const u16* __restrict__ ckv, const u16* __restrict__ wuk, const u16* __restrict__ wuv,
                                                u16* __restrict__ Kf, u16* __restrict__ Vf) {
  extern __shared__ __attribute__((aligned(16))) unsigned char smem[];
  const int b = blockIdx.x, xcd = b & 7, r = b >> 3;
  kv_phase(smem, ckv, wuk, wuv, Kf, Vf, xcd * 4 + (r >> 3), r & 7);
}


constexpr int L_KS = 0, L_VS = 65536, L_X = 131072, ATT_LDS = 163840;
constexpr float QSCALE = 0.08838834764831845f * 1.4426950408889634f;
constexpr float THR = 16.f;
#define PIN(x) asm volatile("" : "+v"(x))
#define EX2(v) __builtin_amdgcn_exp2f(v)
DI float max3f(float a, float b, float c) { return __builtin_fmaxf(__builtin_fmaxf(a, b), c); }
DI float xhalf_max(float m) { auto rr = __builtin_amdgcn_permlane32_swap(__float_as_uint(m), __float_as_uint(m), false, false); return __builtin_fmaxf(__uint_as_float(rr[0]), __uint_as_float(rr[1])); }
DI float xhalf_sum(float m) { auto rr = __builtin_amdgcn_permlane32_swap(__float_as_uint(m), __float_as_uint(m), false, false); return __uint_as_float(rr[0]) + __uint_as_float(rr[1]); }
DI float max16(const f32x16& p) {
  float a = max3f(p[0], p[1], p[2]), b = max3f(p[3], p[4], p[5]);
  a = max3f(a, p[6], p[7]); b = max3f(b, p[8], p[9]); a = max3f(a, p[10], p[11]); b = max3f(b, p[12], p[13]); a = max3f(a, p[14], p[15]);
  return __builtin_fmaxf(a, b);
}
DI void cmask(f32x16& p0, f32x16& p1, int dq) {
#pragma unroll
  for (int r = 0; r < 16; ++r) { const int c = (r & 3) + 8 * (r >> 2); if (c > dq) p0[r] = -__builtin_inff(); if (c + 32 > dq) p1[r] = -__builtin_inff(); }
}
DI void gload16(u32x4& dst, unsigned voff, const void* sbase, int) {}
#define GLOAD4(d, voff, sbase) asm volatile("global_load_dwordx4 %0, %4, %5\n\tglobal_load_dwordx4 %1, %4, %5 offset:1024\n\tglobal_load_dwordx4 %2, %4, %5 offset:2048\n\tglobal_load_dwordx4 %3, %4, %5 offset:3072" \
    : "=&v"(d[0]), "=&v"(d[1]), "=&v"(d[2]), "=&v"(d[3]) : "v"(voff), "s"(sbase) : "memory")
#define WAITV_TIE4(n, d) asm volatile("s_waitcnt vmcnt(" #n ")" : "+v"(d[0]), "+v"(d[1]), "+v"(d[2]), "+v"(d[3]) :: "memory")

DI void attn_phase(unsigned char* smem, const u16* __restrict__ xnf, const u16* __restrict__ wq, const u16* __restrict__ Kf, const u16* __restrict__ Vf,
                   u16* __restrict__ ctxf, int h, int j) {
  const int tid = threadIdx.x, lane = tid & 63;
  const int wid = rfl(tid >> 6), kh = wid >> 2, qsub = wid & 3;
  const int l31 = lane & 31, hh = lane >> 5;
  ldsp lds = (ldsp)smem;
  const unsigned lds0 = (unsigned)(uintptr_t)smem;
  const int nB = 16 - j, nA = j + 1;
  const char* Kh = (const char*)Kf + (size_t)h * 64 * 8192;
  const char* Vh = (const char*)Vf + (size_t)h * 128 * 4096;
  const unsigned vlin = (unsigned)(wid * 4096 + lane * 16);
  const unsigned dlin = lds0 + wid * 4096;
#define TI(g) ((g) < nB ? (g) : (g) - nB)
#define DMA_K(g) glds16x4(vlin, Kh + (size_t)TI(g) * 32768, dlin + L_KS + ((g) & 1) * 32768)
#define DMA_V(g) glds16x4(vlin, Vh + (size_t)TI(g) * 32768, dlin + L_VS + ((g) & 1) * 32768)

  bf16x8 qr[8];
  {
    unsigned voffW;
    { const int row = 8 * (wid >> 1) + ((lane >> 2) & 7); const int chq = 2 * (wid & 1) + (lane >> 5);
      const int ch = 4 * chq + ((lane & 3) ^ ((row >> 2) & 3)); voffW = (unsigned)(row * 8192 + ch * 16); }
    const char* wqb = (const char*)wq + h * 256;
    const unsigned dstw = lds0 + 98304 + wid * 1024;
#define STAGE_Q(c) do { const char* s_ = wqb + (size_t)(c) * 64 * 8192; glds16(voffW, s_, dstw + ((c) & 3) * 16384); glds16(voffW, s_ + 32 * 8192, dstw + ((c) & 3) * 16384 + 8192); } while (0)
    const int qb128 = kh == 0 ? 15 - j : j;
    const char* xb = (const char*)xnf + (size_t)(4 * qb128 + qsub) * 40 * 1024;
    const unsigned vl16 = lane * 16;
    int boff[2];
    { const int blk = (lane >> 4) & 1, q = (lane & 15) >> 2, p = lane & 3;
#pragma unroll
      for (int t = 0; t < 2; ++t) boff[t] = 98304 + 2048 * hh + 64 * (4 * t + q) + (((2 * blk + (p >> 1)) ^ ((2 * hh + t) & 3)) << 4) + 8 * (p & 1); }
    u32x4 xf[3][4];
    f32x16 acc[4] = {};
    GLOAD4(xf[0], vl16, xb); STAGE_Q(0);
    GLOAD4(xf[1], vl16, xb + 4096); STAGE_Q(1);
    STAGE_Q(2);
#pragma unroll
    for (int c = 0; c < 10; ++c) {
      if (c < 7) { WAITV_TIE4(8, xf[c % 3]); }
      else if (c == 7) { WAITV_TIE4(8, xf[c % 3]); }
      else if (c == 8) { WAITV_TIE4(16, xf[c % 3]); }
      else { WAITV_TIE4(12, xf[c % 3]); }
      WAIT_L(0); BAR();
      if (c + 2 < 10) GLOAD4(xf[(c + 2) % 3], vl16, xb + (c + 2) * 4096);
      if (c + 3 < 10) STAGE_Q(c + 3);
      if (c == 7) { DMA_K(0); DMA_V(0); DMA_K(1); }
#pragma unroll
      for (int ks = 0; ks < 4; ++ks) {
        const bf16x8 xb8 = __builtin_bit_cast(bf16x8, xf[c % 3][ks]);
#pragma unroll
        for (int dt = 0; dt < 4; ++dt) {
          const int o = (c & 3) * 16384 + ks * 4096 + dt * 512;
          const bf16x8 a = cat8(vtr(lds + o + boff[0]), vtr(lds + o + boff[1]));
          acc[dt] = MFMA32(a, xb8, acc[dt]);
        }
      }
    }
#pragma unroll
    for (int dt = 0; dt < 4; ++dt) { acc[dt] = acc[dt] * QSCALE; qr[2 * dt] = pack8(acc[dt], 0); qr[2 * dt + 1] = pack8(acc[dt], 1); }
    WAIT_L(0); BAR();
    { ldsp dst = lds + (kh == 0 ? 98304 : L_X) + qsub * 8192 + lane * 16;
#pragma unroll
      for (int s = 0; s < 8; ++s) *(LAS bf16x8*)(dst + s * 1024) = qr[s]; }
    WAIT_L(0); BAR();
    if (kh == 1) {
#pragma unroll
      for (int s = 0; s < 8; ++s) qr[s] = ldsr128(lds + 98304 + qsub * 8192 + lane * 16 + s * 1024);
    }
#undef STAGE_Q
  }
  WAIT_V(0); WAIT_L(0); BAR();
  const int rdoff = kh * 16384 + lane * 16;
  bf16x8 kq[4], vq[4], pw[4];
#pragma unroll
  for (int i = 0; i < 4; ++i) kq[i] = ldsr128(lds + L_KS + rdoff + i * 1024);
  f32x16 C0, C1, o[4], negm;
  float mhat = 0.f, lsum = 0.f;
  int g = 0;
  for (int blk = 0; blk < 2; ++blk) {
    const int n = blk == 0 ? nB : nA;
    const int qb128 = blk == 0 ? 15 - j : j;
    const int dq = 32 * qsub + l31 - 64 * kh - 4 * hh;
    {
      ldscp kb = lds + L_KS + (g & 1) * 32768 + rdoff;
      const f32x16 z = {};
#pragma unroll
      for (int u = 0; u < 16; ++u) {
        if (u < 8) C0 = MFMA32(kq[u & 3], qr[u & 7], u == 0 ? z : C0); else C1 = MFMA32(kq[u & 3], qr[u & 7], u == 8 ? z : C1);
        if (u + 4 < 16) kq[u & 3] = ldsr128(kb + (u + 4) * 1024);
      }
      if (n == 1) cmask(C0, C1, dq);
      float rm = xhalf_max(__builtin_fmaxf(max16(C0), max16(C1)));
      mhat = __builtin_fmaxf(rm, -1e30f);
#pragma unroll
      for (int r = 0; r < 16; ++r) { C0[r] -= mhat; C1[r] -= mhat; negm[r] = -mhat; }
      PIN(negm);
      WAIT_V(0); WAIT_L(0); BAR();
      if (n > 1) { if (g + 2 < 17) DMA_K(g + 2); }
      else if (false) {}
      if (g + 1 < 17) DMA_V(g + 1);
      { ldscp kn = lds + L_KS + ((g + 1) & 1) * 32768 + rdoff;
#pragma unroll
        for (int i = 0; i < 4; ++i) kq[i] = ldsr128(kn + i * 1024); }
      float s0 = 0.f;
#pragma unroll
      for (int r = 0; r < 16; ++r) { C0[r] = EX2(C0[r]); s0 += C0[r]; }
#pragma unroll
      for (int r = 0; r < 8; ++r) C1[r] = EX2(C1[r]);
      lsum = s0;
      pw[0] = pack8(C0, 0); pw[1] = pack8(C0, 1);
#pragma unroll
      for (int d = 0; d < 4; ++d) o[d] = z;
      ++g;
    }
    for (int t = 1; t < n; ++t, ++g) {
      ldscp kb = lds + L_KS + (g & 1) * 32768 + rdoff;
      ldscp vb = lds + L_VS + ((g - 1) & 1) * 32768 + rdoff;
      float sa = 0.f, sb = 0.f;
      SBAR();
#define KRD(u) kq[(u) & 3] = ldsr128(kb + ((u) + 4) * 1024)
#define VRD(v) vq[(v) & 3] = ldsr128(vb + (v) * 1024)
      f32x16 N0 = MFMA32(kq[0], qr[0], negm); KRD(0); C1[8] = EX2(C1[8]); C1[9] = EX2(C1[9]); sa += C1[0]; sb += C1[1]; sa += C1[2]; sb += C1[3]; PIN(C1); PIN(sa); PIN(sb); SBAR();
      N0 = MFMA32(kq[1], qr[1], N0); KRD(1); C1[10] = EX2(C1[10]); C1[11] = EX2(C1[11]); sa += C1[4]; sb += C1[5]; sa += C1[6]; sb += C1[7]; PIN(C1); PIN(sa); PIN(sb); SBAR();
      N0 = MFMA32(kq[2], qr[2], N0); KRD(2); C1[12] = EX2(C1[12]); C1[13] = EX2(C1[13]); pw[2] = pack8(C1, 0); PIN(C1); PIN(pw[2]); SBAR();
      N0 = MFMA32(kq[3], qr[3], N0); KRD(3); C1[14] = EX2(C1[14]); C1[15] = EX2(C1[15]); sa += C1[8]; sb += C1[9]; sa += C1[10]; sb += C1[11]; PIN(C1); PIN(sa); PIN(sb); SBAR();
      N0 = MFMA32(kq[0], qr[4], N0); KRD(4); sa += C1[12]; sb += C1[13]; sa += C1[14]; sb += C1[15]; PIN(sa); PIN(sb); SBAR();
      N0 = MFMA32(kq[1], qr[5], N0); KRD(5); pw[3] = pack8(C1, 1); PIN(pw[3]); lsum += sa + sb; PIN(lsum); SBAR();
      N0 = MFMA32(kq[2], qr[6], N0); KRD(6); SBAR();
      N0 = MFMA32(kq[3], qr[7], N0); KRD(7); SBAR();
      C0 = N0;
      C1 = MFMA32(kq[0], qr[0], negm); KRD(8); SBAR();
      C1 = MFMA32(kq[1], qr[1], C1); KRD(9); SBAR();
      C1 = MFMA32(kq[2], qr[2], C1); KRD(10); SBAR();
      float mxa, mxb;
      C1 = MFMA32(kq[3], qr[3], C1); KRD(11); mxa = max3f(C0[0], C0[1], C0[2]); mxb = max3f(C0[3], C0[4], C0[5]); PIN(mxa); PIN(mxb); SBAR();
      C1 = MFMA32(kq[0], qr[4], C1); VRD(0); mxa = max3f(mxa, C0[6], C0[7]); mxb = max3f(mxb, C0[8], C0[9]); PIN(mxa); PIN(mxb); SBAR();
      C1 = MFMA32(kq[1], qr[5], C1); VRD(1); mxa = max3f(mxa, C0[10], C0[11]); mxb = max3f(mxb, C0[12], C0[13]); PIN(mxa); PIN(mxb); SBAR();
      C1 = MFMA32(kq[2], qr[6], C1); VRD(2); mxa = max3f(mxa, C0[14], C0[15]); mxa = __builtin_fmaxf(mxa, mxb); PIN(mxa); SBAR();
      C1 = MFMA32(kq[3], qr[7], C1); VRD(3); SBAR();
      o[0] = MFMA32(vq[0], pw[0], o[0]); VRD(4); SBAR();
      o[1] = MFMA32(vq[1], pw[0], o[1]); VRD(5); SBAR();
      float rm;
      if (t == n - 1) { cmask(C0, C1, dq); rm = __builtin_fmaxf(max16(C0), max16(C1)); }
      else rm = __builtin_fmaxf(mxa, max16(C1));
      rm = xhalf_max(rm);
      float fsc = 1.f; bool resc = false;
      if (__builtin_expect(__any(rm > THR), 0)) {
        const float dl = __builtin_fmaxf(rm, 0.f);
        mhat += dl;
#pragma unroll
        for (int r = 0; r < 16; ++r) { C0[r] -= dl; C1[r] -= dl; negm[r] = -mhat; }
        PIN(negm);
        fsc = EX2(-dl); lsum *= fsc; resc = true;
      }
      SBAR();
      sa = 0.f; sb = 0.f;
      o[2] = MFMA32(vq[2], pw[0], o[2]); VRD(6); C0[0] = EX2(C0[0]); C0[1] = EX2(C0[1]); PIN(C0); SBAR();
      o[3] = MFMA32(vq[3], pw[0], o[3]); VRD(7); C0[2] = EX2(C0[2]); C0[3] = EX2(C0[3]); PIN(C0); SBAR();
      o[0] = MFMA32(vq[0], pw[1], o[0]); VRD(8); C0[4] = EX2(C0[4]); C0[5] = EX2(C0[5]); sa += C0[0]; sb += C0[1]; PIN(C0); PIN(sa); PIN(sb); SBAR();
      o[1] = MFMA32(vq[1], pw[1], o[1]); VRD(9); C0[6] = EX2(C0[6]); C0[7] = EX2(C0[7]); sa += C0[2]; sb += C0[3]; PIN(C0); PIN(sa); PIN(sb); SBAR();
      o[2] = MFMA32(vq[2], pw[1], o[2]); VRD(10); C0[8] = EX2(C0[8]); C0[9] = EX2(C0[9]); sa += C0[4]; sb += C0[5]; PIN(C0); PIN(sa); PIN(sb); SBAR();
      o[3] = MFMA32(vq[3], pw[1], o[3]); VRD(11); C0[10] = EX2(C0[10]); C0[11] = EX2(C0[11]); sa += C0[6]; sb += C0[7]; PIN(C0); PIN(sa); PIN(sb); SBAR();
      o[0] = MFMA32(vq[0], pw[2], o[0]); VRD(12); pw[0] = pack8(C0, 0); C0[12] = EX2(C0[12]); PIN(pw[0]); PIN(C0); SBAR();
      o[1] = MFMA32(vq[1], pw[2], o[1]); VRD(13); C0[13] = EX2(C0[13]); C0[14] = EX2(C0[14]); sa += C0[8]; sb += C0[9]; PIN(C0); PIN(sa); PIN(sb); SBAR();
      o[2] = MFMA32(vq[2], pw[2], o[2]); VRD(14); C0[15] = EX2(C0[15]); C1[0] = EX2(C1[0]); sa += C0[10]; sb += C0[11]; PIN(C0); PIN(C1); PIN(sa); PIN(sb); SBAR();
      o[3] = MFMA32(vq[3], pw[2], o[3]); VRD(15); C1[1] = EX2(C1[1]); C1[2] = EX2(C1[2]); sa += C0[12]; sb += C0[13]; PIN(C1); PIN(sa); PIN(sb); SBAR();
      WAIT_V(0); WAIT_L(0); BAR();
      if (t != n - 1 && g + 2 < 17) DMA_K(g + 2);
      if (g + 1 < 17) DMA_V(g + 1);
      { ldscp kn = lds + L_KS + ((g + 1) & 1) * 32768 + rdoff;
#pragma unroll
        for (int i = 0; i < 4; ++i) kq[i] = ldsr128(kn + i * 1024); }
      SBAR();
      o[0] = MFMA32(vq[0], pw[3], o[0]); pw[1] = pack8(C0, 1); C1[3] = EX2(C1[3]); PIN(pw[1]); PIN(C1); SBAR();
      o[1] = MFMA32(vq[1], pw[3], o[1]); C1[4] = EX2(C1[4]); C1[5] = EX2(C1[5]); sa += C0[14]; sb += C0[15]; PIN(C1); PIN(sa); PIN(sb); SBAR();
      o[2] = MFMA32(vq[2], pw[3], o[2]); C1[6] = EX2(C1[6]); C1[7] = EX2(C1[7]); PIN(C1); SBAR();
      o[3] = MFMA32(vq[3], pw[3], o[3]); lsum += sa + sb; PIN(lsum); SBAR();
      if (resc) {
#pragma unroll
        for (int d = 0; d < 4; ++d) o[d] = o[d] * fsc;
      }
    }
    {
      const int gl = g - 1;
      ldscp vb = lds + L_VS + (gl & 1) * 32768 + rdoff;
#pragma unroll
      for (int i = 0; i < 4; ++i) vq[i] = ldsr128(vb + i * 1024);
      if (blk == 0) {
#pragma unroll
        for (int s = 0; s < 8; ++s) qr[s] = ldsr128(lds + L_X + qsub * 8192 + lane * 16 + s * 1024);
      }
      float sa = 0.f;
#pragma unroll
      for (int r = 8; r < 16; ++r) C1[r] = EX2(C1[r]);
#pragma unroll
      for (int r = 0; r < 16; ++r) sa += C1[r];
      lsum += sa;
      pw[2] = pack8(C1, 0); pw[3] = pack8(C1, 1);
#pragma unroll
      for (int v = 0; v < 16; ++v) {
        o[v & 3] = MFMA32(vq[v & 3], pw[v >> 2], o[v & 3]);
        if (v + 4 < 16) vq[v & 3] = ldsr128(vb + (v + 4) * 1024);
      }
      lsum = xhalf_sum(lsum);
      WAIT_L(0); BAR();
      ldsp xw = lds + (kh == 0 ? L_VS + (gl & 1) * 32768 : L_X) + qsub * 8192 + lane * 16;
      ldscp xr = lds + (kh == 1 ? L_VS + (gl & 1) * 32768 : L_X) + qsub * 8192 + lane * 16;
      ldsp st = lds + L_KS + (gl & 1) * 32768;
#pragma unroll
      for (int d = 0; d < 2; ++d)
#pragma unroll
        for (int q4 = 0; q4 < 4; ++q4) {
          const f32x16& src = kh == 0 ? o[2 + d] : o[d];
          f32x4 v4 = {src[4 * q4], src[4 * q4 + 1], src[4 * q4 + 2], src[4 * q4 + 3]};
          *(LAS f32x4*)(xw + (d * 4 + q4) * 1024) = v4;
        }
      *(LAS f32x2*)(st + (wid * 64 + lane) * 8) = (f32x2){mhat, lsum};
      WAIT_L(0); BAR();
      const f32x2 ot = *(const LAS f32x2*)(st + ((wid ^ 4) * 64 + lane) * 8);
      const float m = __builtin_fmaxf(mhat, ot[0]);
      const float f0 = EX2(mhat - m), f1 = EX2(ot[0] - m);
      const float inv = __builtin_amdgcn_rcpf(lsum * f0 + ot[1] * f1);
      const float g0 = f0 * inv, g1 = f1 * inv;
      u16* cdst = ctxf + ((size_t)((4 * qb128 + qsub) * 32 + h) * 8 * 64 + lane) * 8;
#pragma unroll
      for (int d = 0; d < 2; ++d) {
        f32x16 r;
        const f32x16& own = kh == 0 ? o[d] : o[2 + d];
#pragma unroll
        for (int q4 = 0; q4 < 4; ++q4) {
          const f32x4 v4 = *(const LAS f32x4*)(xr + (d * 4 + q4) * 1024);
#pragma unroll
          for (int e = 0; e < 4; ++e) r[4 * q4 + e] = own[4 * q4 + e] * g0 + v4[e] * g1;
        }
        const int dt = 2 * kh + d;
        *(bf16x8*)(cdst + (size_t)(2 * dt) * 512) = pack8(r, 0);
        *(bf16x8*)(cdst + (size_t)(2 * dt + 1) * 512) = pack8(r, 1);
      }
      WAIT_L(0); BAR();
      if (blk == 0 && g + 1 < 17) DMA_K(g + 1);
    }
  }
#undef TI
#undef DMA_K
#undef DMA_V
#undef KRD
#undef VRD
}

__global__ __launch_bounds__(512, 2) void kB_attn(const u16* __restrict__ xnf, const u16* __restrict__ wq, const u16* __restrict__ Kf, const u16* __restrict__ Vf,
                                                  u16* __restrict__ ctxf) {
  extern __shared__ __attribute__((aligned(16))) unsigned char smem[];
  const int b = blockIdx.x, xcd = b & 7, r = b >> 3;
  attn_phase(smem, xnf, wq, Kf, Vf, ctxf, xcd * 4 + (r >> 3), r & 7);
}


constexpr int OP_LDS = 3 * 40960;
__global__ __launch_bounds__(512, 2) void kC_oproj(const u16* __restrict__ ctxf, const u16* __restrict__ wo, float* __restrict__ slab) {
  extern __shared__ __attribute__((aligned(16))) unsigned char smem[];
  const int b = blockIdx.x, xcd = b & 7, r = b >> 3;
  const int ksp = xcd >> 1, panel = (xcd & 1) * 8 + (r >> 2), ct = r & 3;
  const int tid = threadIdx.x, lane = tid & 63;
  const int wid = rfl(tid >> 6), kh = wid >> 2, st = wid & 3;
  const int hh = lane >> 5;
  ldsp lds = (ldsp)smem;
  const unsigned lds0 = (unsigned)(uintptr_t)smem;
  unsigned voff[5];
#pragma unroll
  for (int i = 0; i < 5; ++i) {
    const int id = 2 * (5 * wid + i) + (lane >> 5), half = id / 40, rem = id % 40, rg = rem / 5, c = rem % 5;
    const int row = 64 * half + 8 * rg + ((lane >> 2) & 7);
    voff[i] = (unsigned)(row * 1280 + (32 * c + 8 * ((lane & 3) ^ ((row >> 2) & 3))) * 2);
  }
  const char* wb = (const char*)wo + (size_t)(ksp * 1024) * 1280 + ct * 320;
  const unsigned dstw = lds0 + wid * 5120;
#define STAGE_O(hd) do { const char* s_ = wb + (size_t)(hd) * 128 * 1280; const unsigned d_ = dstw + ((hd) % 3) * 40960; \
    glds16(voff[0], s_, d_); glds16(voff[1], s_, d_ + 1024); glds16(voff[2], s_, d_ + 2048); glds16(voff[3], s_, d_ + 3072); glds16(voff[4], s_, d_ + 4096); } while (0)
  const char* cb = (const char*)ctxf + ((size_t)((4 * panel + st) * 32 + 8 * ksp) * 8 + 4 * kh) * 1024;
  const unsigned vl16 = lane * 16;
  int boff[2];
  { const int blk = (lane >> 4) & 1, q = (lane & 15) >> 2, p = lane & 3;
#pragma unroll
    for (int t = 0; t < 2; ++t) boff[t] = kh * 20480 + 2560 * t + 64 * (4 * hh + q) + (((2 * blk + (p >> 1)) ^ ((2 * t + hh) & 3)) << 4) + 8 * (p & 1); }
  u32x4 xf[3][4];
  f32x16 acc[5] = {};
  GLOAD4(xf[0], vl16, cb); STAGE_O(0);
  GLOAD4(xf[1], vl16, cb + 8192); STAGE_O(1);
#pragma unroll
  for (int hd = 0; hd < 8; ++hd) {
    if (hd < 7) { WAITV_TIE4(9, xf[hd % 3]); } else { WAITV_TIE4(0, xf[hd % 3]); }
    SBAR(); BAR();
    if (hd + 2 < 8) { GLOAD4(xf[(hd + 2) % 3], vl16, cb + (size_t)(hd + 2) * 8192); STAGE_O(hd + 2); }
    bf16x8 aq[5];
#define ARD(u) aq[(u) % 5] = cat8(vtr(lds + ((hd) % 3) * 40960 + ((u) / 5) * 5120 + ((u) % 5) * 512 + boff[0]), vtr(lds + ((hd) % 3) * 40960 + ((u) / 5) * 5120 + ((u) % 5) * 512 + boff[1]))
    ARD(0); ARD(1); ARD(2); ARD(3); ARD(4);
    SBAR();
#pragma unroll
    for (int u = 0; u < 20; ++u) {
      acc[u % 5] = MFMA32(aq[u % 5], __builtin_bit_cast(bf16x8, xf[hd % 3][u / 5]), acc[u % 5]);
      if (u + 5 < 20) ARD(u + 5);
      SBAR();
    }
#undef ARD
  }
#undef STAGE_O
  BAR();
  ldsp xw = lds + (kh == 0 ? 49152 : 0) + st * 12288 + lane * 16;
  ldscp xr = lds + (kh == 0 ? 0 : 49152) + st * 12288 + lane * 16;
  if (kh == 0) {
#pragma unroll
    for (int i = 0; i < 2; ++i)
#pragma unroll
      for (int g = 0; g < 4; ++g) *(LAS f32x4*)(xw + (i * 4 + g) * 1024) = (f32x4){acc[3 + i][4 * g], acc[3 + i][4 * g + 1], acc[3 + i][4 * g + 2], acc[3 + i][4 * g + 3]};
  } else {
#pragma unroll
    for (int i = 0; i < 3; ++i)
#pragma unroll
      for (int g = 0; g < 4; ++g) *(LAS f32x4*)(xw + (i * 4 + g) * 1024) = (f32x4){acc[i][4 * g], acc[i][4 * g + 1], acc[i][4 * g + 2], acc[i][4 * g + 3]};
  }
  WAIT_L(0); BAR();
  float* sb = slab + ((size_t)((ksp * 64 + 4 * panel + st) * 20 + 5 * ct) * 4) * 256 + lane * 4;
  if (kh == 0) {
#pragma unroll
    for (int i = 0; i < 3; ++i)
#pragma unroll
      for (int g = 0; g < 4; ++g) { const f32x4 o = *(const LAS f32x4*)(xr + (i * 4 + g) * 1024);
        *(f32x4*)(sb + (i * 4 + g) * 256) = (f32x4){acc[i][4 * g] + o[0], acc[i][4 * g + 1] + o[1], acc[i][4 * g + 2] + o[2], acc[i][4 * g + 3] + o[3]}; }
  } else {
#pragma unroll
    for (int i = 0; i < 2; ++i)
#pragma unroll
      for (int g = 0; g < 4; ++g) { const f32x4 o = *(const LAS f32x4*)(xr + (i * 4 + g) * 1024);
        *(f32x4*)(sb + ((3 + i) * 4 + g) * 256) = (f32x4){acc[3 + i][4 * g] + o[0], acc[3 + i][4 * g + 1] + o[1], acc[3 + i][4 * g + 2] + o[2], acc[3 + i][4 * g + 3] + o[3]}; }
  }
}

__global__ __launch_bounds__(512) void kD_red(const float* __restrict__ slab, const u16* __restrict__ x, u16* __restrict__ hbuf, u16* __restrict__ hnf) {
  __shared__ __attribute__((aligned(16))) u16 tr[640 * 8];
  __shared__ float red[2][8][8];
  const int b = blockIdx.x, t = threadIdx.x, r = t & 7, seg = t >> 3, wid = t >> 6, lane = t & 63;
  const int s = 8 * b + r, st = b >> 2;
  float hv[3][4];
  int n0[3];
#pragma unroll
  for (int i = 0; i < 3; ++i) {
    const int sg = seg + 64 * i;
    const bool ok = sg < 160;
    const int sgc = ok ? sg : 0;
    const int nt = sgc >> 3, g = (sgc >> 1) & 3, hh = sgc & 1;
    n0[i] = 32 * nt + 8 * g + 4 * hh;
    const float* p = slab + ((size_t)(st * 20 + nt) * 4 + g) * 256 + ((8 * (b & 3) + r) + 32 * hh) * 4;
    f32x4 a = *(const f32x4*)p;
#pragma unroll
    for (int k = 1; k < 4; ++k) a += *(const f32x4*)(p + (size_t)k * 64 * 20 * 4 * 256);
    const u32x2 xv = *(const u32x2*)(x + (size_t)s * DM + n0[i]);
    const unsigned p0 = cvtpk(a[0] + bflo(xv[0]), a[1] + bfhi(xv[0])), p1 = cvtpk(a[2] + bflo(xv[1]), a[3] + bfhi(xv[1]));
    if (ok) *(u32x2*)(hbuf + (size_t)s * DM + n0[i]) = (u32x2){p0, p1};
    hv[i][0] = ok ? bflo(p0) : 0.f; hv[i][1] = ok ? bfhi(p0) : 0.f; hv[i][2] = ok ? bflo(p1) : 0.f; hv[i][3] = ok ? bfhi(p1) : 0.f;
  }
  float sum = 0.f;
#pragma unroll
  for (int i = 0; i < 3; ++i) sum += (hv[i][0] + hv[i][1]) + (hv[i][2] + hv[i][3]);
  sum += __shfl_xor(sum, 8); sum += __shfl_xor(sum, 16); sum += __shfl_xor(sum, 32);
  if (lane < 8) red[0][wid][lane] = sum;
  __syncthreads();
  float tot = 0.f;
#pragma unroll
  for (int w = 0; w < 8; ++w) tot += red[0][w][r];
  const float mean = tot * (1.0f / DM);
  float ss = 0.f;
#pragma unroll
  for (int i = 0; i < 3; ++i) {
    if (seg + 64 * i < 160) {
#pragma unroll
      for (int e = 0; e < 4; ++e) { const float d = hv[i][e] - mean; ss += d * d; }
    }
  }
  ss += __shfl_xor(ss, 8); ss += __shfl_xor(ss, 16); ss += __shfl_xor(ss, 32);
  if (lane < 8) red[1][wid][lane] = ss;
  __syncthreads();
  float tv = 0.f;
#pragma unroll
  for (int w = 0; w < 8; ++w) tv += red[1][w][r];
  const float rstd = 1.0f / sqrtf(tv * (1.0f / DM) + LN_EPS);
#pragma unroll
  for (int i = 0; i < 3; ++i) {
    if (seg + 64 * i < 160) {
      const unsigned p0 = cvtpk((hv[i][0] - mean) * rstd, (hv[i][1] - mean) * rstd), p1 = cvtpk((hv[i][2] - mean) * rstd, (hv[i][3] - mean) * rstd);
      tr[(n0[i] + 0) * 8 + r] = (u16)(p0 & 0xffff); tr[(n0[i] + 1) * 8 + r] = (u16)(p0 >> 16);
      tr[(n0[i] + 2) * 8 + r] = (u16)(p1 & 0xffff); tr[(n0[i] + 3) * 8 + r] = (u16)(p1 >> 16);
    }
  }
  __syncthreads();
  u32x4* dst = (u32x4*)hnf + (size_t)st * 40 * 64 + (b & 3) * 16;
  for (int d = t; d < DM; d += 512) dst[(d >> 4) * 64 + (d & 15)] = *(const u32x4*)(tr + d * 8);
}

constexpr int ML_STG = 36864, ML_LDS = 4 * ML_STG;
template <bool FIRST>
DI void mlp_gemm(unsigned char* smem, const u16* __restrict__ W, const u16* __restrict__ act, const u16* __restrict__ bias,
                 const u16* __restrict__ hbuf, u16* __restrict__ outp, int wt, int dg) {
  constexpr int PITCH = FIRST ? FF * 2 : SEQ * 2;
  constexpr int NKT = FIRST ? SEQ / 64 : FF / 64;
  constexpr int NST = (NKT + 1) / 2;
  const int tid = threadIdx.x, lane = tid & 63;
  const int wid = rfl(tid >> 6), kh = wid >> 2, ct = wid & 3;
  ldsp lds = (ldsp)smem;
  const unsigned lds0 = (unsigned)(uintptr_t)smem;
  unsigned voffW;
  { const int row = 8 * wid + (lane >> 3); const int f = ((row >> 1) & 1) | (((row >> 3) & 1) << 1); voffW = (unsigned)(row * PITCH + (((lane & 7) ^ (f << 1)) << 4)); }
  const unsigned vl16 = lane * 16;
  const char* wb = (const char*)W + wt * 128;
  const char* ab = (const char*)act + (size_t)(5 * dg) * 1024;
#define APIECE(stg, i) do { const int T_ = 4 * (stg) + (i) / 5; const int Tc_ = T_ < 2 * NKT ? T_ : 2 * NKT - 1; \
    glds16(vl16, ab + ((size_t)Tc_ * 40 + (i) % 5) * 1024, lds0 + ((stg) & 3) * ML_STG + 16384 + (i) * 1024); } while (0)
#define STAGE_M(stg) do { const int k0_ = 2 * (stg), k1_ = (2 * (stg) + 1 < NKT) ? 2 * (stg) + 1 : NKT - 1; \
    glds16(voffW, wb + (size_t)k0_ * 64 * PITCH, lds0 + ((stg) & 3) * ML_STG + wid * 1024); \
    glds16(voffW, wb + (size_t)k1_ * 64 * PITCH, lds0 + ((stg) & 3) * ML_STG + 8192 + wid * 1024); \
    APIECE(stg, wid); APIECE(stg, wid + 8); if (kh == 0) APIECE(stg, wid + 16); } while (0)
  int woff;
  { const int g = lane >> 4, q = (lane & 15) >> 2, p = lane & 3; const int f = (q >> 1) | ((g & 1) << 1);
    woff = 128 * (32 * kh + 8 * g + q) + (((2 * ct + (p >> 1)) ^ (f << 1)) << 4) + 8 * (p & 1); }
  const int aoff = 16384 + kh * 5120 + lane * 16;
  f32x4 acc[5] = {};
  STAGE_M(0); STAGE_M(1); STAGE_M(2);
  for (int s = 0; s < NST; ++s) {
    if (s + 2 < NST) { if (kh == 0) WAIT_V(10); else WAIT_V(8); }
    else if (s + 1 < NST) { if (kh == 0) WAIT_V(5); else WAIT_V(4); }
    else WAIT_V(0);
    SBAR(); BAR();
    if (s + 3 < NST) STAGE_M(s + 3);
    ldscp sb = lds + (s & 3) * ML_STG;
    bf16x8 wf[2], af[2][5];
#pragma unroll
    for (int k = 0; k < 2; ++k) {
      wf[k] = cat8(vtr(sb + k * 8192 + woff), vtr(sb + k * 8192 + woff + 512));
#pragma unroll
      for (int d = 0; d < 5; ++d) af[k][d] = ldsr128(sb + aoff + k * 10240 + d * 1024);
    }
#pragma unroll
    for (int k = 0; k < 2; ++k) {
      if (k == 0 || 2 * s + 1 < NKT) {
#pragma unroll
        for (int d = 0; d < 5; ++d) acc[d] = FIRST ? MFMA16(wf[k], af[k][d], acc[d]) : MFMA16(af[k][d], wf[k], acc[d]);
      }
    }
    SBAR();
  }
  BAR();
#pragma unroll
  for (int d = 0; d < 5; ++d) *(LAS f32x4*)(lds + ((kh * 4 + ct) * 5 + d) * 1024 + lane * 16) = acc[d];
  WAIT_L(0); BAR();
  for (int it = tid; it < 640; it += 512) {
    float v[8];
    if (FIRST) {
      const int d15 = it & 15, gp = (it >> 4) & 3, fr = it >> 6, dt = fr % 5, Tp = fr / 5;
      const int c2 = 2 * Tp + (gp >> 1), gD = 2 * (gp & 1);
#pragma unroll
      for (int hf = 0; hf < 2; ++hf) {
        const f32x4 a = *(const LAS f32x4*)(lds + ((0 + c2) * 5 + dt) * 1024 + ((gD + hf) * 16 + d15) * 16);
        const f32x4 c = *(const LAS f32x4*)(lds + ((4 + c2) * 5 + dt) * 1024 + ((gD + hf) * 16 + d15) * 16);
#pragma unroll
        for (int e = 0; e < 4; ++e) v[4 * hf + e] = a[e] + c[e];
      }
      const int f0 = 64 * wt + 32 * Tp + 8 * gp;
      const u32x4 bv = *(const u32x4*)(bias + f0);
      u32x4 o;
#pragma unroll
      for (int e = 0; e < 4; ++e) {
        float z[2] = {v[2 * e] + bflo(bv[e]), v[2 * e + 1] + bfhi(bv[e])};
#pragma unroll
        for (int k = 0; k < 2; ++k) {
          const float xx = z[k];
          const float a = xx * (1.0f + 0.044715f * xx * xx) * (-2.0f * 0.7978845608028654f * 1.4426950408889634f);
          z[k] = xx * __builtin_amdgcn_rcpf(1.0f + __builtin_amdgcn_exp2f(a));
        }
        o[e] = cvtpk(z[0], z[1]);
      }
      *(u32x4*)(outp + ((size_t)((2 * wt + Tp) * 40 + 5 * dg + dt) * 64 + gp * 16 + d15) * 8) = o;
    } else {
      const int sl = it / 10, c = it % 10, dt = c >> 1, gD = 2 * (c & 1), c2 = sl >> 4, s15 = sl & 15;
#pragma unroll
      for (int hf = 0; hf < 2; ++hf) {
        const f32x4 a = *(const LAS f32x4*)(lds + ((0 + c2) * 5 + dt) * 1024 + ((gD + hf) * 16 + s15) * 16);
        const f32x4 cc = *(const LAS f32x4*)(lds + ((4 + c2) * 5 + dt) * 1024 + ((gD + hf) * 16 + s15) * 16);
#pragma unroll
        for (int e = 0; e < 4; ++e) v[4 * hf + e] = a[e] + cc[e];
      }
      const int srow = 64 * wt + sl;
      const float bb = bflo((unsigned)bias[srow]);
      const size_t o = (size_t)srow * DM + 80 * dg + 8 * c;
      const u32x4 hv = *(const u32x4*)(hbuf + o);
      u32x4 ov;
#pragma unroll
      for (int e = 0; e < 4; ++e) ov[e] = cvtpk(v[2 * e] + bb + bflo(hv[e]), v[2 * e + 1] + bb + bfhi(hv[e]));
      *(u32x4*)(outp + o) = ov;
    }
  }
#undef APIECE
#undef STAGE_M
}
__global__ __launch_bounds__(512, 2) void kE_mlp1(const u16* __restrict__ w1, const u16* __restrict__ hnf, const u16* __restrict__ b1, u16* __restrict__ uf) {
  extern __shared__ __attribute__((aligned(16))) unsigned char smem[];
  const int b = blockIdx.x, xcd = b & 7, r = b >> 3;
  const int idx = xcd * 27 + r;
  mlp_gemm<true>(smem, w1, hnf, b1, nullptr, uf, idx >> 3, idx & 7);
}
__global__ __launch_bounds__(512, 2) void kF_mlp2(const u16* __restrict__ w2, const u16* __restrict__ uf, const u16* __restrict__ b2, const u16* __restrict__ hbuf, u16* __restrict__ out) {
  extern __shared__ __attribute__((aligned(16))) unsigned char smem[];
  const int b = blockIdx.x, xcd = b & 7, r = b >> 3;
  mlp_gemm<false>(smem, w2, uf, b2, hbuf, out, xcd * 4 + (r >> 3), r & 7);
}

extern "C" void kernel_launch(void* const* d_in, const int* in_sizes, int n_in, void* d_out, int out_size, void* d_ws, size_t ws_size,
                              hipStream_t stream) {
  const u16* x = (const u16*)d_in[0];
  const u16* ckv = (const u16*)d_in[1];
  const u16* wuk = (const u16*)d_in[3];
  const u16* wuv = (const u16*)d_in[4];
  char* ws = (char*)d_ws;
  const u16* wq = (const u16*)d_in[2];
  const u16* wo = (const u16*)d_in[5];
  const u16* w1 = (const u16*)d_in[7];
  const u16* b1 = (const u16*)d_in[8];
  const u16* w2 = (const u16*)d_in[9];
  const u16* b2 = (const u16*)d_in[10];
  (void)hipFuncSetAttribute((const void*)kC_oproj, hipFuncAttributeMaxDynamicSharedMemorySize, OP_LDS);
  (void)hipFuncSetAttribute((const void*)kE_mlp1, hipFuncAttributeMaxDynamicSharedMemorySize, ML_LDS);
  (void)hipFuncSetAttribute((const void*)kF_mlp2, hipFuncAttributeMaxDynamicSharedMemorySize, ML_LDS);
  (void)hipFuncSetAttribute((const void*)kA_kv, hipFuncAttributeMaxDynamicSharedMemorySize, 131072);
  (void)hipFuncSetAttribute((const void*)kB_attn, hipFuncAttributeMaxDynamicSharedMemorySize, ATT_LDS);
  hipLaunchKernelGGL(k0_ln, dim3(256), dim3(512), 0, stream, x, (u16*)(ws + WS_XNF), (unsigned*)(ws + WS_FLAGS));
  hipLaunchKernelGGL(kA_kv, dim3(256), dim3(512), 131072, stream, ckv, wuk, wuv, (u16*)(ws + WS_KF), (u16*)(ws + WS_VF));
  hipLaunchKernelGGL(kB_attn, dim3(256), dim3(512), ATT_LDS, stream, (const u16*)(ws + WS_XNF), wq, (const u16*)(ws + WS_KF), (const u16*)(ws + WS_VF), (u16*)(ws + WS_CTX));
  hipLaunchKernelGGL(kC_oproj, dim3(256), dim3(512), OP_LDS, stream, (const u16*)(ws + WS_CTX), wo, (float*)(ws + WS_SLAB));
  hipLaunchKernelGGL(kD_red, dim3(256), dim3(512), 0, stream, (const float*)(ws + WS_SLAB), x, (u16*)(ws + WS_H), (u16*)(ws + WS_HNF));
  hipLaunchKernelGGL(kE_mlp1, dim3(216), dim3(512), ML_LDS, stream, w1, (const u16*)(ws + WS_HNF), b1, (u16*)(ws + WS_UF));
  hipLaunchKernelGGL(kF_mlp2, dim3(256), dim3(512), ML_LDS, stream, w2, (const u16*)(ws + WS_UF), b2, (const u16*)(ws + WS_H), (u16*)d_out);
}
```

```cpp
#include <hip/hip_runtime.h>
#include <stdint.h>

#define LAS __attribute__((address_space(3)))
#define DI __device__ __forceinline__
typedef unsigned short u16;
typedef short bf16x8 __attribute__((ext_vector_type(8)));
typedef short s16x4 __attribute__((ext_vector_type(4)));
typedef float f32x2 __attribute__((ext_vector_type(2)));
typedef float f32x4 __attribute__((ext_vector_type(4)));
typedef float f32x16 __attribute__((ext_vector_type(16)));
typedef unsigned u32x2 __attribute__((ext_vector_type(2)));
typedef unsigned u32x4 __attribute__((ext_vector_type(4)));
typedef __bf16 bf16x2_t __attribute__((ext_vector_type(2)));
typedef LAS unsigned char* ldsp;
typedef LAS const unsigned char* ldscp;

constexpr int SEQ = 2048, DM = 640, DC = 512, NH = 32, HD = 128, FF = 1728;
constexpr float LN_EPS = 1e-5f;

constexpr size_t WS_FLAGS = 0;
constexpr size_t WS_XNF = 131072;
constexpr size_t WS_KF = WS_XNF + (size_t)SEQ * DM * 2;
constexpr size_t WS_VF = WS_KF + (size_t)NH * SEQ * HD * 2;
constexpr size_t WS_CTX = WS_VF + (size_t)NH * SEQ * HD * 2;
constexpr size_t WS_SLAB = WS_CTX + (size_t)NH * SEQ * HD * 2;
constexpr size_t WS_H = WS_SLAB + (size_t)4 * SEQ * DM * 4;
constexpr size_t WS_HNF = WS_H + (size_t)SEQ * DM * 2;
constexpr size_t WS_UF = WS_HNF + (size_t)SEQ * DM * 2;
constexpr size_t WS_QF = WS_UF + (size_t)DM * FF * 2;
constexpr size_t WS_PF = WS_QF + (size_t)NH * SEQ * HD * 2;
constexpr size_t WS_PS = WS_PF + (size_t)NH * SEQ * HD * 2;
constexpr size_t WS_END = WS_PS + (size_t)NH * 8 * 8 * 64 * 8;
constexpr int FL_TMO = 0;
constexpr int FL_KV = 64;
constexpr int FL_Q = 4096;
constexpr int FL_P = 4096 + 8192;
constexpr int FL_WORDS = 32768;

#define SBAR() __builtin_amdgcn_sched_barrier(0)
#define BAR() asm volatile("s_barrier" ::: "memory")
#define WAIT_V(n) asm volatile("s_waitcnt vmcnt(" #n ")" ::: "memory")
#define WAIT_L(n) asm volatile("s_waitcnt lgkmcnt(" #n ")" ::: "memory")
#define MFMA32(a, b, c) __builtin_amdgcn_mfma_f32_32x32x16_bf16((a), (b), (c), 0, 0, 0)
#define MFMA16(a, b, c) __builtin_amdgcn_mfma_f32_16x16x32_bf16((a), (b), (c), 0, 0, 0)

DI unsigned cvtpk(float lo, float hi) { f32x2 v = {lo, hi}; bf16x2_t b = __builtin_convertvector(v, bf16x2_t); return __builtin_bit_cast(unsigned, b); }
DI float bflo(unsigned u) { return __uint_as_float(u << 16); }
DI float bfhi(unsigned u) { return __uint_as_float(u & 0xffff0000u); }
DI unsigned rfl(unsigned x) { return __builtin_amdgcn_readfirstlane(x); }
DI void glds16(unsigned voff, const void* sbase, unsigned lds_dst) {
  unsigned keep;
  asm volatile("s_mov_b32 %0, m0\n\ts_mov_b32 m0, %3\n\ts_nop 2\n\tglobal_load_lds_dwordx4 %1, %2\n\ts_mov_b32 m0, %0"
               : "=&s"(keep) : "v"(voff), "s"(sbase), "s"(lds_dst) : "memory");
}
DI void glds16x4(unsigned voff, const void* sbase, unsigned lds_dst) {
  unsigned keep;
  asm volatile("s_mov_b32 %0, m0\n\ts_mov_b32 m0, %3\n\ts_nop 2\n\tglobal_load_lds_dwordx4 %1, %2\n\tglobal_load_lds_dwordx4 %1, %2 offset:1024\n\t"
               "global_load_lds_dwordx4 %1, %2 offset:2048\n\tglobal_load_lds_dwordx4 %1, %2 offset:3072\n\ts_mov_b32 m0, %0"
               : "=&s"(keep) : "v"(voff), "s"(sbase), "s"(lds_dst) : "memory");
}
typedef short v4i16_t __attribute__((ext_vector_type(4)));
DI s16x4 vtr(ldscp p) { return __builtin_bit_cast(s16x4, __builtin_amdgcn_ds_read_tr16_b64_v4i16((LAS v4i16_t*)p)); }
DI bf16x8 cat8(s16x4 lo, s16x4 hi) { return __builtin_shufflevector(lo, hi, 0, 1, 2, 3, 4, 5, 6, 7); }
DI bf16x8 ldsr128(ldscp p) { return *(const LAS bf16x8*)p; }
DI bf16x8 pack8(const f32x16& x, int s) {
  u32x4 w = {cvtpk(x[8 * s], x[8 * s + 1]), cvtpk(x[8 * s + 2], x[8 * s + 3]), cvtpk(x[8 * s + 4], x[8 * s + 5]), cvtpk(x[8 * s + 6], x[8 * s + 7])};
  return __builtin_bit_cast(bf16x8, w);
}
DI float wave_sum(float v) {
#pragma unroll
  for (int o = 32; o >= 1; o >>= 1) v += __shfl_xor(v, o);
  return v;
}

__global__ __launch_bounds__(512) void k0_ln(const u16* __restrict__ x, u16* __restrict__ xnf, unsigned* __restrict__ flags) {
  const int w = threadIdx.x >> 6, lane = threadIdx.x & 63;
  const int q = blockIdx.x * 8 + w;
  if (threadIdx.x < 128) flags[blockIdx.x * 128 + threadIdx.x] = 0u;
  const u32x4* row = (const u32x4*)(x + (size_t)q * DM);
  u32x4 c0 = row[lane];
  u32x4 c1 = {0u, 0u, 0u, 0u};
  if (lane < 16) c1 = row[64 + lane];
  float v[16];
#pragma unroll
  for (int i = 0; i < 4; ++i) { v[2 * i] = bflo(c0[i]); v[2 * i + 1] = bfhi(c0[i]); v[8 + 2 * i] = bflo(c1[i]); v[8 + 2 * i + 1] = bfhi(c1[i]); }
  float s = 0.f;
#pragma unroll
  for (int i = 0; i < 16; ++i) s += v[i];
  const float mean = wave_sum(s) * (1.0f / DM);
  float ss = 0.f;
#pragma unroll
  for (int i = 0; i < 8; ++i) { const float d = v[i] - mean; ss += d * d; }
  if (lane < 16) {
#pragma unroll
    for (int i = 8; i < 16; ++i) { const float d = v[i] - mean; ss += d * d; }
  }
  const float rstd = 1.0f / sqrtf(wave_sum(ss) * (1.0f / DM) + LN_EPS);
  u32x4 o0, o1;
#pragma unroll
  for (int i = 0; i < 4; ++i) { o0[i] = cvtpk((v[2 * i] - mean) * rstd, (v[2 * i + 1] - mean) * rstd); o1[i] = cvtpk((v[8 + 2 * i] - mean) * rstd, (v[8 + 2 * i + 1] - mean) * rstd); }
  u32x4* dst = (u32x4*)xnf + (size_t)(q >> 5) * 40 * 64 + (q & 31);
  dst[(lane >> 1) * 64 + (lane & 1) * 32] = o0;
  if (lane < 16) dst[((64 + lane) >> 1) * 64 + (lane & 1) * 32] = o1;
}

constexpr int F1_LDS = 163840;
constexpr int L_K = 0, L_V = 49152, L_Q = 98304;
constexpr float QSCALE = 0.08838834764831845f * 1.4426950408889634f;
constexpr float THR = 16.f;
#define PIN(x) asm volatile("" : "+v"(x))
#define EX2(v) __builtin_amdgcn_exp2f(v)
#define MEMFENCE() asm volatile("" ::: "memory")
DI float max3f(float a, float b, float c) { return __builtin_fmaxf(__builtin_fmaxf(a, b), c); }
DI float xhalf_max(float m) { auto rr = __builtin_amdgcn_permlane32_swap(__float_as_uint(m), __float_as_uint(m), false, false); return __builtin_fmaxf(__uint_as_float(rr[0]), __uint_as_float(rr[1])); }
DI float xhalf_sum(float m) { auto rr = __builtin_amdgcn_permlane32_swap(__float_as_uint(m), __float_as_uint(m), false, false); return __uint_as_float(rr[0]) + __uint_as_float(rr[1]); }
DI float max16(const f32x16& p) {
  float a = max3f(p[0], p[1], p[2]), b = max3f(p[3], p[4], p[5]);
  a = max3f(a, p[6], p[7]); b = max3f(b, p[8], p[9]); a = max3f(a, p[10], p[11]); b = max3f(b, p[12], p[13]); a = max3f(a, p[14], p[15]);
  return __builtin_fmaxf(a, b);
}
DI void cmask(f32x16& p0, f32x16& p1, int dq) {
#pragma unroll
  for (int r = 0; r < 16; ++r) { const int c = (r & 3) + 8 * (r >> 2); if (c > dq) p0[r] = -__builtin_inff(); if (c + 32 > dq) p1[r] = -__builtin_inff(); }
}
#define GLOAD4(d, voff, sbase) asm volatile("s_nop 4\n\tglobal_load_dwordx4 %0, %4, %5\n\tglobal_load_dwordx4 %1, %4, %5 offset:1024\n\tglobal_load_dwordx4 %2, %4, %5 offset:2048\n\tglobal_load_dwordx4 %3, %4, %5 offset:3072" \
    : "=&v"(d[0]), "=&v"(d[1]), "=&v"(d[2]), "=&v"(d[3]) : "v"(voff), "s"(sbase) : "memory")
#define GLOAD1(d, voff, sbase, OFF) asm volatile("s_nop 4\n\tglobal_load_dwordx4 %0, %1, %2 offset:" #OFF : "=&v"(d) : "v"(voff), "s"(sbase) : "memory")
#define GLOAD1_SC1(d, voff, sbase, OFF) asm volatile("s_nop 4\n\tglobal_load_dwordx4 %0, %1, %2 offset:" #OFF " sc1" : "=&v"(d) : "v"(voff), "s"(sbase) : "memory")
#define WAITV_TIE4(n, d) asm volatile("s_waitcnt vmcnt(" #n ")" : "+v"(d[0]), "+v"(d[1]), "+v"(d[2]), "+v"(d[3]) :: "memory")
DI void gstore_sc1(unsigned voff, const bf16x8& v, const void* sbase) {
  asm volatile("s_nop 4\n\tglobal_store_dwordx4 %0, %1, %2 sc1\n\ts_nop 1" :: "v"(voff), "v"(v), "s"(sbase) : "memory");
}
#define GLDS_OFF(voff, sbase, ldst, OFF) do { unsigned keep_; asm volatile("s_mov_b32 %0, m0\n\ts_mov_b32 m0, %3\n\ts_nop 2\n\tglobal_load_lds_dwordx4 %1, %2 offset:" #OFF "\n\ts_mov_b32 m0, %0" \
    : "=&s"(keep_) : "v"(voff), "s"(sbase), "s"(ldst) : "memory"); } while (0)
DI void wait_word(unsigned* word, unsigned want, unsigned* tmo) {
  for (unsigned spins = 0;; ++spins) {
    if ((unsigned)rfl(__hip_atomic_load(word, __ATOMIC_RELAXED, __HIP_MEMORY_SCOPE_AGENT)) >= want) break;
    if (spins > (1u << 16)) { __hip_atomic_store(tmo, 1u, __ATOMIC_RELAXED, __HIP_MEMORY_SCOPE_AGENT); break; }
    __builtin_amdgcn_s_sleep(1);
  }
}
#define ACQUIRE() __builtin_amdgcn_fence(__ATOMIC_ACQUIRE, "agent")

template <int GRP>
DI void attn_main(ldsp lds, unsigned lds0, char* __restrict__ ws, unsigned* flags, int h, int j, int wid, int lane, int tid, bf16x8 (&qr)[8]) {
  const int l31 = lane & 31, hh = lane >> 5;
  const unsigned vl16 = lane * 16;
  const char* Kh = ws + WS_KF + (size_t)h * 64 * 8192;
  const char* Vh = ws + WS_VF + (size_t)h * 128 * 4096;
  const unsigned vt2 = (unsigned)(wid * 2048 + lane * 16);
  const unsigned dt2 = lds0 + wid * 2048;
  const int n1 = j >= 4 ? 16 : 4 * j + 2;
  const int t2 = j >= 4 ? 4 * (8 - j) - 2 : 16;
  const int jp = 7 - j;
#define TILE(g) ((g) < n1 ? (g) : ((g) < 18 ? t2 + (g) - n1 : 0))
#define KSRC(g) (Kh + (size_t)TILE(g) * 16384)
#define VSRC(g) (Vh + (size_t)TILE(g) * 16384)
  const int rdoff = lane * 16;
  bf16x8 kq[4], vq[4], pw[4];
  f32x16 C0, C1, o[4], negm;
  float mhat, lsum;
  const f32x16 z = {};
#pragma unroll
  for (int i = 0; i < 4; ++i) kq[i] = ldsr128(lds + L_K + rdoff + i * 1024);
  {
    ldscp kb = lds + L_K + rdoff;
#pragma unroll
    for (int u = 0; u < 16; ++u) {
      if (u < 8) C0 = MFMA32(kq[u & 3], qr[u & 7], u == 0 ? z : C0); else C1 = MFMA32(kq[u & 3], qr[u & 7], u == 8 ? z : C1);
      if (u + 4 < 16) kq[u & 3] = ldsr128(kb + (u + 4) * 1024);
      if (GRP == 1 && u == 11) {
        SBAR(); WAIT_V(0); WAIT_L(0); BAR();
        GLDS_OFF(vt2, KSRC(3), dt2 + L_K, 0);
      }
      SBAR();
    }
    if (j == 0) cmask(C0, C1, 32 * wid + l31 - 4 * hh);
    float rm = xhalf_max(__builtin_fmaxf(max16(C0), max16(C1)));
    mhat = __builtin_fmaxf(rm, -1e30f);
#pragma unroll
    for (int r = 0; r < 16; ++r) { C0[r] -= mhat; C1[r] -= mhat; negm[r] = -mhat; }
    PIN(negm);
    if (GRP == 0) {
      SBAR(); WAIT_V(0); WAIT_L(0); BAR();
      if (tid == 0) __hip_atomic_store(flags + FL_Q + 32 * (8 * h + j), 1u, __ATOMIC_RELAXED, __HIP_MEMORY_SCOPE_AGENT);
      GLDS_OFF(vt2, KSRC(3), dt2 + L_K, 0);
    }
    GLDS_OFF(vt2, KSRC(3), dt2 + L_K, 1024);
#pragma unroll
    for (int i = 0; i < 4; ++i) kq[i] = ldsr128(lds + L_K + 16384 + rdoff + i * 1024);
    float s0 = 0.f;
#pragma unroll
    for (int r = 0; r < 16; ++r) { C0[r] = EX2(C0[r]); s0 += C0[r]; }
#pragma unroll
    for (int r = 0; r < 8; ++r) C1[r] = EX2(C1[r]);
    lsum = s0;
    pw[0] = pack8(C0, 0); pw[1] = pack8(C0, 1);
#pragma unroll
    for (int d = 0; d < 4; ++d) o[d] = z;
  }
  int sl_m1 = 0, sl_0 = 16384, sl_p1 = 32768;
  for (int g = 1; g < 18; ++g) {
    const bool seam = (g == n1);
    const int X = g < n1 ? j : jp;
    const int dd = TILE(g) - 4 * X;
    ldscp kb = lds + L_K + sl_0 + rdoff;
    ldscp vb = lds + L_V + sl_m1 + rdoff;
    ldscp kn = lds + L_K + sl_p1 + rdoff;
    const char* vs_n = VSRC(g + 1); const char* ks_n = KSRC(g + 3);
    const char* vs_p = VSRC(g);     const char* ks_p = KSRC(g + 2);
    const bool gp = g > 1;
    float lsum1 = 0.f, mhat1 = 0.f;
    if (seam) {
      mhat1 = mhat; mhat = 0.f;
#pragma unroll
      for (int r = 0; r < 16; ++r) negm[r] = 0.f;
      PIN(negm);
    }
    float sa = 0.f, sb = 0.f;
    SBAR();
#define KRD(u) kq[(u) & 3] = ldsr128(kb + ((u) + 4) * 1024)
#define VRD(v) vq[(v) & 3] = ldsr128(vb + (v) * 1024)
#define KPRE(i) kq[i] = ldsr128(kn + (i) * 1024)
#define BARRIER_G() do { if (g < 17) WAIT_V(2); else WAIT_V(0); WAIT_L(0); BAR(); SBAR(); \
      if ((g == n1 + 2 || (g == 17 && n1 + 2 > 17)) && tid == 64 * 4 * GRP && GRP == 0) \
        __hip_atomic_store(flags + FL_P + 32 * (8 * h + j), 1u, __ATOMIC_RELAXED, __HIP_MEMORY_SCOPE_AGENT); } while (0)
    C0 = MFMA32(kq[0], qr[0], negm); KRD(0); C1[8] = EX2(C1[8]); sa += C1[0]; sb += C1[1]; sa += C1[2]; sb += C1[3]; PIN(C1); PIN(sa); PIN(sb); SBAR();
    C0 = MFMA32(kq[1], qr[1], C0); KRD(1); C1[9] = EX2(C1[9]); sa += C1[4]; sb += C1[5]; sa += C1[6]; sb += C1[7]; PIN(C1); PIN(sa); PIN(sb); SBAR();
    C0 = MFMA32(kq[2], qr[2], C0); KRD(2); if (GRP == 0) { if (gp) GLDS_OFF(vt2, vs_p, dt2 + L_V + sl_0, 1024); } else { if (gp) GLDS_OFF(vt2, ks_p, dt2 + L_K + sl_m1, 1024); }
        C1[10] = EX2(C1[10]); pw[2] = pack8(C1, 0); PIN(C1); PIN(pw[2]); SBAR();
    C0 = MFMA32(kq[3], qr[3], C0); KRD(3); C1[11] = EX2(C1[11]); C1[12] = EX2(C1[12]); sa += C1[8]; sb += C1[9]; PIN(C1); PIN(sa); PIN(sb); SBAR();
    C0 = MFMA32(kq[0], qr[4], C0); KRD(4); C1[13] = EX2(C1[13]); C1[14] = EX2(C1[14]); sa += C1[10]; sb += C1[11]; PIN(C1); PIN(sa); PIN(sb); SBAR();
    C0 = MFMA32(kq[1], qr[5], C0); KRD(5); C1[15] = EX2(C1[15]); sa += C1[12]; sb += C1[13]; sa += C1[14]; PIN(C1); PIN(sa); PIN(sb); SBAR();
    C0 = MFMA32(kq[2], qr[6], C0); KRD(6); sb += C1[15]; pw[3] = pack8(C1, 1); PIN(pw[3]); PIN(sb); SBAR();
    C0 = MFMA32(kq[3], qr[7], C0); KRD(7); lsum += sa + sb; PIN(lsum); SBAR();
    C1 = MFMA32(kq[0], qr[0], negm); KRD(8); if (GRP == 0 && gp) GLDS_OFF(vt2, ks_p, dt2 + L_K + sl_m1, 0); SBAR();
    C1 = MFMA32(kq[1], qr[1], C1); KRD(9); SBAR();
    C1 = MFMA32(kq[2], qr[2], C1); KRD(10); SBAR();
    float mxa, mxb;
    C1 = MFMA32(kq[3], qr[3], C1); KRD(11); mxa = max3f(C0[0], C0[1], C0[2]); mxb = max3f(C0[3], C0[4], C0[5]); PIN(mxa); PIN(mxb); SBAR();
    if (GRP == 1) BARRIER_G();
    C1 = MFMA32(kq[0], qr[4], C1); VRD(0); if (GRP == 1) GLDS_OFF(vt2, vs_n, dt2 + L_V + sl_p1, 0); mxa = max3f(mxa, C0[6], C0[7]); mxb = max3f(mxb, C0[8], C0[9]); PIN(mxa); PIN(mxb); SBAR();
    C1 = MFMA32(kq[1], qr[5], C1); VRD(1); mxa = max3f(mxa, C0[10], C0[11]); mxb = max3f(mxb, C0[12], C0[13]); PIN(mxa); PIN(mxb); SBAR();
    C1 = MFMA32(kq[2], qr[6], C1); VRD(2); mxa = max3f(mxa, C0[14], C0[15]); mxa = __builtin_fmaxf(mxa, mxb); PIN(mxa); SBAR();
    C1 = MFMA32(kq[3], qr[7], C1); VRD(3); SBAR();
    o[0] = MFMA32(vq[0], pw[0], o[0]); VRD(4); if (GRP == 1) GLDS_OFF(vt2, vs_n, dt2 + L_V + sl_p1, 1024); SBAR();
    o[1] = MFMA32(vq[1], pw[0], o[1]); VRD(5); SBAR();
    float mxc, mxd;
    o[2] = MFMA32(vq[2], pw[0], o[2]); VRD(6); if (GRP == 0 && gp) GLDS_OFF(vt2, ks_p, dt2 + L_K + sl_m1, 1024);
        mxc = max3f(C1[0], C1[1], C1[2]); mxd = max3f(C1[3], C1[4], C1[5]); mxc = max3f(mxc, C1[6], C1[7]); mxd = max3f(mxd, C1[8], C1[9]); PIN(mxc); PIN(mxd); SBAR();
    o[3] = MFMA32(vq[3], pw[0], o[3]); VRD(7); mxc = max3f(mxc, C1[10], C1[11]); mxd = max3f(mxd, C1[12], C1[13]); mxc = max3f(mxc, C1[14], C1[15]); mxa = max3f(mxa, mxc, mxd); PIN(mxa); SBAR();
    o[0] = MFMA32(vq[0], pw[1], o[0]); VRD(8);
    float rm = mxa;
    MEMFENCE(); SBAR();
    if (dd >= 0) { cmask(C0, C1, 32 * wid + l31 - 64 * dd - 4 * hh); rm = __builtin_fmaxf(max16(C0), max16(C1)); }
    rm = xhalf_max(rm);
    float fsc = 1.f; bool resc = false;
    if (__builtin_expect(seam || __any(rm > THR), 0)) {
      const float dl = seam ? __builtin_fmaxf(rm, -1e30f) : __builtin_fmaxf(rm, 0.f);
      mhat += dl;
#pragma unroll
      for (int r = 0; r < 16; ++r) { C0[r] -= dl; C1[r] -= dl; negm[r] = -mhat; }
      PIN(negm);
      if (seam) { lsum1 = lsum; lsum = 0.f; }
      else { fsc = EX2(-dl); lsum *= fsc; resc = true; }
    }
    SBAR();
    sa = 0.f; sb = 0.f;
    o[1] = MFMA32(vq[1], pw[1], o[1]); VRD(9); C0[0] = EX2(C0[0]); C0[1] = EX2(C0[1]); C0[2] = EX2(C0[2]); PIN(C0); SBAR();
    o[2] = MFMA32(vq[2], pw[1], o[2]); VRD(10); C0[3] = EX2(C0[3]); C0[4] = EX2(C0[4]); sa += C0[0]; sb += C0[1]; PIN(C0); PIN(sa); PIN(sb); SBAR();
    o[3] = MFMA32(vq[3], pw[1], o[3]); VRD(11); C0[5] = EX2(C0[5]); C0[6] = EX2(C0[6]); sa += C0[2]; sb += C0[3]; PIN(C0); PIN(sa); PIN(sb); SBAR();
    o[0] = MFMA32(vq[0], pw[2], o[0]); VRD(12); if (GRP == 1) GLDS_OFF(vt2, ks_n, dt2 + L_K + sl_0, 0); C0[7] = EX2(C0[7]); C0[8] = EX2(C0[8]); sa += C0[4]; sb += C0[5]; PIN(C0); PIN(sa); PIN(sb); SBAR();
    o[1] = MFMA32(vq[1], pw[2], o[1]); VRD(13); pw[0] = pack8(C0, 0); C0[9] = EX2(C0[9]); PIN(pw[0]); PIN(C0); SBAR();
    o[2] = MFMA32(vq[2], pw[2], o[2]); VRD(14); C0[10] = EX2(C0[10]); C0[11] = EX2(C0[11]); sa += C0[6]; sb += C0[7]; PIN(C0); PIN(sa); PIN(sb); SBAR();
    o[3] = MFMA32(vq[3], pw[2], o[3]); VRD(15); C0[12] = EX2(C0[12]); C0[13] = EX2(C0[13]); sa += C0[8]; sb += C0[9]; PIN(C0); PIN(sa); PIN(sb); SBAR();
    if (GRP == 0) BARRIER_G();
    o[0] = MFMA32(vq[0], pw[3], o[0]); KPRE(0); if (GRP == 0) GLDS_OFF(vt2, vs_n, dt2 + L_V + sl_p1, 0); C0[14] = EX2(C0[14]); C0[15] = EX2(C0[15]); sa += C0[10]; sb += C0[11]; PIN(C0); PIN(sa); PIN(sb); SBAR();
    o[1] = MFMA32(vq[1], pw[3], o[1]); KPRE(1); C1[0] = EX2(C1[0]); C1[1] = EX2(C1[1]); sa += C0[12]; sb += C0[13]; PIN(C1); PIN(sa); PIN(sb); SBAR();
    o[2] = MFMA32(vq[2], pw[3], o[2]); KPRE(2); pw[1] = pack8(C0, 1); C1[2] = EX2(C1[2]); C1[3] = EX2(C1[3]); PIN(pw[1]); PIN(C1); SBAR();
    o[3] = MFMA32(vq[3], pw[3], o[3]); KPRE(3); C1[4] = EX2(C1[4]); C1[5] = EX2(C1[5]); sa += C0[14]; sb += C0[15]; PIN(C1); PIN(sa); PIN(sb); SBAR();
    C1[6] = EX2(C1[6]); C1[7] = EX2(C1[7]); lsum += sa + sb; PIN(C1); PIN(lsum);
    if (resc) {
#pragma unroll
      for (int d = 0; d < 4; ++d) o[d] = o[d] * fsc;
    }
    if (seam) {
      const float lt = xhalf_sum(lsum1);
      const char* pst = ws + WS_PF + (size_t)((h * 8 + j) * 8 + wid) * 8192;
#pragma unroll
      for (int d = 0; d < 4; ++d) { gstore_sc1(vl16, pack8(o[d], 0), pst + (2 * d) * 1024); gstore_sc1(vl16, pack8(o[d], 1), pst + (2 * d + 1) * 1024); o[d] = z; }
      const unsigned long long sv = ((unsigned long long)__float_as_uint(lt) << 32) | __float_as_uint(mhat1);
      __hip_atomic_store((unsigned long long*)(ws + WS_PS) + (size_t)((h * 8 + j) * 8 + wid) * 64 + lane, sv, __ATOMIC_RELAXED, __HIP_MEMORY_SCOPE_AGENT);
    }
    if (g == n1 - 1) {
      wait_word(flags + FL_Q + 32 * (8 * h + jp), 1u, flags + FL_TMO);
      const char* qsrc = ws + WS_QF + (size_t)(h * 64 + 8 * jp + wid) * 8192;
      u32x4 qa[4], qb[4];
      GLOAD1_SC1(qa[0], vl16, qsrc, 0); GLOAD1_SC1(qa[1], vl16, qsrc, 1024); GLOAD1_SC1(qa[2], vl16, qsrc, 2048); GLOAD1_SC1(qa[3], vl16, qsrc, 3072);
      GLOAD1_SC1(qb[0], vl16, qsrc + 4096, 0); GLOAD1_SC1(qb[1], vl16, qsrc + 4096, 1024); GLOAD1_SC1(qb[2], vl16, qsrc + 4096, 2048); GLOAD1_SC1(qb[3], vl16, qsrc + 4096, 3072);
      WAITV_TIE4(0, qa); WAITV_TIE4(0, qb);
#pragma unroll
      for (int s = 0; s < 4; ++s) { qr[s] = __builtin_bit_cast(bf16x8, qa[s]); qr[4 + s] = __builtin_bit_cast(bf16x8, qb[s]); }
    }
    { const int t_ = sl_m1; sl_m1 = sl_0; sl_0 = sl_p1; sl_p1 = t_; }
  }
  {
    ldscp vb = lds + L_V + sl_m1 + rdoff;
#pragma unroll
    for (int i = 0; i < 4; ++i) vq[i] = ldsr128(vb + i * 1024);
    float sa = 0.f;
#pragma unroll
    for (int r = 8; r < 16; ++r) C1[r] = EX2(C1[r]);
#pragma unroll
    for (int r = 0; r < 16; ++r) sa += C1[r];
    lsum += sa;
    pw[2] = pack8(C1, 0); pw[3] = pack8(C1, 1);
    SBAR();
#pragma unroll
    for (int v = 0; v < 16; ++v) {
      o[v & 3] = MFMA32(vq[v & 3], pw[v >> 2], o[v & 3]);
      if (v + 4 < 16) vq[v & 3] = ldsr128(vb + (v + 4) * 1024);
      SBAR();
    }
    lsum = xhalf_sum(lsum);
    wait_word(flags + FL_P + 32 * (8 * h + jp), 1u, flags + FL_TMO);
    const char* psrc = ws + WS_PF + (size_t)((h * 8 + jp) * 8 + wid) * 8192;
    u32x4 pa[4], pb[4];
    GLOAD1_SC1(pa[0], vl16, psrc, 0); GLOAD1_SC1(pa[1], vl16, psrc, 1024); GLOAD1_SC1(pa[2], vl16, psrc, 2048); GLOAD1_SC1(pa[3], vl16, psrc, 3072);
    GLOAD1_SC1(pb[0], vl16, psrc + 4096, 0); GLOAD1_SC1(pb[1], vl16, psrc + 4096, 1024); GLOAD1_SC1(pb[2], vl16, psrc + 4096, 2048); GLOAD1_SC1(pb[3], vl16, psrc + 4096, 3072);
    const unsigned long long sv = __hip_atomic_load((const unsigned long long*)(ws + WS_PS) + (size_t)((h * 8 + jp) * 8 + wid) * 64 + lane, __ATOMIC_RELAXED, __HIP_MEMORY_SCOPE_AGENT);
    WAITV_TIE4(0, pa); WAITV_TIE4(0, pb);
    const float om = __uint_as_float((unsigned)sv), ol = __uint_as_float((unsigned)(sv >> 32));
    const float m = __builtin_fmaxf(mhat, om);
    const float f0 = EX2(mhat - m), f1 = EX2(om - m);
    const float inv = __builtin_amdgcn_rcpf(lsum * f0 + ol * f1);
    const float g0 = f0 * inv, g1 = f1 * inv;
    u16* cdst = (u16*)(ws + WS_CTX) + ((size_t)((8 * jp + wid) * 32 + h) * 8 * 64 + lane) * 8;
#pragma unroll
    for (int d = 0; d < 4; ++d)
#pragma unroll
      for (int sp = 0; sp < 2; ++sp) {
        const u32x4 v4 = (2 * d + sp) < 4 ? pa[2 * d + sp] : pb[2 * d + sp - 4];
        u32x4 w;
#pragma unroll
        for (int e = 0; e < 4; ++e) w[e] = cvtpk(o[d][8 * sp + 2 * e] * g0 + bflo(v4[e]) * g1, o[d][8 * sp + 2 * e + 1] * g0 + bfhi(v4[e]) * g1);
        *(u32x4*)(cdst + (size_t)(2 * d + sp) * 512) = w;
      }
  }
#undef KRD
#undef VRD
#undef KPRE
#undef BARRIER_G
#undef TILE
#undef KSRC
#undef VSRC
}

__global__ __launch_bounds__(512, 2) void kF1(const u16* __restrict__ ckv, const u16* __restrict__ wuk, const u16* __restrict__ wuv, const u16* __restrict__ wq,
                                              char* __restrict__ ws) {
  extern __shared__ __attribute__((aligned(16))) unsigned char smem[];
  const int bidx = blockIdx.x, xcd = bidx & 7, rr_ = bidx >> 3;
  const int h = xcd * 4 + (rr_ >> 3), j = rr_ & 7;
  const int tid = threadIdx.x, lane = tid & 63;
  const int wid = rfl(tid >> 6);
  const int l31 = lane & 31, hh = lane >> 5;
  ldsp lds = (ldsp)smem;
  const unsigned lds0 = (unsigned)(uintptr_t)smem;
  unsigned* flags = (unsigned*)(ws + WS_FLAGS);
  const unsigned vl16 = lane * 16;
  bf16x8 kvp[16];
  {
    const int wr = wid >> 2, wc = wid & 3;
    unsigned voffC, voffW;
    { const int row = 8 * wid + (lane >> 3); voffC = (unsigned)(row * 1024 + (((lane & 7) ^ ((row >> 1) & 7)) << 4)); }
    { const int row = 8 * (wid >> 1) + ((lane >> 2) & 7); const int chq = 2 * (wid & 1) + (lane >> 5);
      const int ch = 4 * chq + ((lane & 3) ^ ((row >> 2) & 3)); voffW = (unsigned)(row * 8192 + ch * 16); }
    const char* cbase = (const char*)ckv + (size_t)(256 * j) * 1024;
    const char* wkb = (const char*)wuk + h * 256;
    const char* wvb = (const char*)wuv + h * 256;
    const unsigned dstw = lds0 + wid * 1024;
#define CBUF(b, a) (((b) * 2 + (a)) * 16384)
#define WBUF(b, a) (65536 + ((b) * 2 + (a)) * 16384)
#define STAGE_C(b, a, kt) do { const char* s_ = cbase + (a) * 131072 + (kt) * 128; glds16(voffC, s_, dstw + CBUF(b, a)); glds16(voffC, s_ + 65536, dstw + CBUF(b, a) + 8192); } while (0)
#define STAGE_W(b, a, kt) do { const char* s_ = ((a) ? wvb : wkb) + (size_t)(kt) * 64 * 8192; glds16(voffW, s_, dstw + WBUF(b, a)); glds16(voffW, s_ + 32 * 8192, dstw + WBUF(b, a) + 8192); } while (0)
    int aoff[4], boff[2];
#pragma unroll
    for (int s = 0; s < 4; ++s) aoff[s] = 128 * (wr * 64 + l31) + (((2 * s + hh) ^ ((l31 >> 1) & 7)) << 4);
    { const int blk = (lane >> 4) & 1, q = (lane & 15) >> 2, p = lane & 3;
#pragma unroll
      for (int t = 0; t < 2; ++t) boff[t] = 2048 * hh + 512 * wc + 64 * (4 * t + q) + (((2 * blk + (p >> 1)) ^ ((2 * hh + t) & 3)) << 4) + 8 * (p & 1); }
#define LDA(dst, b, a) do { _Pragma("unroll") for (int m = 0; m < 2; ++m) _Pragma("unroll") for (int s = 0; s < 4; ++s) dst[m][s] = ldsr128(lds + CBUF(b, a) + m * 4096 + aoff[s]); } while (0)
#define LDB(dst, b, a) do { _Pragma("unroll") for (int s = 0; s < 4; ++s) dst[s] = cat8(vtr(lds + WBUF(b, a) + s * 4096 + boff[0]), vtr(lds + WBUF(b, a) + s * 4096 + boff[1])); } while (0)
#define MMAK(ai, At, Bt) do { __builtin_amdgcn_s_setprio(1); _Pragma("unroll") for (int s = 0; s < 4; ++s) _Pragma("unroll") for (int m = 0; m < 2; ++m) \
    accK[ai][m] = MFMA32(Bt[s], At[m][s], accK[ai][m]); __builtin_amdgcn_s_setprio(0); } while (0)
#define MMAV(ai, At, Bt) do { __builtin_amdgcn_s_setprio(1); _Pragma("unroll") for (int s = 0; s < 4; ++s) _Pragma("unroll") for (int m = 0; m < 2; ++m) \
    accV[ai][m] = MFMA32(At[m][s], Bt[s], accV[ai][m]); __builtin_amdgcn_s_setprio(0); } while (0)
    f32x16 accK[2][2] = {}, accV[2][2] = {};
    bf16x8 At[2][4], B0[4], B1[4];
    constexpr int nt = DC / 64;
    STAGE_W(0, 0, 0); STAGE_C(0, 0, 0); STAGE_W(0, 1, 0); STAGE_C(0, 1, 0);
    if (wr == 1) BAR();
    WAIT_V(4); BAR();
    STAGE_W(1, 0, 1); STAGE_C(1, 0, 1); STAGE_W(1, 1, 1);
    WAIT_V(6); BAR();
    for (int t = 0; t < nt - 2; t += 2) {
      LDB(B0, 0, 0); SBAR(); LDA(At, 0, 0); STAGE_C(1, 1, t + 1);
      WAIT_L(8); BAR(); WAIT_L(0); MMAK(0, At, B0); BAR(); SBAR();
      LDB(B1, 0, 1); STAGE_W(0, 0, t + 2);
      BAR(); WAIT_L(0); MMAV(0, At, B1); BAR();
      LDA(At, 0, 1); STAGE_C(0, 0, t + 2);
      BAR(); WAIT_L(0); MMAK(1, At, B0); BAR(); SBAR();
      STAGE_W(0, 1, t + 2);
      WAIT_V(6); BAR(); MMAV(1, At, B1); BAR();
      LDB(B0, 1, 0); SBAR(); LDA(At, 1, 0); STAGE_C(0, 1, t + 2);
      WAIT_L(8); BAR(); WAIT_L(0); MMAK(0, At, B0); BAR(); SBAR();
      LDB(B1, 1, 1); STAGE_W(1, 0, t + 3);
      BAR(); WAIT_L(0); MMAV(0, At, B1); BAR();
      LDA(At, 1, 1); STAGE_C(1, 0, t + 3);
      BAR(); WAIT_L(0); MMAK(1, At, B0); BAR(); SBAR();
      STAGE_W(1, 1, t + 3);
      WAIT_V(6); BAR(); MMAV(1, At, B1); BAR();
    }
    { LDB(B0, 0, 0); WAIT_V(0); LDA(At, 0, 0); STAGE_C(1, 1, nt - 1);
      BAR(); WAIT_L(0); MMAK(0, At, B0); BAR();
      LDB(B1, 0, 1); BAR(); WAIT_L(0); MMAV(0, At, B1); BAR();
      LDA(At, 0, 1); WAIT_V(4); BAR(); WAIT_L(0); MMAK(1, At, B0); MMAV(1, At, B1); BAR(); }
    { LDB(B0, 1, 0); LDA(At, 1, 0); WAIT_V(2); BAR(); WAIT_L(0); MMAK(0, At, B0); BAR();
      LDB(B1, 1, 1); WAIT_V(0); BAR(); WAIT_L(0); MMAV(0, At, B1); BAR();
      LDA(At, 1, 1); BAR(); WAIT_L(0); MMAK(1, At, B0); MMAV(1, At, B1); BAR(); }
    if (wr == 0) BAR();
#pragma unroll
    for (int ai = 0; ai < 2; ++ai)
#pragma unroll
      for (int m = 0; m < 2; ++m)
#pragma unroll
        for (int sp = 0; sp < 2; ++sp) {
          kvp[((ai * 2 + m) * 2 + sp) * 2] = pack8(accK[ai][m], sp);
          kvp[((ai * 2 + m) * 2 + sp) * 2 + 1] = pack8(accV[ai][m], sp);
        }
#undef CBUF
#undef WBUF
#undef STAGE_C
#undef STAGE_W
#undef LDA
#undef LDB
#undef MMAK
#undef MMAV
  }
  const char* Kh = ws + WS_KF + (size_t)h * 64 * 8192;
  const char* Vh = ws + WS_VF + (size_t)h * 128 * 4096;
  const unsigned vt2 = (unsigned)(wid * 2048 + lane * 16);
  const unsigned dt2 = lds0 + wid * 2048;
  const int n1 = j >= 4 ? 16 : 4 * j + 2;
  const int t2 = j >= 4 ? 4 * (8 - j) - 2 : 16;
  const int jp = 7 - j;
#define TILE(g) ((g) < n1 ? (g) : ((g) < 18 ? t2 + (g) - n1 : 0))
#define KSRC(g) (Kh + (size_t)TILE(g) * 16384)
#define VSRC(g) (Vh + (size_t)TILE(g) * 16384)
#define DMA_T(src, ldsoff) do { GLDS_OFF(vt2, src, dt2 + (ldsoff), 0); GLDS_OFF(vt2, src, dt2 + (ldsoff), 1024); } while (0)
  bf16x8 qr[8];
  {
    const int wr = wid >> 2, wc = wid & 3;
    const char* kst = ws + WS_KF + ((size_t)(h * 64 + 8 * j + 2 * wr) * 8 + 2 * wc) * 1024;
    const char* vst = ws + WS_VF + ((size_t)(h * 128 + 2 * (8 * j + 2 * wr)) * 4 + wc) * 1024;
#define KVST(i) gstore_sc1(vl16, kvp[i], ((i) & 1) ? vst + (((i) >> 3) * 8 + (((i) >> 2) & 1) * 2 + (((i) >> 1) & 1)) * 4096 : kst + (((i) >> 3) * 4 + (((i) >> 2) & 1)) * 8192 + (((i) >> 1) & 1) * 1024)
    unsigned voffW;
    { const int row = 8 * (wid >> 1) + ((lane >> 2) & 7); const int chq = 2 * (wid & 1) + (lane >> 5);
      const int ch = 4 * chq + ((lane & 3) ^ ((row >> 2) & 3)); voffW = (unsigned)(row * 8192 + ch * 16); }
    const char* wqb = (const char*)wq + h * 256;
    const unsigned dstw = lds0 + L_Q + wid * 1024;
#define STAGE_Q1(c, i) glds16(voffW, wqb + (size_t)(c) * 64 * 8192 + (i) * 32 * 8192, dstw + ((c) & 3) * 16384 + (i) * 8192)
    const char* xb = ws + WS_XNF + (size_t)(8 * j + wid) * 40 * 1024;
    int boff[2];
    { const int blk = (lane >> 4) & 1, q = (lane & 15) >> 2, p = lane & 3;
#pragma unroll
      for (int t = 0; t < 2; ++t) boff[t] = L_Q + 2048 * hh + 64 * (4 * t + q) + (((2 * blk + (p >> 1)) ^ ((2 * hh + t) & 3)) << 4) + 8 * (p & 1); }
    u32x4 xf[3][4];
    f32x16 acc[4] = {};
    bf16x8 aq[8];
#define ARD(u) aq[(u) & 7] = cat8(vtr(lds + (((u) >> 4) & 3) * 16384 + (((u) >> 2) & 3) * 4096 + ((u) & 3) * 512 + boff[0]), vtr(lds + (((u) >> 4) & 3) * 16384 + (((u) >> 2) & 3) * 4096 + ((u) & 3) * 512 + boff[1]))
    GLOAD4(xf[0], vl16, xb); STAGE_Q1(0, 0); STAGE_Q1(0, 1);
    STAGE_Q1(1, 0); STAGE_Q1(1, 1);
    GLOAD4(xf[1], vl16, xb + 4096); STAGE_Q1(2, 0); STAGE_Q1(2, 1);
    WAIT_V(8); BAR();
#pragma unroll
    for (int u = 0; u < 6; ++u) ARD(u);
#pragma unroll
    for (int c = 0; c < 10; ++c) {
      if (c == 0) { WAITV_TIE4(6, xf[0]); }
      else if (c == 1) { WAITV_TIE4(9, xf[1]); }
      else if (c <= 5) { WAITV_TIE4(12, xf[c % 3]); }
      else if (c == 6) { WAITV_TIE4(10, xf[c % 3]); }
      else if (c == 7) { WAITV_TIE4(7, xf[c % 3]); }
      else if (c == 8) { WAITV_TIE4(4, xf[c % 3]); }
      else {
        if (wid == 0) { wait_word(flags + FL_KV + 64 * h, 8u, flags + FL_TMO); ACQUIRE(); }
        WAITV_TIE4(0, xf[c % 3]);
      }
      SBAR(); BAR();
      if (c == 8 && tid == 0) __hip_atomic_fetch_add(flags + FL_KV + 64 * h, 1u, __ATOMIC_RELAXED, __HIP_MEMORY_SCOPE_AGENT);
      SBAR();
      const char* xn_ = xb + (c + 2) * 4096;
#pragma unroll
      for (int i = 0; i < 16; ++i) {
        const int u = 16 * c + i;
        acc[i & 3] = MFMA32(aq[u & 7], __builtin_bit_cast(bf16x8, xf[c % 3][i >> 2]), acc[i & 3]);
        if (u + 6 < 160) ARD(u + 6);
        if (c + 2 < 10) {
          if (i == 1) GLOAD1(xf[(c + 2) % 3][0], vl16, xn_, 0);
          if (i == 3) GLOAD1(xf[(c + 2) % 3][1], vl16, xn_, 1024);
          if (i == 5) GLOAD1(xf[(c + 2) % 3][2], vl16, xn_, 2048);
          if (i == 7) GLOAD1(xf[(c + 2) % 3][3], vl16, xn_, 3072);
        }
        if (c + 3 < 10) { if (i == 9) STAGE_Q1(c + 3, 0); if (i == 11) STAGE_Q1(c + 3, 1); }
        if (c < 5) { if (i == 13) KVST(3 * c); if (i == 14) KVST(3 * c + 1); if (i == 15) KVST(3 * c + 2); }
        if (c == 5 && i == 13) KVST(15);
        if (c == 9) {
          if (i == 1) DMA_T(KSRC(0), L_K);
          if (i == 4) DMA_T(VSRC(0), L_V);
          if (i == 7) DMA_T(KSRC(1), L_K + 16384);
          if (i == 10) DMA_T(VSRC(1), L_V + 16384);
          if (i == 13) DMA_T(KSRC(2), L_K + 32768);
        }
        SBAR();
      }
    }
#pragma unroll
    for (int dt = 0; dt < 4; ++dt) { acc[dt] = acc[dt] * QSCALE; qr[2 * dt] = pack8(acc[dt], 0); qr[2 * dt + 1] = pack8(acc[dt], 1); }
    { const char* qst = ws + WS_QF + (size_t)(h * 64 + 8 * j + wid) * 8192;
#pragma unroll
      for (int s = 0; s < 8; ++s) gstore_sc1(vl16, qr[s], qst + s * 1024); }
#undef KVST
#undef STAGE_Q1
#undef ARD
  }
  WAIT_V(16); BAR();
  if (wid < 4) attn_main<0>(lds, lds0, ws, flags, h, j, wid, lane, tid, qr);
  else attn_main<1>(lds, lds0, ws, flags, h, j, wid, lane, tid, qr);
#undef TILE
#undef KSRC
#undef VSRC
#undef DMA_T
}

constexpr int T_LDS = 163840;
constexpr int FL_P3 = 20480;
constexpr int FL_H = 22528;
constexpr int FL_U = 23552;
#define GLDS_SC1(voff, sbase, ldst) do { unsigned keep_; asm volatile("s_mov_b32 %0, m0\n\ts_mov_b32 m0, %3\n\ts_nop 2\n\tglobal_load_lds_dwordx4 %1, %2 sc1\n\ts_mov_b32 m0, %0" \
    : "=&s"(keep_) : "v"(voff), "s"(sbase), "s"(ldst) : "memory"); } while (0)
DI void gstore_sc1_f(unsigned voff, const f32x4& v, const void* sbase) {
  asm volatile("s_nop 4\n\tglobal_store_dwordx4 %0, %1, %2 sc1\n\ts_nop 1" :: "v"(voff), "v"(v), "s"(sbase) : "memory");
}
DI void gstore_sc1_u(unsigned voff, const u32x4& v, const void* sbase) {
  asm volatile("s_nop 4\n\tglobal_store_dwordx4 %0, %1, %2 sc1\n\ts_nop 1" :: "v"(voff), "v"(v), "s"(sbase) : "memory");
}
#define WAITVN(n) asm volatile("s_waitcnt vmcnt(%0)" :: "i"(n) : "memory")

constexpr int ML_W = 0, ML_A = 98304;
template <bool FIRST> struct MlpCfg {
  static constexpr int PITCH = FIRST ? FF * 2 : SEQ * 2;
  static constexpr int NKT = FIRST ? SEQ / 64 : FF / 64;
  static constexpr int NST = (NKT + 1) / 2;
};
template <bool FIRST> DI void mlp_wtile(unsigned lds0, const char* wb, unsigned voffW, int wid, int kt) {
  constexpr int NKT = MlpCfg<FIRST>::NKT, PITCH = MlpCfg<FIRST>::PITCH;
  const int kc = kt < NKT ? kt : NKT - 1;
  glds16(voffW, wb + (size_t)kc * 64 * PITCH, lds0 + ML_W + (kt % 12) * 8192 + wid * 1024);
}
template <bool FIRST> DI unsigned mlp_voffw(int wid, int lane) {
  const int row = 8 * wid + (lane >> 3); const int f = ((row >> 1) & 1) | (((row >> 3) & 1) << 1);
  return (unsigned)(row * MlpCfg<FIRST>::PITCH + (((lane & 7) ^ (f << 1)) << 4));
}
template <bool FIRST> DI void mlp_prefetch_w(unsigned lds0, const u16* W, int wt, int wid, int lane) {
  const char* wb = (const char*)W + wt * 128;
  const unsigned voffW = mlp_voffw<FIRST>(wid, lane);
#pragma unroll
  for (int kt = 0; kt < 12; ++kt) mlp_wtile<FIRST>(lds0, wb, voffW, wid, kt);
}
constexpr int mlp_allowed(int s, int nst, int na) {
  int n = 0;
  if (s == 0) return na;
  if (s - 2 >= 1 && s + 3 < nst) n += 2;
  if (s + 1 < nst) n += na;
  if (s - 1 >= 1 && s + 4 < nst) n += 2;
  return n;
}
template <bool FIRST>
DI void mlp_main(unsigned char* smem, const u16* __restrict__ W, const char* __restrict__ act, int wt, int dg, f32x4 (&acc)[5]) {
  constexpr int NKT = MlpCfg<FIRST>::NKT, NST = MlpCfg<FIRST>::NST;
  const int tid = threadIdx.x, lane = tid & 63;
  const int wid = rfl(tid >> 6), kh = wid >> 2, ct = wid & 3;
  ldsp lds = (ldsp)smem;
  const unsigned lds0 = (unsigned)(uintptr_t)smem;
  const unsigned voffW = mlp_voffw<FIRST>(wid, lane);
  const unsigned vl16 = lane * 16;
  const char* wb = (const char*)W + wt * 128;
  const char* ab = act + (size_t)(5 * dg) * 1024;
#define APIECE(stg, i) do { const int T_ = 4 * (stg) + (i) / 5; const int Tc_ = T_ < 2 * NKT ? T_ : 2 * NKT - 1; \
    GLDS_SC1(vl16, ab + ((size_t)Tc_ * 40 + (i) % 5) * 1024, lds0 + ML_A + ((stg) % 3) * 20480 + (i) * 1024); } while (0)
#define ASTAGE(stg) do { APIECE(stg, wid); APIECE(stg, wid + 8); if (kh == 0) APIECE(stg, wid + 16); } while (0)
  int woff;
  { const int g = lane >> 4, q = (lane & 15) >> 2, p = lane & 3; const int f = (q >> 1) | ((g & 1) << 1);
    woff = ML_W + 128 * (32 * kh + 8 * g + q) + (((2 * ct + (p >> 1)) ^ (f << 1)) << 4) + 8 * (p & 1); }
  const int aoff = ML_A + kh * 5120 + lane * 16;
#pragma unroll
  for (int d = 0; d < 5; ++d) acc[d] = (f32x4){0.f, 0.f, 0.f, 0.f};
  ASTAGE(0); ASTAGE(1);
#pragma unroll
  for (int s = 0; s < NST; ++s) {
    if (kh == 0) WAITVN(mlp_allowed(s, NST, 3)); else WAITVN(mlp_allowed(s, NST, 2));
    SBAR(); BAR();
    if (s + 2 < NST) ASTAGE(s + 2);
    if (s >= 1 && s + 5 < NST) { mlp_wtile<FIRST>(lds0, wb, voffW, wid, 2 * (s + 5)); mlp_wtile<FIRST>(lds0, wb, voffW, wid, 2 * (s + 5) + 1); }
    bf16x8 wf[2], af[2][5];
#pragma unroll
    for (int k = 0; k < 2; ++k) {
      const int slot = ((2 * s + k) % 12) * 8192;
      wf[k] = cat8(vtr(lds + slot + woff), vtr(lds + slot + woff + 512));
#pragma unroll
      for (int d = 0; d < 5; ++d) af[k][d] = ldsr128(lds + (s % 3) * 20480 + aoff + k * 10240 + d * 1024);
    }
#pragma unroll
    for (int k = 0; k < 2; ++k) {
      if (k == 0 || 2 * s + 1 < NKT) {
#pragma unroll
        for (int d = 0; d < 5; ++d) acc[d] = FIRST ? MFMA16(wf[k], af[k][d], acc[d]) : MFMA16(af[k][d], wf[k], acc[d]);
      }
    }
    SBAR();
  }
  BAR();
#pragma unroll
  for (int d = 0; d < 5; ++d) *(LAS f32x4*)(lds + ((kh * 4 + ct) * 5 + d) * 1024 + lane * 16) = acc[d];
  WAIT_L(0); BAR();
#undef APIECE
#undef ASTAGE
}

__global__ __launch_bounds__(512, 2) void kT(const u16* __restrict__ x, const u16* __restrict__ wo, const u16* __restrict__ w1, const u16* __restrict__ b1,
                                             const u16* __restrict__ w2, const u16* __restrict__ b2, char* __restrict__ ws, u16* __restrict__ out) {
  extern __shared__ __attribute__((aligned(16))) unsigned char smem[];
  const int b = blockIdx.x, xcd = b & 7, r = b >> 3;
  const int tid = threadIdx.x, lane = tid & 63;
  const int wid = rfl(tid >> 6);
  ldsp lds = (ldsp)smem;
  const unsigned lds0 = (unsigned)(uintptr_t)smem;
  unsigned* flags = (unsigned*)(ws + WS_FLAGS);
  const unsigned vl16 = lane * 16;
  const int ksp = xcd >> 1, panel = (xcd & 1) * 8 + (r >> 2), ctile = r & 3;
  const int idx5 = xcd * 27 + r;
  const int wt5 = idx5 >> 3, dg5 = idx5 & 7;
  const int wt6 = xcd * 4 + (r >> 3), dg6 = r & 7;
  {
    const int kh = wid >> 2, st = wid & 3, hh = lane >> 5;
    unsigned voff[5];
#pragma unroll
    for (int i = 0; i < 5; ++i) {
      const int id = 2 * (5 * wid + i) + (lane >> 5), half = id / 40, rem = id % 40, rg = rem / 5, c = rem % 5;
      const int row = 64 * half + 8 * rg + ((lane >> 2) & 7);
      voff[i] = (unsigned)(row * 1280 + (32 * c + 8 * ((lane & 3) ^ ((row >> 2) & 3))) * 2);
    }
    const char* wb = (const char*)wo + (size_t)(ksp * 1024) * 1280 + ctile * 320;
    const unsigned dstw = lds0 + wid * 5120;
#define OPIECE(hd, i) glds16(voff[i], wb + (size_t)(hd) * 128 * 1280, dstw + ((hd) & 3) * 40960 + (i) * 1024)
#define STAGE_O(hd) do { OPIECE(hd, 0); OPIECE(hd, 1); OPIECE(hd, 2); OPIECE(hd, 3); OPIECE(hd, 4); } while (0)
    const char* cb = ws + WS_CTX + ((size_t)((4 * panel + st) * 32 + 8 * ksp) * 8 + 4 * kh) * 1024;
    int boff[2];
    { const int blk = (lane >> 4) & 1, q = (lane & 15) >> 2, p = lane & 3;
#pragma unroll
      for (int t = 0; t < 2; ++t) boff[t] = kh * 20480 + 2560 * t + 64 * (4 * hh + q) + (((2 * blk + (p >> 1)) ^ ((2 * t + hh) & 3)) << 4) + 8 * (p & 1); }
    u32x4 xf[3][4];
    f32x16 acc[5] = {};
    bf16x8 aq[6];
#define ARD(U) aq[(U) % 6] = cat8(vtr(lds + (((U) / 20) & 3) * 40960 + (((U) % 20) / 5) * 5120 + ((U) % 5) * 512 + boff[0]), vtr(lds + (((U) / 20) & 3) * 40960 + (((U) % 20) / 5) * 5120 + ((U) % 5) * 512 + boff[1]))
    GLOAD4(xf[0], vl16, cb); STAGE_O(0);
    STAGE_O(1);
    GLOAD4(xf[1], vl16, cb + 8192); STAGE_O(2);
    WAIT_V(14); BAR();
#pragma unroll
    for (int U = 0; U < 5; ++U) ARD(U);
#pragma unroll
    for (int hd = 0; hd < 8; ++hd) {
      if (hd == 0) { WAITV_TIE4(9, xf[0]); }
      else if (hd <= 5) { WAITV_TIE4(9, xf[hd % 3]); }
      else if (hd == 6) { WAITV_TIE4(4, xf[hd % 3]); }
      else { WAITV_TIE4(0, xf[hd % 3]); }
      SBAR(); BAR(); SBAR();
      const char* xn_ = cb + (size_t)(hd + 2) * 8192;
#pragma unroll
      for (int u = 0; u < 20; ++u) {
        const int U = 20 * hd + u;
        acc[u % 5] = MFMA32(aq[U % 6], __builtin_bit_cast(bf16x8, xf[hd % 3][u / 5]), acc[u % 5]);
        if (U + 5 < 160) ARD(U + 5);
        if (hd + 2 < 8) {
          if (u == 1) GLOAD1(xf[(hd + 2) % 3][0], vl16, xn_, 0);
          if (u == 3) GLOAD1(xf[(hd + 2) % 3][1], vl16, xn_, 1024);
          if (u == 5) GLOAD1(xf[(hd + 2) % 3][2], vl16, xn_, 2048);
          if (u == 7) GLOAD1(xf[(hd + 2) % 3][3], vl16, xn_, 3072);
        }
        if (hd + 3 < 8) {
          if (u == 9) OPIECE(hd + 3, 0);
          if (u == 11) OPIECE(hd + 3, 1);
          if (u == 13) OPIECE(hd + 3, 2);
          if (u == 15) OPIECE(hd + 3, 3);
          if (u == 17) OPIECE(hd + 3, 4);
        }
        SBAR();
      }
    }
#undef OPIECE
#undef STAGE_O
#undef ARD
    BAR();
    ldsp xw = lds + (kh == 0 ? 49152 : 0) + st * 12288 + lane * 16;
    ldscp xr = lds + (kh == 0 ? 0 : 49152) + st * 12288 + lane * 16;
    if (kh == 0) {
#pragma unroll
      for (int i = 0; i < 2; ++i)
#pragma unroll
        for (int g = 0; g < 4; ++g) *(LAS f32x4*)(xw + (i * 4 + g) * 1024) = (f32x4){acc[3 + i][4 * g], acc[3 + i][4 * g + 1], acc[3 + i][4 * g + 2], acc[3 + i][4 * g + 3]};
    } else {
#pragma unroll
      for (int i = 0; i < 3; ++i)
#pragma unroll
        for (int g = 0; g < 4; ++g) *(LAS f32x4*)(xw + (i * 4 + g) * 1024) = (f32x4){acc[i][4 * g], acc[i][4 * g + 1], acc[i][4 * g + 2], acc[i][4 * g + 3]};
    }
    WAIT_L(0); BAR();
    const char* sb = ws + WS_SLAB + (((size_t)((ksp * 64 + 4 * panel + st) * 20 + 5 * ctile) * 4) * 256) * 4;
    if (kh == 0) {
#pragma unroll
      for (int i = 0; i < 3; ++i)
#pragma unroll
        for (int g = 0; g < 4; ++g) { const f32x4 o = *(const LAS f32x4*)(xr + (i * 4 + g) * 1024);
          gstore_sc1_f(vl16, (f32x4){acc[i][4 * g] + o[0], acc[i][4 * g + 1] + o[1], acc[i][4 * g + 2] + o[2], acc[i][4 * g + 3] + o[3]}, sb + (i * 4 + g) * 1024); }
    } else {
#pragma unroll
      for (int i = 0; i < 2; ++i)
#pragma unroll
        for (int g = 0; g < 4; ++g) { const f32x4 o = *(const LAS f32x4*)(xr + (i * 4 + g) * 1024);
          gstore_sc1_f(vl16, (f32x4){acc[3 + i][4 * g] + o[0], acc[3 + i][4 * g + 1] + o[1], acc[3 + i][4 * g + 2] + o[2], acc[3 + i][4 * g + 3] + o[3]}, sb + ((3 + i) * 4 + g) * 1024); }
    }
    WAIT_V(0); WAIT_L(0); BAR();
    if (tid == 0) __hip_atomic_fetch_add(flags + FL_P3 + 64 * panel, 1u, __ATOMIC_RELAXED, __HIP_MEMORY_SCOPE_AGENT);
  }
  if (r < 27) mlp_prefetch_w<true>(lds0, w1, wt5, wid, lane);
  {
    ldsp tr = lds + 98304;
    LAS float* red = (LAS float*)(lds + 98304 + 10240);
    const int rr = tid & 7, seg = tid >> 3;
    const int r8 = ksp * 4 + ctile;
    const int s = 128 * panel + 8 * r8 + rr, st = s >> 5;
    int n0[3]; unsigned so[3];
    u32x2 xv[3];
#pragma unroll
    for (int i = 0; i < 3; ++i) {
      const int sg = seg + 64 * i; const int sgc = sg < 160 ? sg : 0;
      const int nt = sgc >> 3, g = (sgc >> 1) & 3, hh = sgc & 1;
      n0[i] = 32 * nt + 8 * g + 4 * hh;
      so[i] = (unsigned)((((st * 20 + nt) * 4 + g) * 256 + ((s & 31) + 32 * hh) * 4) * 4);
      xv[i] = *(const u32x2*)(x + (size_t)s * DM + n0[i]);
    }
    if (wid == 0) wait_word(flags + FL_P3 + 64 * panel, 16u, flags + FL_TMO);
    BAR();
    u32x4 sv[3][4];
    const char* sl = ws + WS_SLAB;
#pragma unroll
    for (int i = 0; i < 3; ++i)
#pragma unroll
      for (int k = 0; k < 4; ++k) GLOAD1_SC1(sv[i][k], so[i], sl + (size_t)k * (64 * 20 * 4 * 1024), 0);
    WAITV_TIE4(0, sv[0]); WAITV_TIE4(0, sv[1]); WAITV_TIE4(0, sv[2]);
    float hv[3][4];
#pragma unroll
    for (int i = 0; i < 3; ++i) {
      const bool ok = seg + 64 * i < 160;
      float a[4];
#pragma unroll
      for (int e = 0; e < 4; ++e) a[e] = (__uint_as_float(sv[i][0][e]) + __uint_as_float(sv[i][1][e])) + (__uint_as_float(sv[i][2][e]) + __uint_as_float(sv[i][3][e]));
      const unsigned p0 = cvtpk(a[0] + bflo(xv[i][0]), a[1] + bfhi(xv[i][0])), p1 = cvtpk(a[2] + bflo(xv[i][1]), a[3] + bfhi(xv[i][1]));
      if (ok) __hip_atomic_store((unsigned long long*)(ws + WS_H + ((size_t)s * DM + n0[i]) * 2), ((unsigned long long)p1 << 32) | p0, __ATOMIC_RELAXED, __HIP_MEMORY_SCOPE_AGENT);
      hv[i][0] = ok ? bflo(p0) : 0.f; hv[i][1] = ok ? bfhi(p0) : 0.f; hv[i][2] = ok ? bflo(p1) : 0.f; hv[i][3] = ok ? bfhi(p1) : 0.f;
    }
    float sum = 0.f;
#pragma unroll
    for (int i = 0; i < 3; ++i) sum += (hv[i][0] + hv[i][1]) + (hv[i][2] + hv[i][3]);
    sum += __shfl_xor(sum, 8); sum += __shfl_xor(sum, 16); sum += __shfl_xor(sum, 32);
    if (lane < 8) red[wid * 8 + lane] = sum;
    WAIT_L(0); BAR();
    float tot = 0.f;
#pragma unroll
    for (int w = 0; w < 8; ++w) tot += red[w * 8 + rr];
    const float mean = tot * (1.0f / DM);
    float ss = 0.f;
#pragma unroll
    for (int i = 0; i < 3; ++i) {
      if (seg + 64 * i < 160) {
#pragma unroll
        for (int e = 0; e < 4; ++e) { const float d = hv[i][e] - mean; ss += d * d; }
      }
    }
    ss += __shfl_xor(ss, 8); ss += __shfl_xor(ss, 16); ss += __shfl_xor(ss, 32);
    if (lane < 8) red[64 + wid * 8 + lane] = ss;
    WAIT_L(0); BAR();
    float tv = 0.f;
#pragma unroll
    for (int w = 0; w < 8; ++w) tv += red[64 + w * 8 + rr];
    const float rstd = 1.0f / sqrtf(tv * (1.0f / DM) + LN_EPS);
#pragma unroll
    for (int i = 0; i < 3; ++i) {
      if (seg + 64 * i < 160) {
        const unsigned p0 = cvtpk((hv[i][0] - mean) * rstd, (hv[i][1] - mean) * rstd), p1 = cvtpk((hv[i][2] - mean) * rstd, (hv[i][3] - mean) * rstd);
        LAS u16* t16 = (LAS u16*)tr;
        t16[(n0[i] + 0) * 8 + rr] = (u16)(p0 & 0xffff); t16[(n0[i] + 1) * 8 + rr] = (u16)(p0 >> 16);
        t16[(n0[i] + 2) * 8 + rr] = (u16)(p1 & 0xffff); t16[(n0[i] + 3) * 8 + rr] = (u16)(p1 >> 16);
      }
    }
    WAIT_L(0); BAR();
    const char* hdst = ws + WS_HNF + ((size_t)st * 40 * 64 + ((s >> 3) & 3) * 16) * 16;
    { const int d = tid; const u32x4 tv4 = *(const LAS u32x4*)(tr + d * 16); gstore_sc1_u((unsigned)(((d >> 4) * 64 + (d & 15)) * 16), tv4, hdst); }
    if (tid < 128) { const int d = 512 + tid; const u32x4 tv4 = *(const LAS u32x4*)(tr + d * 16); gstore_sc1_u((unsigned)(((d >> 4) * 64 + (d & 15)) * 16), tv4, hdst); }
    WAIT_V(0); WAIT_L(0); BAR();
    if (tid == 0) __hip_atomic_fetch_add(flags + FL_H + 64 * (b & 7), 1u, __ATOMIC_RELAXED, __HIP_MEMORY_SCOPE_AGENT);
  }
  if (r < 27) {
    if (wid == 0) {
      for (unsigned spins = 0;; ++spins) {
        const unsigned v = __hip_atomic_load(flags + FL_H + 64 * (lane & 7), __ATOMIC_RELAXED, __HIP_MEMORY_SCOPE_AGENT);
        if (__all(v >= 32u)) break;
        if (spins > (1u << 16)) { __hip_atomic_store(flags + FL_TMO, 1u, __ATOMIC_RELAXED, __HIP_MEMORY_SCOPE_AGENT); break; }
        __builtin_amdgcn_s_sleep(1);
      }
    }
    BAR();
    f32x4 acc[5];
    mlp_main<true>(smem, w1, ws + WS_HNF, wt5, dg5, acc);
    for (int it = tid; it < 640; it += 512) {
      float v[8];
      const int d15 = it & 15, gp = (it >> 4) & 3, fr = it >> 6, dt = fr % 5, Tp = fr / 5;
      const int c2 = 2 * Tp + (gp >> 1), gD = 2 * (gp & 1);
#pragma unroll
      for (int hf = 0; hf < 2; ++hf) {
        const f32x4 a = *(const LAS f32x4*)(lds + ((0 + c2) * 5 + dt) * 1024 + ((gD + hf) * 16 + d15) * 16);
        const f32x4 c = *(const LAS f32x4*)(lds + ((4 + c2) * 5 + dt) * 1024 + ((gD + hf) * 16 + d15) * 16);
#pragma unroll
        for (int e = 0; e < 4; ++e) v[4 * hf + e] = a[e] + c[e];
      }
      const int f0 = 64 * wt5 + 32 * Tp + 8 * gp;
      const u32x4 bv = *(const u32x4*)(b1 + f0);
      u32x4 o;
#pragma unroll
      for (int e = 0; e < 4; ++e) {
        float z[2] = {v[2 * e] + bflo(bv[e]), v[2 * e + 1] + bfhi(bv[e])};
#pragma unroll
        for (int k = 0; k < 2; ++k) {
          const float xx = z[k];
          const float a = xx * (1.0f + 0.044715f * xx * xx) * (-2.0f * 0.7978845608028654f * 1.4426950408889634f);
          z[k] = xx * __builtin_amdgcn_rcpf(1.0f + __builtin_amdgcn_exp2f(a));
        }
        o[e] = cvtpk(z[0], z[1]);
      }
      gstore_sc1_u((unsigned)((((2 * wt5 + Tp) * 40 + 5 * dg5 + dt) * 64 + gp * 16 + d15) * 16), o, ws + WS_UF);
    }
    WAIT_V(0); WAIT_L(0); BAR();
    if (tid == 0) __hip_atomic_fetch_add(flags + FL_U + 64 * dg5, 1u, __ATOMIC_RELAXED, __HIP_MEMORY_SCOPE_AGENT);
  }
  mlp_prefetch_w<false>(lds0, w2, wt6, wid, lane);
  if (wid == 0) wait_word(flags + FL_U + 64 * dg6, 27u, flags + FL_TMO);
  BAR();
  {
    f32x4 acc[5];
    mlp_main<false>(smem, w2, ws + WS_UF, wt6, dg6, acc);
    for (int it = tid; it < 640; it += 512) {
      float v[8];
      const int sl = it / 10, c = it % 10, dt = c >> 1, gD = 2 * (c & 1), c2 = sl >> 4, s15 = sl & 15;
#pragma unroll
      for (int hf = 0; hf < 2; ++hf) {
        const f32x4 a = *(const LAS f32x4*)(lds + ((0 + c2) * 5 + dt) * 1024 + ((gD + hf) * 16 + s15) * 16);
        const f32x4 cc = *(const LAS f32x4*)(lds + ((4 + c2) * 5 + dt) * 1024 + ((gD + hf) * 16 + s15) * 16);
#pragma unroll
        for (int e = 0; e < 4; ++e) v[4 * hf + e] = a[e] + cc[e];
      }
      const int srow = 64 * wt6 + sl;
      const float bb = bflo((unsigned)b2[srow]);
      const size_t o = (size_t)srow * DM + 80 * dg6 + 8 * c;
      u32x4 hv;
      GLOAD1_SC1(hv, (unsigned)(o * 2), ws + WS_H, 0);
      asm volatile("s_waitcnt vmcnt(0)" : "+v"(hv) :: "memory");
      u32x4 ov;
#pragma unroll
      for (int e = 0; e < 4; ++e) ov[e] = cvtpk(v[2 * e] + bb + bflo(hv[e]), v[2 * e + 1] + bb + bfhi(hv[e]));
      *(u32x4*)(out + o) = ov;
    }
  }
}

extern "C" void kernel_launch(void* const* d_in, const int* in_sizes, int n_in, void* d_out, int out_size, void* d_ws, size_t ws_size,
                              hipStream_t stream) {
  const u16* x = (const u16*)d_in[0];
  const u16* ckv = (const u16*)d_in[1];
  const u16* wuk = (const u16*)d_in[3];
  const u16* wuv = (const u16*)d_in[4];
  char* ws = (char*)d_ws;
  const u16* wq = (const u16*)d_in[2];
  const u16* wo = (const u16*)d_in[5];
  const u16* w1 = (const u16*)d_in[7];
  const u16* b1 = (const u16*)d_in[8];
  const u16* w2 = (const u16*)d_in[9];
  const u16* b2 = (const u16*)d_in[10];
  (void)hipFuncSetAttribute((const void*)kF1, hipFuncAttributeMaxDynamicSharedMemorySize, F1_LDS);
  (void)hipFuncSetAttribute((const void*)kT, hipFuncAttributeMaxDynamicSharedMemorySize, T_LDS);
  hipLaunchKernelGGL(k0_ln, dim3(256), dim3(512), 0, stream, x, (u16*)(ws + WS_XNF), (unsigned*)(ws + WS_FLAGS));
  hipLaunchKernelGGL(kF1, dim3(256), dim3(512), F1_LDS, stream, ckv, wuk, wuv, wq, ws);
  hipLaunchKernelGGL(kT, dim3(256), dim3(512), T_LDS, stream, x, wo, w1, b1, w2, b2, ws, (u16*)d_out);
}
```

```cpp
#include <hip/hip_runtime.h>
#include <stdint.h>

#define LAS __attribute__((address_space(3)))
#define DI __device__ __forceinline__
typedef unsigned short u16;
typedef short bf16x8 __attribute__((ext_vector_type(8)));
typedef short s16x4 __attribute__((ext_vector_type(4)));
typedef float f32x2 __attribute__((ext_vector_type(2)));
typedef float f32x4 __attribute__((ext_vector_type(4)));
typedef float f32x16 __attribute__((ext_vector_type(16)));
typedef unsigned u32x2 __attribute__((ext_vector_type(2)));
typedef unsigned u32x4 __attribute__((ext_vector_type(4)));
typedef __bf16 bf16x2_t __attribute__((ext_vector_type(2)));
typedef LAS unsigned char* ldsp;
typedef LAS const unsigned char* ldscp;

constexpr int SEQ = 2048, DM = 640, DC = 512, NH = 32, HD = 128, FF = 1728;
constexpr float LN_EPS = 1e-5f;

constexpr size_t WS_FLAGS = 0;
constexpr size_t WS_XNF = 131072;
constexpr size_t WS_KF = WS_XNF + (size_t)SEQ * DM * 2;
constexpr size_t WS_VF = WS_KF + (size_t)NH * SEQ * HD * 2;
constexpr size_t WS_CTX = WS_VF + (size_t)NH * SEQ * HD * 2;
constexpr size_t WS_SLAB = WS_CTX + (size_t)NH * SEQ * HD * 2;
constexpr size_t WS_H = WS_SLAB + (size_t)4 * SEQ * DM * 4;
constexpr size_t WS_HNF = WS_H + (size_t)SEQ * DM * 2;
constexpr size_t WS_UF = WS_HNF + (size_t)SEQ * DM * 2;
constexpr size_t WS_QF = WS_UF + (size_t)DM * FF * 2;
constexpr size_t WS_PF = WS_QF + (size_t)NH * SEQ * HD * 2;
constexpr size_t WS_PS = WS_PF + (size_t)NH * SEQ * HD * 2;
constexpr size_t WS_END = WS_PS + (size_t)NH * 8 * 8 * 64 * 8;
constexpr int FL_TMO = 0;
constexpr int FL_KV = 24576;
constexpr int FL_Q = 4096;
constexpr int FL_P = 4096 + 8192;
constexpr int FL_WORDS = 32768;

#define SBAR() __builtin_amdgcn_sched_barrier(0)
#define BAR() asm volatile("s_barrier" ::: "memory")
#define WAIT_V(n) asm volatile("s_waitcnt vmcnt(" #n ")" ::: "memory")
#define WAIT_L(n) asm volatile("s_waitcnt lgkmcnt(" #n ")" ::: "memory")
#define MFMA32(a, b, c) __builtin_amdgcn_mfma_f32_32x32x16_bf16((a), (b), (c), 0, 0, 0)
#define MFMA16(a, b, c) __builtin_amdgcn_mfma_f32_16x16x32_bf16((a), (b), (c), 0, 0, 0)

DI unsigned cvtpk(float lo, float hi) { f32x2 v = {lo, hi}; bf16x2_t b = __builtin_convertvector(v, bf16x2_t); return __builtin_bit_cast(unsigned, b); }
DI float bflo(unsigned u) { return __uint_as_float(u << 16); }
DI float bfhi(unsigned u) { return __uint_as_float(u & 0xffff0000u); }
DI unsigned rfl(unsigned x) { return __builtin_amdgcn_readfirstlane(x); }
DI void glds16(unsigned voff, const void* sbase, unsigned lds_dst) {
  unsigned keep;
  asm volatile("s_mov_b32 %0, m0\n\ts_mov_b32 m0, %3\n\ts_nop 2\n\tglobal_load_lds_dwordx4 %1, %2\n\ts_mov_b32 m0, %0"
               : "=&s"(keep) : "v"(voff), "s"(sbase), "s"(lds_dst) : "memory");
}
DI void glds16x4(unsigned voff, const void* sbase, unsigned lds_dst) {
  unsigned keep;
  asm volatile("s_mov_b32 %0, m0\n\ts_mov_b32 m0, %3\n\ts_nop 2\n\tglobal_load_lds_dwordx4 %1, %2\n\tglobal_load_lds_dwordx4 %1, %2 offset:1024\n\t"
               "global_load_lds_dwordx4 %1, %2 offset:2048\n\tglobal_load_lds_dwordx4 %1, %2 offset:3072\n\ts_mov_b32 m0, %0"
               : "=&s"(keep) : "v"(voff), "s"(sbase), "s"(lds_dst) : "memory");
}
typedef short v4i16_t __attribute__((ext_vector_type(4)));
DI s16x4 vtr(ldscp p) { return __builtin_bit_cast(s16x4, __builtin_amdgcn_ds_read_tr16_b64_v4i16((LAS v4i16_t*)p)); }
DI bf16x8 cat8(s16x4 lo, s16x4 hi) { return __builtin_shufflevector(lo, hi, 0, 1, 2, 3, 4, 5, 6, 7); }
DI bf16x8 ldsr128(ldscp p) { return *(const LAS bf16x8*)p; }
DI bf16x8 pack8(const f32x16& x, int s) {
  u32x4 w = {cvtpk(x[8 * s], x[8 * s + 1]), cvtpk(x[8 * s + 2], x[8 * s + 3]), cvtpk(x[8 * s + 4], x[8 * s + 5]), cvtpk(x[8 * s + 6], x[8 * s + 7])};
  return __builtin_bit_cast(bf16x8, w);
}
DI float wave_sum(float v) {
#pragma unroll
  for (int o = 32; o >= 1; o >>= 1) v += __shfl_xor(v, o);
  return v;
}

__global__ __launch_bounds__(512) void k0_ln(const u16* __restrict__ x, u16* __restrict__ xnf, unsigned* __restrict__ flags) {
  const int w = threadIdx.x >> 6, lane = threadIdx.x & 63;
  const int q = blockIdx.x * 8 + w;
  if (threadIdx.x < 128) flags[blockIdx.x * 128 + threadIdx.x] = 0u;
  const u32x4* row = (const u32x4*)(x + (size_t)q * DM);
  u32x4 c0 = row[lane];
  u32x4 c1 = {0u, 0u, 0u, 0u};
  if (lane < 16) c1 = row[64 + lane];
  float v[16];
#pragma unroll
  for (int i = 0; i < 4; ++i) { v[2 * i] = bflo(c0[i]); v[2 * i + 1] = bfhi(c0[i]); v[8 + 2 * i] = bflo(c1[i]); v[8 + 2 * i + 1] = bfhi(c1[i]); }
  float s = 0.f;
#pragma unroll
  for (int i = 0; i < 16; ++i) s += v[i];
  const float mean = wave_sum(s) * (1.0f / DM);
  float ss = 0.f;
#pragma unroll
  for (int i = 0; i < 8; ++i) { const float d = v[i] - mean; ss += d * d; }
  if (lane < 16) {
#pragma unroll
    for (int i = 8; i < 16; ++i) { const float d = v[i] - mean; ss += d * d; }
  }
  const float rstd = 1.0f / sqrtf(wave_sum(ss) * (1.0f / DM) + LN_EPS);
  u32x4 o0, o1;
#pragma unroll
  for (int i = 0; i < 4; ++i) { o0[i] = cvtpk((v[2 * i] - mean) * rstd, (v[2 * i + 1] - mean) * rstd); o1[i] = cvtpk((v[8 + 2 * i] - mean) * rstd, (v[8 + 2 * i + 1] - mean) * rstd); }
  u32x4* dst = (u32x4*)xnf + (size_t)(q >> 5) * 40 * 64 + (q & 31);
  dst[(lane >> 1) * 64 + (lane & 1) * 32] = o0;
  if (lane < 16) dst[((64 + lane) >> 1) * 64 + (lane & 1) * 32] = o1;
}

constexpr int F1_LDS = 163840;
constexpr int L_K = 0, L_V = 65536, L_Q = 98304;
constexpr float QSCALE = 0.08838834764831845f * 1.4426950408889634f;
constexpr float THR = 16.f;
#define PIN(x) asm volatile("" : "+v"(x))
#define EX2(v) __builtin_amdgcn_exp2f(v)
#define MEMFENCE() asm volatile("" ::: "memory")
DI float max3f(float a, float b, float c) { return __builtin_fmaxf(__builtin_fmaxf(a, b), c); }
DI float xhalf_max(float m) { auto rr = __builtin_amdgcn_permlane32_swap(__float_as_uint(m), __float_as_uint(m), false, false); return __builtin_fmaxf(__uint_as_float(rr[0]), __uint_as_float(rr[1])); }
DI float xhalf_sum(float m) { auto rr = __builtin_amdgcn_permlane32_swap(__float_as_uint(m), __float_as_uint(m), false, false); return __uint_as_float(rr[0]) + __uint_as_float(rr[1]); }
DI float max16(const f32x16& p) {
  float a = max3f(p[0], p[1], p[2]), b = max3f(p[3], p[4], p[5]);
  a = max3f(a, p[6], p[7]); b = max3f(b, p[8], p[9]); a = max3f(a, p[10], p[11]); b = max3f(b, p[12], p[13]); a = max3f(a, p[14], p[15]);
  return __builtin_fmaxf(a, b);
}
DI void cmask(f32x16& p0, f32x16& p1, int dq) {
#pragma unroll
  for (int r = 0; r < 16; ++r) { const int c = (r & 3) + 8 * (r >> 2); if (c > dq) p0[r] = -__builtin_inff(); if (c + 32 > dq) p1[r] = -__builtin_inff(); }
}
#define GLOAD4(d, voff, sbase) asm volatile("s_nop 4\n\tglobal_load_dwordx4 %0, %4, %5\n\tglobal_load_dwordx4 %1, %4, %5 offset:1024\n\tglobal_load_dwordx4 %2, %4, %5 offset:2048\n\tglobal_load_dwordx4 %3, %4, %5 offset:3072" \
    : "=&v"(d[0]), "=&v"(d[1]), "=&v"(d[2]), "=&v"(d[3]) : "v"(voff), "s"(sbase) : "memory")
#define GLOAD1(d, voff, sbase, OFF) asm volatile("s_nop 4\n\tglobal_load_dwordx4 %0, %1, %2 offset:" #OFF : "=&v"(d) : "v"(voff), "s"(sbase) : "memory")
#define GLOAD1_SC1(d, voff, sbase, OFF) asm volatile("s_nop 4\n\tglobal_load_dwordx4 %0, %1, %2 offset:" #OFF " sc1" : "=&v"(d) : "v"(voff), "s"(sbase) : "memory")
#define WAITV_TIE4(n, d) asm volatile("s_waitcnt vmcnt(" #n ")" : "+v"(d[0]), "+v"(d[1]), "+v"(d[2]), "+v"(d[3]) :: "memory")
DI void gstore_sc1(unsigned voff, const bf16x8& v, const void* sbase) {
  asm volatile("s_nop 4\n\tglobal_store_dwordx4 %0, %1, %2 sc1\n\ts_nop 1" :: "v"(voff), "v"(v), "s"(sbase) : "memory");
}
template <int OFFS> DI void gstore_sc1_c(unsigned voff, const bf16x8& v, const void* sbase) {
  unsigned t;
  asm volatile("v_add_u32 %0, %4, %1\n\ts_nop 3\n\tglobal_store_dwordx4 %0, %2, %3 sc1\n\ts_nop 1" : "=&v"(t) : "v"(voff), "v"(v), "s"(sbase), "i"(OFFS) : "memory");
}
template <int OFFS> DI void glds16_c(unsigned voff, const void* sbase, unsigned lds_dst) {
  unsigned keep, t;
  asm volatile("s_mov_b32 %0, m0\n\ts_mov_b32 m0, %4\n\tv_add_u32 %1, %5, %2\n\ts_nop 1\n\tglobal_load_lds_dwordx4 %1, %3\n\ts_mov_b32 m0, %0"
               : "=&s"(keep), "=&v"(t) : "v"(voff), "s"(sbase), "s"(lds_dst), "i"(OFFS) : "memory");
}
#define GLDS_OFF(voff, sbase, ldst, OFF) do { unsigned keep_; asm volatile("s_mov_b32 %0, m0\n\ts_mov_b32 m0, %3\n\ts_nop 2\n\tglobal_load_lds_dwordx4 %1, %2 offset:" #OFF "\n\ts_mov_b32 m0, %0" \
    : "=&s"(keep_) : "v"(voff), "s"(sbase), "s"(ldst) : "memory"); } while (0)
#define WAITVN(n) asm volatile("s_waitcnt vmcnt(%0)" :: "i"(n) : "memory")
DI const char* upt(const char* p) {
  const unsigned long long v = (unsigned long long)p;
  return (const char*)(((unsigned long long)rfl((unsigned)(v >> 32)) << 32) | rfl((unsigned)v));
}
DI void wait_word(unsigned* word, unsigned want, unsigned* tmo) {
  for (unsigned spins = 0;; ++spins) {
    if ((unsigned)rfl(__hip_atomic_load(word, __ATOMIC_RELAXED, __HIP_MEMORY_SCOPE_AGENT)) >= want) break;
    if (spins > (1u << 16)) { __hip_atomic_store(tmo, 1u, __ATOMIC_RELAXED, __HIP_MEMORY_SCOPE_AGENT); break; }
    __builtin_amdgcn_s_sleep(1);
  }
}
#define ACQUIRE() __builtin_amdgcn_fence(__ATOMIC_ACQUIRE, "agent")

template <int V> struct IC { static constexpr int value = V; };
template <int N, int I = 0, class F> DI void static_for(F&& f) { if constexpr (I < N) { f(IC<I>{}); static_for<N, I + 1>(f); } }
DI int lane_id() { unsigned l; asm volatile("v_mbcnt_lo_u32_b32 %0, -1, 0\n\tv_mbcnt_hi_u32_b32 %0, -1, %0" : "=v"(l)); return (int)l; }
struct PfW { const char* wo; const char* w1; const char* w2; };
template <int GRP>
DI void attn_main(ldsp lds, unsigned lds0, char* __restrict__ ws, unsigned* flags, int h, int j, int wid, int lane, int tid, bf16x8 (&qr)[8], unsigned kvmask, const PfW& pfw) {
  const int l31 = lane & 31, hh = lane >> 5;
  const unsigned vl16 = lane * 16;
  const char* Kh = ws + WS_KF + (size_t)h * 64 * 8192;
  const char* Vh = ws + WS_VF + (size_t)h * 128 * 4096;
  const unsigned vt2 = (unsigned)(wid * 2048 + lane * 16);
  const unsigned dt2 = lds0 + wid * 2048;
  const int n1 = j >= 4 ? 16 : 4 * j + 2;
  const int t2 = j >= 4 ? 4 * (8 - j) - 2 : 16;
  const int jp = 7 - j;
#define TILE(g) ((g) < n1 ? (g) : ((g) < 18 ? t2 + (g) - n1 : 0))
#define KSRC(g) (Kh + (size_t)TILE(g) * 16384)
#define VSRC(g) (Vh + (size_t)TILE(g) * 16384)
#define GLDS_T(src, dst, OFF) do { unsigned keep_; asm volatile("s_mov_b32 %0, m0\n\ts_mov_b32 m0, %3\n\ts_nop 2\n\tglobal_load_lds_dwordx4 %1, %2 offset:" #OFF "\n\ts_mov_b32 m0, %0" \
    : "=&s"(keep_) : "v"(vt2), "s"(src), "s"(dst) : "memory"); } while (0)
#define ENSURE_KV(g) do { if (wid == 0) { const int bk_ = TILE(g) >> 2; if (!((kvmask >> bk_) & 1u)) { wait_word(flags + FL_KV + 16 * (8 * h + bk_), 1u, flags + FL_TMO); \
      const unsigned fv_ = __hip_atomic_load(flags + FL_KV + 16 * (8 * h + (lane & 7)), __ATOMIC_RELAXED, __HIP_MEMORY_SCOPE_AGENT); \
      kvmask = ((unsigned)__builtin_amdgcn_ballot_w64(fv_ != 0u) & 0xffu) | (1u << bk_); ACQUIRE(); WAIT_V(0); } } } while (0)
  const int rdoff = lane * 16;
  bf16x8 kq[4], vq[4], pw[4];
  f32x16 C0, C1, o[4], negm;
  float mhat, lsum;
  const f32x16 z = {};
  unsigned pfd = 0;
#pragma unroll
  for (int i = 0; i < 4; ++i) kq[i] = ldsr128(lds + L_K + rdoff + i * 1024);
  {
    ldscp kb = lds + L_K + rdoff;
#pragma unroll
    for (int u = 0; u < 16; ++u) {
      if (u < 8) C0 = MFMA32(kq[u & 3], qr[u & 7], u == 0 ? z : C0); else C1 = MFMA32(kq[u & 3], qr[u & 7], u == 8 ? z : C1);
      if (u + 4 < 16) kq[u & 3] = ldsr128(kb + (u + 4) * 1024);
      if (GRP == 1 && u == 11) {
        SBAR(); WAIT_V(0); WAIT_L(0); BAR();
        GLDS_T(VSRC(2), dt2 + L_V + 32768, 0); GLDS_T(VSRC(2), dt2 + L_V + 32768, 1024);
        GLDS_T(KSRC(4), dt2 + L_K, 0);
      }
      SBAR();
    }
    if (j == 0) cmask(C0, C1, 32 * wid + l31 - 4 * hh);
    float rm = xhalf_max(__builtin_fmaxf(max16(C0), max16(C1)));
    mhat = __builtin_fmaxf(rm, -1e30f);
#pragma unroll
    for (int r = 0; r < 16; ++r) { C0[r] -= mhat; C1[r] -= mhat; negm[r] = -mhat; }
    PIN(negm);
    if (GRP == 0) {
      ENSURE_KV(2); ENSURE_KV(4);
      SBAR(); WAIT_V(0); WAIT_L(0); BAR();
      if (tid == 0) {
        __hip_atomic_store(flags + FL_Q + 32 * (8 * h + j), 1u, __ATOMIC_RELAXED, __HIP_MEMORY_SCOPE_AGENT);
        if ((j & 3) >= 2) __hip_atomic_store(flags + FL_KV + 16 * (8 * h + j), 1u, __ATOMIC_RELAXED, __HIP_MEMORY_SCOPE_AGENT);
      }
      GLDS_T(VSRC(2), dt2 + L_V + 32768, 0);
    } else {
      GLDS_T(KSRC(4), dt2 + L_K, 1024);
    }
#pragma unroll
    for (int i = 0; i < 4; ++i) kq[i] = ldsr128(lds + L_K + 16384 + rdoff + i * 1024);
    float s0 = 0.f;
#pragma unroll
    for (int r = 0; r < 16; ++r) { C0[r] = EX2(C0[r]); s0 += C0[r]; }
#pragma unroll
    for (int r = 0; r < 8; ++r) C1[r] = EX2(C1[r]);
    lsum = s0;
    pw[0] = pack8(C0, 0); pw[1] = pack8(C0, 1);
#pragma unroll
    for (int d = 0; d < 4; ++d) o[d] = z;
  }
  for (int g = 1; g < 18; ++g) {
    const bool seam = (g == n1);
    const int X = g < n1 ? j : jp;
    const int dd = TILE(g) - 4 * X;
    ldscp kb = lds + L_K + (g & 3) * 16384 + rdoff;
    ldscp vb = lds + L_V + ((g - 1) & 3) * 16384 + rdoff;
    ldscp kn = lds + L_K + ((g + 1) & 3) * 16384 + rdoff;
    const char* vs_n = VSRC(g + 2); const char* ks_n = KSRC(g + 4);
    const char* vs_p = VSRC(g + 1); const char* ks_p = KSRC(g + 3);
    const unsigned vd_n = dt2 + L_V + ((g + 2) & 3) * 16384, kd_n = dt2 + L_K + (g & 3) * 16384;
    const unsigned vd_p = dt2 + L_V + ((g + 1) & 3) * 16384, kd_p = dt2 + L_K + ((g - 1) & 3) * 16384;
    const bool gq = (GRP == 0) || g > 1;
    float lsum1 = 0.f, mhat1 = 0.f;
    if (seam) {
      mhat1 = mhat; mhat = 0.f;
#pragma unroll
      for (int r = 0; r < 16; ++r) negm[r] = 0.f;
      PIN(negm);
    }
    if (g >= 2 && g < 12 && wid == ((g - 2) & 7)) {
      const unsigned lc = (unsigned)(g - 2) * 64u + lane, gid = blockIdx.x * 592u + lc;
      const char* p = gid < 40960u ? pfw.wo + (size_t)gid * 128 : (gid < 96256u ? pfw.w1 + (size_t)(gid - 40960u) * 128 : pfw.w2 + (size_t)(gid - 96256u) * 128);
      if (lc < 592u && gid < 151552u) asm volatile("global_load_dword %0, %1, off" : "+v"(pfd) : "v"(p) : "memory");
    }
    float sa = 0.f, sb = 0.f;
    SBAR();
#define KRD(u) kq[(u) & 3] = ldsr128(kb + ((u) + 4) * 1024)
#define VRD(v) vq[(v) & 3] = ldsr128(vb + (v) * 1024)
#define KPRE(i) kq[i] = ldsr128(kn + (i) * 1024)
#define BARRIER_G() do { if (GRP == 0) { ENSURE_KV(g + 4); if (wid == 0 && g == n1 - 2 && j != 0) wait_word(flags + FL_Q + 32 * (8 * h + jp), 1u, flags + FL_TMO); } \
      if (g < 17) WAIT_V(6); else WAIT_V(0); WAIT_L(0); BAR(); SBAR(); \
      if (GRP == 0 && (g == n1 + 2 || (g == 17 && n1 + 2 > 17)) && tid == 0) \
        __hip_atomic_store(flags + FL_P + 32 * (8 * h + j), 1u, __ATOMIC_RELAXED, __HIP_MEMORY_SCOPE_AGENT); } while (0)
    C0 = MFMA32(kq[0], qr[0], negm); KRD(0); C1[8] = EX2(C1[8]); sa += C1[0]; sb += C1[1]; sa += C1[2]; sb += C1[3]; PIN(C1); PIN(sa); PIN(sb); SBAR();
    C0 = MFMA32(kq[1], qr[1], C0); KRD(1); C1[9] = EX2(C1[9]); sa += C1[4]; sb += C1[5]; sa += C1[6]; sb += C1[7]; PIN(C1); PIN(sa); PIN(sb); SBAR();
    C0 = MFMA32(kq[2], qr[2], C0); KRD(2); if (GRP == 0) GLDS_T(vs_p, vd_p, 1024); else if (gq) GLDS_T(ks_p, kd_p, 1024);
        C1[10] = EX2(C1[10]); pw[2] = pack8(C1, 0); PIN(C1); PIN(pw[2]); SBAR();
    C0 = MFMA32(kq[3], qr[3], C0); KRD(3); C1[11] = EX2(C1[11]); C1[12] = EX2(C1[12]); sa += C1[8]; sb += C1[9]; PIN(C1); PIN(sa); PIN(sb); SBAR();
    C0 = MFMA32(kq[0], qr[4], C0); KRD(4); C1[13] = EX2(C1[13]); C1[14] = EX2(C1[14]); sa += C1[10]; sb += C1[11]; PIN(C1); PIN(sa); PIN(sb); SBAR();
    C0 = MFMA32(kq[1], qr[5], C0); KRD(5); C1[15] = EX2(C1[15]); sa += C1[12]; sb += C1[13]; sa += C1[14]; PIN(C1); PIN(sa); PIN(sb); SBAR();
    C0 = MFMA32(kq[2], qr[6], C0); KRD(6); sb += C1[15]; pw[3] = pack8(C1, 1); PIN(pw[3]); PIN(sb); SBAR();
    C0 = MFMA32(kq[3], qr[7], C0); KRD(7); lsum += sa + sb; PIN(lsum); SBAR();
    C1 = MFMA32(kq[0], qr[0], negm); KRD(8); if (GRP == 0) GLDS_T(ks_p, kd_p, 0); SBAR();
    C1 = MFMA32(kq[1], qr[1], C1); KRD(9); SBAR();
    C1 = MFMA32(kq[2], qr[2], C1); KRD(10); SBAR();
    float mxa, mxb;
    C1 = MFMA32(kq[3], qr[3], C1); KRD(11); mxa = max3f(C0[0], C0[1], C0[2]); mxb = max3f(C0[3], C0[4], C0[5]); PIN(mxa); PIN(mxb); SBAR();
    if (GRP == 1) BARRIER_G();
    C1 = MFMA32(kq[0], qr[4], C1); VRD(0); if (GRP == 1) GLDS_T(vs_n, vd_n, 0); mxa = max3f(mxa, C0[6], C0[7]); mxb = max3f(mxb, C0[8], C0[9]); PIN(mxa); PIN(mxb); SBAR();
    C1 = MFMA32(kq[1], qr[5], C1); VRD(1); mxa = max3f(mxa, C0[10], C0[11]); mxb = max3f(mxb, C0[12], C0[13]); PIN(mxa); PIN(mxb); SBAR();
    C1 = MFMA32(kq[2], qr[6], C1); VRD(2); mxa = max3f(mxa, C0[14], C0[15]); mxa = __builtin_fmaxf(mxa, mxb); PIN(mxa); SBAR();
    C1 = MFMA32(kq[3], qr[7], C1); VRD(3); SBAR();
    if (g == n1 - 1) {
      if (j == 0) wait_word(flags + FL_Q + 32 * (8 * h + jp), 1u, flags + FL_TMO);
      const char* qsrc = ws + WS_QF + (size_t)(h * 64 + 8 * jp + wid) * 8192;
      GLOAD1_SC1(qr[0], vl16, qsrc, 0); GLOAD1_SC1(qr[1], vl16, qsrc, 1024); GLOAD1_SC1(qr[2], vl16, qsrc, 2048); GLOAD1_SC1(qr[3], vl16, qsrc, 3072);
      GLOAD1_SC1(qr[4], vl16, qsrc + 4096, 0); GLOAD1_SC1(qr[5], vl16, qsrc + 4096, 1024); GLOAD1_SC1(qr[6], vl16, qsrc + 4096, 2048); GLOAD1_SC1(qr[7], vl16, qsrc + 4096, 3072);
    }
    SBAR();
    o[0] = MFMA32(vq[0], pw[0], o[0]); VRD(4); if (GRP == 1) GLDS_T(vs_n, vd_n, 1024); SBAR();
    o[1] = MFMA32(vq[1], pw[0], o[1]); VRD(5); SBAR();
    float mxc, mxd;
    o[2] = MFMA32(vq[2], pw[0], o[2]); VRD(6); if (GRP == 0) GLDS_T(ks_p, kd_p, 1024);
        mxc = max3f(C1[0], C1[1], C1[2]); mxd = max3f(C1[3], C1[4], C1[5]); mxc = max3f(mxc, C1[6], C1[7]); mxd = max3f(mxd, C1[8], C1[9]); PIN(mxc); PIN(mxd); SBAR();
    o[3] = MFMA32(vq[3], pw[0], o[3]); VRD(7); mxc = max3f(mxc, C1[10], C1[11]); mxd = max3f(mxd, C1[12], C1[13]); mxc = max3f(mxc, C1[14], C1[15]); mxa = max3f(mxa, mxc, mxd); PIN(mxa); SBAR();
    o[0] = MFMA32(vq[0], pw[1], o[0]); VRD(8);
    float rm = mxa;
    MEMFENCE(); SBAR();
    if (dd >= 0) { cmask(C0, C1, 32 * wid + l31 - 64 * dd - 4 * hh); rm = __builtin_fmaxf(max16(C0), max16(C1)); }
    rm = xhalf_max(rm);
    float fsc = 1.f; bool resc = false;
    if (__builtin_expect(seam || __any(rm > THR), 0)) {
      const float dl = seam ? __builtin_fmaxf(rm, -1e30f) : __builtin_fmaxf(rm, 0.f);
      mhat += dl;
#pragma unroll
      for (int r = 0; r < 16; ++r) { C0[r] -= dl; C1[r] -= dl; negm[r] = -mhat; }
      PIN(negm);
      if (seam) { lsum1 = lsum; lsum = 0.f; }
      else { fsc = EX2(-dl); lsum *= fsc; resc = true; }
    }
    SBAR();
    sa = 0.f; sb = 0.f;
    o[1] = MFMA32(vq[1], pw[1], o[1]); VRD(9); C0[0] = EX2(C0[0]); C0[1] = EX2(C0[1]); C0[2] = EX2(C0[2]); PIN(C0); SBAR();
    o[2] = MFMA32(vq[2], pw[1], o[2]); VRD(10); C0[3] = EX2(C0[3]); C0[4] = EX2(C0[4]); sa += C0[0]; sb += C0[1]; PIN(C0); PIN(sa); PIN(sb); SBAR();
    o[3] = MFMA32(vq[3], pw[1], o[3]); VRD(11); C0[5] = EX2(C0[5]); C0[6] = EX2(C0[6]); sa += C0[2]; sb += C0[3]; PIN(C0); PIN(sa); PIN(sb); SBAR();
    o[0] = MFMA32(vq[0], pw[2], o[0]); VRD(12); if (GRP == 1) GLDS_T(ks_n, kd_n, 0); C0[7] = EX2(C0[7]); C0[8] = EX2(C0[8]); sa += C0[4]; sb += C0[5]; PIN(C0); PIN(sa); PIN(sb); SBAR();
    o[1] = MFMA32(vq[1], pw[2], o[1]); VRD(13); pw[0] = pack8(C0, 0); C0[9] = EX2(C0[9]); PIN(pw[0]); PIN(C0); SBAR();
    o[2] = MFMA32(vq[2], pw[2], o[2]); VRD(14); C0[10] = EX2(C0[10]); C0[11] = EX2(C0[11]); sa += C0[6]; sb += C0[7]; PIN(C0); PIN(sa); PIN(sb); SBAR();
    o[3] = MFMA32(vq[3], pw[2], o[3]); VRD(15); C0[12] = EX2(C0[12]); C0[13] = EX2(C0[13]); sa += C0[8]; sb += C0[9]; PIN(C0); PIN(sa); PIN(sb); SBAR();
    if (GRP == 0) BARRIER_G();
    o[0] = MFMA32(vq[0], pw[3], o[0]); KPRE(0); if (GRP == 0) GLDS_T(vs_n, vd_n, 0); C0[14] = EX2(C0[14]); C0[15] = EX2(C0[15]); sa += C0[10]; sb += C0[11]; PIN(C0); PIN(sa); PIN(sb); SBAR();
    o[1] = MFMA32(vq[1], pw[3], o[1]); KPRE(1); C1[0] = EX2(C1[0]); C1[1] = EX2(C1[1]); sa += C0[12]; sb += C0[13]; PIN(C1); PIN(sa); PIN(sb); SBAR();
    o[2] = MFMA32(vq[2], pw[3], o[2]); KPRE(2); pw[1] = pack8(C0, 1); C1[2] = EX2(C1[2]); C1[3] = EX2(C1[3]); PIN(pw[1]); PIN(C1); SBAR();
    o[3] = MFMA32(vq[3], pw[3], o[3]); KPRE(3); C1[4] = EX2(C1[4]); C1[5] = EX2(C1[5]); sa += C0[14]; sb += C0[15]; PIN(C1); PIN(sa); PIN(sb); SBAR();
    C1[6] = EX2(C1[6]); C1[7] = EX2(C1[7]); lsum += sa + sb; PIN(C1); PIN(lsum);
    if (resc) {
#pragma unroll
      for (int d = 0; d < 4; ++d) o[d] = o[d] * fsc;
    }
    if (seam) {
      const float lt = xhalf_sum(lsum1);
      const char* pst = ws + WS_PF + (size_t)((h * 8 + j) * 8 + wid) * 8192;
      static_for<4>([&](auto dd_) { constexpr int d = decltype(dd_)::value; gstore_sc1_c<(2 * d) * 1024>(vl16, pack8(o[d], 0), pst); gstore_sc1_c<(2 * d + 1) * 1024>(vl16, pack8(o[d], 1), pst); o[d] = z; });
      const unsigned long long sv = ((unsigned long long)__float_as_uint(lt) << 32) | __float_as_uint(mhat1);
      __hip_atomic_store((unsigned long long*)(ws + WS_PS) + (size_t)((h * 8 + j) * 8 + wid) * 64 + lane, sv, __ATOMIC_RELAXED, __HIP_MEMORY_SCOPE_AGENT);
    }
    if (g == n1 - 1) {
      asm volatile("s_waitcnt vmcnt(1)" : "+v"(qr[0]), "+v"(qr[1]), "+v"(qr[2]), "+v"(qr[3]), "+v"(qr[4]), "+v"(qr[5]), "+v"(qr[6]), "+v"(qr[7]) :: "memory");
    }
  }
  asm volatile("" :: "v"(pfd));
  {
    ldscp vb = lds + L_V + (17 & 3) * 16384 + rdoff;
#pragma unroll
    for (int i = 0; i < 4; ++i) vq[i] = ldsr128(vb + i * 1024);
    float sa = 0.f;
#pragma unroll
    for (int r = 8; r < 16; ++r) C1[r] = EX2(C1[r]);
#pragma unroll
    for (int r = 0; r < 16; ++r) sa += C1[r];
    lsum += sa;
    pw[2] = pack8(C1, 0); pw[3] = pack8(C1, 1);
    SBAR();
#pragma unroll
    for (int v = 0; v < 16; ++v) {
      o[v & 3] = MFMA32(vq[v & 3], pw[v >> 2], o[v & 3]);
      if (v + 4 < 16) vq[v & 3] = ldsr128(vb + (v + 4) * 1024);
      SBAR();
    }
    lsum = xhalf_sum(lsum);
    wait_word(flags + FL_P + 32 * (8 * h + jp), 1u, flags + FL_TMO);
    const char* psrc = upt(ws + WS_PF + (size_t)((h * 8 + jp) * 8 + wid) * 8192);
    u32x4 qa[4], qb[4];
    GLOAD1_SC1(qa[0], vl16, psrc, 0); GLOAD1_SC1(qa[1], vl16, psrc, 1024); GLOAD1_SC1(qa[2], vl16, psrc, 2048); GLOAD1_SC1(qa[3], vl16, psrc, 3072);
    const char* psrc2 = upt(psrc + 4096);
    GLOAD1_SC1(qb[0], vl16, psrc2, 0); GLOAD1_SC1(qb[1], vl16, psrc2, 1024); GLOAD1_SC1(qb[2], vl16, psrc2, 2048); GLOAD1_SC1(qb[3], vl16, psrc2, 3072);
    const unsigned long long sv = __hip_atomic_load((const unsigned long long*)(ws + WS_PS) + (size_t)((h * 8 + jp) * 8 + wid) * 64 + lane, __ATOMIC_RELAXED, __HIP_MEMORY_SCOPE_AGENT);
    WAITV_TIE4(0, qa); WAITV_TIE4(0, qb);
    const float om = __uint_as_float((unsigned)sv), ol = __uint_as_float((unsigned)(sv >> 32));
    const float m = __builtin_fmaxf(mhat, om);
    const float f0 = EX2(mhat - m), f1 = EX2(om - m);
    const float inv = __builtin_amdgcn_rcpf(lsum * f0 + ol * f1);
    const float g0 = f0 * inv, g1 = f1 * inv;
    u16* cdst = (u16*)(ws + WS_CTX) + ((size_t)((8 * jp + wid) * 32 + h) * 8 * 64 + lane) * 8;
#pragma unroll
    for (int d = 0; d < 4; ++d)
#pragma unroll
      for (int sp = 0; sp < 2; ++sp) {
        const u32x4 v4 = (2 * d + sp) < 4 ? qa[2 * d + sp] : qb[2 * d + sp - 4];
        u32x4 w;
#pragma unroll
        for (int e = 0; e < 4; ++e) w[e] = cvtpk(o[d][8 * sp + 2 * e] * g0 + bflo(v4[e]) * g1, o[d][8 * sp + 2 * e + 1] * g0 + bfhi(v4[e]) * g1);
        *(u32x4*)(cdst + (size_t)(2 * d + sp) * 512) = w;
      }
  }
#undef KRD
#undef VRD
#undef KPRE
#undef BARRIER_G
#undef ENSURE_KV
#undef GLDS_T
#undef TILE
#undef KSRC
#undef VSRC
}

constexpr int qproj_st0(int cls) { return cls == 0 ? 0 : (cls == 1 ? 2 : (cls == 2 ? 5 : 7)); }
constexpr int qproj_ns(int c, int cls) { const int rel = c - qproj_st0(cls); return (rel < 0 || rel > 2) ? 0 : (rel == 0 ? 6 : 5); }
constexpr int qproj_nl(int c) { return (c + 2 < 10 ? 4 : 0) + (c + 3 < 10 ? 2 : 0); }
constexpr int qproj_allowed(int c, int cls) {
  return c == 0 ? 6 : (c == 1 ? qproj_nl(0) + qproj_ns(0, cls) : qproj_ns(c - 2, cls) + qproj_nl(c - 1) + qproj_ns(c - 1, cls));
}
__global__ __launch_bounds__(512, 2) void kF1(const u16* __restrict__ ckv, const u16* __restrict__ wuk, const u16* __restrict__ wuv, const u16* __restrict__ wq,
                                              const u16* __restrict__ wo, const u16* __restrict__ w1, const u16* __restrict__ w2, char* __restrict__ ws) {
  extern __shared__ __attribute__((aligned(16))) unsigned char smem[];
  const int bidx = blockIdx.x, xcd = bidx & 7, rr_ = bidx >> 3;
  const int h = xcd * 4 + (rr_ >> 3), j = rr_ & 7;
  const int wid = rfl(threadIdx.x >> 6);
  ldsp lds = (ldsp)smem;
  const unsigned lds0 = (unsigned)(uintptr_t)smem;
  unsigned* flags = (unsigned*)(ws + WS_FLAGS);
  bf16x8 kvp[16];
  {
    const int wr = wid >> 2, wc = wid & 3;
    const int lane = threadIdx.x & 63, l31 = lane & 31, hh = lane >> 5;
    unsigned voffC, voffW;
    { const int row = 8 * wid + (lane >> 3); voffC = (unsigned)(row * 1024 + (((lane & 7) ^ ((row >> 1) & 7)) << 4)); }
    { const int row = 8 * (wid >> 1) + ((lane >> 2) & 7); const int chq = 2 * (wid & 1) + (lane >> 5);
      const int ch = 4 * chq + ((lane & 3) ^ ((row >> 2) & 3)); voffW = (unsigned)(row * 8192 + ch * 16); }
    const char* cbase = (const char*)ckv + (size_t)(256 * j) * 1024;
    const char* wkb = (const char*)wuk + h * 256;
    const char* wvb = (const char*)wuv + h * 256;
    const unsigned dstw = lds0 + wid * 1024;
#define CBUF(b, a) (((b) * 2 + (a)) * 16384)
#define WBUF(b, a) (65536 + ((b) * 2 + (a)) * 16384)
#define STAGE_C(b, a, kt) do { const char* s_ = cbase + (a) * 131072 + (kt) * 128; glds16(voffC, s_, dstw + CBUF(b, a)); glds16(voffC, s_ + 65536, dstw + CBUF(b, a) + 8192); } while (0)
#define STAGE_W(b, a, kt) do { const char* s_ = ((a) ? wvb : wkb) + (size_t)(kt) * 64 * 8192; glds16(voffW, s_, dstw + WBUF(b, a)); glds16(voffW, s_ + 32 * 8192, dstw + WBUF(b, a) + 8192); } while (0)
    int aoff[4], boff[2];
#pragma unroll
    for (int s = 0; s < 4; ++s) aoff[s] = 128 * (wr * 64 + l31) + (((2 * s + hh) ^ ((l31 >> 1) & 7)) << 4);
    { const int blk = (lane >> 4) & 1, q = (lane & 15) >> 2, p = lane & 3;
#pragma unroll
      for (int t = 0; t < 2; ++t) boff[t] = 2048 * hh + 512 * wc + 64 * (4 * t + q) + (((2 * blk + (p >> 1)) ^ ((2 * hh + t) & 3)) << 4) + 8 * (p & 1); }
#define LDA(dst, b, a) do { _Pragma("unroll") for (int m = 0; m < 2; ++m) _Pragma("unroll") for (int s = 0; s < 4; ++s) dst[m][s] = ldsr128(lds + CBUF(b, a) + m * 4096 + aoff[s]); } while (0)
#define LDB(dst, b, a) do { _Pragma("unroll") for (int s = 0; s < 4; ++s) dst[s] = cat8(vtr(lds + WBUF(b, a) + s * 4096 + boff[0]), vtr(lds + WBUF(b, a) + s * 4096 + boff[1])); } while (0)
#define MMAK(ai, At, Bt) do { __builtin_amdgcn_s_setprio(1); _Pragma("unroll") for (int s = 0; s < 4; ++s) _Pragma("unroll") for (int m = 0; m < 2; ++m) \
    accK[ai][m] = MFMA32(Bt[s], At[m][s], accK[ai][m]); __builtin_amdgcn_s_setprio(0); } while (0)
#define MMAV(ai, At, Bt) do { __builtin_amdgcn_s_setprio(1); _Pragma("unroll") for (int s = 0; s < 4; ++s) _Pragma("unroll") for (int m = 0; m < 2; ++m) \
    accV[ai][m] = MFMA32(At[m][s], Bt[s], accV[ai][m]); __builtin_amdgcn_s_setprio(0); } while (0)
    f32x16 accK[2][2] = {}, accV[2][2] = {};
    bf16x8 At[2][4], B0[4], B1[4];
    constexpr int nt = DC / 64;
    STAGE_W(0, 0, 0); STAGE_C(0, 0, 0); STAGE_W(0, 1, 0); STAGE_C(0, 1, 0);
    if (wr == 1) BAR();
    WAIT_V(4); BAR();
    STAGE_W(1, 0, 1); STAGE_C(1, 0, 1); STAGE_W(1, 1, 1);
    WAIT_V(6); BAR();
    unsigned pfd = 0;
    if (wid < 3) {
      const int ln = wid * 64 + lane;
      if (ln < 160) asm volatile("global_load_dword %0, %1, off" : "+v"(pfd) : "v"((const char*)wq + (size_t)(80 * j + (ln >> 1)) * 8192 + h * 256 + (ln & 1) * 128) : "memory");
    }
    for (int t = 0; t < nt - 2; t += 2) {
      LDB(B0, 0, 0); SBAR(); LDA(At, 0, 0); STAGE_C(1, 1, t + 1);
      WAIT_L(8); BAR(); WAIT_L(0); MMAK(0, At, B0); BAR(); SBAR();
      LDB(B1, 0, 1); STAGE_W(0, 0, t + 2);
      BAR(); WAIT_L(0); MMAV(0, At, B1); BAR();
      LDA(At, 0, 1); STAGE_C(0, 0, t + 2);
      BAR(); WAIT_L(0); MMAK(1, At, B0); BAR(); SBAR();
      STAGE_W(0, 1, t + 2);
      WAIT_V(6); BAR(); MMAV(1, At, B1); BAR();
      LDB(B0, 1, 0); SBAR(); LDA(At, 1, 0); STAGE_C(0, 1, t + 2);
      WAIT_L(8); BAR(); WAIT_L(0); MMAK(0, At, B0); BAR(); SBAR();
      LDB(B1, 1, 1); STAGE_W(1, 0, t + 3);
      BAR(); WAIT_L(0); MMAV(0, At, B1); BAR();
      LDA(At, 1, 1); STAGE_C(1, 0, t + 3);
      BAR(); WAIT_L(0); MMAK(1, At, B0); BAR(); SBAR();
      STAGE_W(1, 1, t + 3);
      WAIT_V(6); BAR(); MMAV(1, At, B1); BAR();
    }
    { LDB(B0, 0, 0); WAIT_V(0); LDA(At, 0, 0); STAGE_C(1, 1, nt - 1);
      BAR(); WAIT_L(0); MMAK(0, At, B0); BAR();
      LDB(B1, 0, 1); BAR(); WAIT_L(0); MMAV(0, At, B1); BAR();
      LDA(At, 0, 1); WAIT_V(4); BAR(); WAIT_L(0); MMAK(1, At, B0); MMAV(1, At, B1); BAR(); }
    { LDB(B0, 1, 0); LDA(At, 1, 0); WAIT_V(2); BAR(); WAIT_L(0); MMAK(0, At, B0); BAR();
      LDB(B1, 1, 1); WAIT_V(0); BAR(); WAIT_L(0); MMAV(0, At, B1); BAR();
      LDA(At, 1, 1); BAR(); WAIT_L(0); MMAK(1, At, B0); MMAV(1, At, B1); BAR(); }
    if (wr == 0) BAR();
    asm volatile("" :: "v"(pfd));
#pragma unroll
    for (int ai = 0; ai < 2; ++ai)
#pragma unroll
      for (int m = 0; m < 2; ++m)
#pragma unroll
        for (int sp = 0; sp < 2; ++sp) {
          kvp[((ai * 2 + m) * 2 + sp) * 2] = pack8(accK[ai][m], sp);
          kvp[((ai * 2 + m) * 2 + sp) * 2 + 1] = pack8(accV[ai][m], sp);
          PIN(kvp[((ai * 2 + m) * 2 + sp) * 2]); PIN(kvp[((ai * 2 + m) * 2 + sp) * 2 + 1]);
        }
#undef CBUF
#undef WBUF
#undef STAGE_C
#undef STAGE_W
#undef LDA
#undef LDB
#undef MMAK
#undef MMAV
  }
  SBAR();
  bf16x8 qr[8];
  unsigned kvmask = 0;
  const int lane = lane_id(), tid = wid * 64 + lane;
  const unsigned vl16 = lane * 16;
  {
    const int hh = lane >> 5;
    const char* Kh = ws + WS_KF + (size_t)h * 64 * 8192;
    const char* Vh = ws + WS_VF + (size_t)h * 128 * 4096;
    const unsigned vt2 = (unsigned)(wid * 2048 + lane * 16);
    const unsigned dt2 = lds0 + wid * 2048;
    const int n1 = j >= 4 ? 16 : 4 * j + 2;
#define TILE(g) ((g) < n1 ? (g) : 16 + (g) - n1)
#define DMA_T(src, ldsoff) do { unsigned keep_; asm volatile("s_mov_b32 %0, m0\n\ts_mov_b32 m0, %3\n\ts_nop 2\n\tglobal_load_lds_dwordx4 %1, %2\n\tglobal_load_lds_dwordx4 %1, %2 offset:1024\n\ts_mov_b32 m0, %0" \
    : "=&s"(keep_) : "v"(vt2), "s"(src), "s"(dt2 + (ldsoff)) : "memory"); } while (0)
    const int wr = wid >> 2, wc = wid & 3, cls = j & 3;
    const char* kst = ws + WS_KF + ((size_t)(h * 64 + 8 * j + 2 * wr) * 8 + 2 * wc) * 1024;
    const char* vst = ws + WS_VF + ((size_t)(h * 128 + 2 * (8 * j + 2 * wr)) * 4 + wc) * 1024;
#define KVST(i) gstore_sc1_c<(((i) & 1) ? (((i) >> 3) * 8 + (((i) >> 2) & 1) * 2 + (((i) >> 1) & 1)) * 4096 : (((i) >> 3) * 4 + (((i) >> 2) & 1)) * 8192 + (((i) >> 1) & 1) * 1024)>(vl16, kvp[i], ((i) & 1) ? vst : kst)
    unsigned voffW;
    { const int row = 8 * (wid >> 1) + ((lane >> 2) & 7); const int chq = 2 * (wid & 1) + (lane >> 5);
      const int ch = 4 * chq + ((lane & 3) ^ ((row >> 2) & 3)); voffW = (unsigned)(row * 8192 + ch * 16); }
    const char* wqb = (const char*)wq + h * 256;
    const unsigned dstw = lds0 + L_Q + wid * 1024;
#define STAGE_Q1(c, i) glds16_c<(c) * 64 * 8192 + (i) * 32 * 8192>(voffW, wqb, dstw + ((c) & 3) * 16384 + (i) * 8192)
    const char* xb = ws + WS_XNF + (size_t)(8 * j + wid) * 40 * 1024;
    int boff[2];
    { const int blk = (lane >> 4) & 1, q = (lane & 15) >> 2, p = lane & 3;
#pragma unroll
      for (int t = 0; t < 2; ++t) boff[t] = L_Q + 2048 * hh + 64 * (4 * t + q) + (((2 * blk + (p >> 1)) ^ ((2 * hh + t) & 3)) << 4) + 8 * (p & 1); }
    u32x4 xf[3][4];
    f32x16 acc[4] = {};
    bf16x8 aq[6];
    ldscp bp0 = lds + boff[0], bp1 = lds + boff[1];
#define ARD(u) aq[(u) % 6] = cat8(vtr(bp0 + (((u) >> 4) & 3) * 16384 + (((u) >> 2) & 3) * 4096 + ((u) & 3) * 512), vtr(bp1 + (((u) >> 4) & 3) * 16384 + (((u) >> 2) & 3) * 4096 + ((u) & 3) * 512))
    GLOAD4(xf[0], vl16, xb); STAGE_Q1(0, 0); STAGE_Q1(0, 1);
    STAGE_Q1(1, 0); STAGE_Q1(1, 1);
    GLOAD4(xf[1], vl16, xb + 4096); STAGE_Q1(2, 0); STAGE_Q1(2, 1);
    WAIT_V(8); BAR();
#pragma unroll
    for (int u = 0; u < 5; ++u) ARD(u);
    static_for<10>([&](auto cc_) {
      constexpr int c = decltype(cc_)::value;
#define QW(K) WAITVN(qproj_allowed(c, K))
      if (c == 9 && wid == 0) {
        const unsigned fv = __hip_atomic_load(flags + FL_KV + 16 * (8 * h + (lane & 7)), __ATOMIC_RELAXED, __HIP_MEMORY_SCOPE_AGENT);
        kvmask = (unsigned)__builtin_amdgcn_ballot_w64(fv != 0u) & 0xffu;
        if (!(kvmask & 1u)) { wait_word(flags + FL_KV + 16 * (8 * h), 1u, flags + FL_TMO); kvmask |= 1u; }
        if (j == 0 && !(kvmask & 16u)) { wait_word(flags + FL_KV + 16 * (8 * h + 4), 1u, flags + FL_TMO); kvmask |= 16u; }
        ACQUIRE(); WAIT_V(0);
      }
      if (cls == 0) QW(0); else if (cls == 1) QW(1); else if (cls == 2) QW(2); else QW(3);
      asm volatile("" : "+v"(xf[c % 3][0]), "+v"(xf[c % 3][1]), "+v"(xf[c % 3][2]), "+v"(xf[c % 3][3]) :: "memory");
#undef QW
      SBAR(); BAR();
      if (tid == 0 && ((cls == 0 && c == 5) || (cls == 1 && c == 7)))
        __hip_atomic_store(flags + FL_KV + 16 * (8 * h + j), 1u, __ATOMIC_RELAXED, __HIP_MEMORY_SCOPE_AGENT);
      SBAR();
      const char* xn_ = xb + (c + 2) * 4096;
      static_for<16>([&](auto ii_) {
        constexpr int i = decltype(ii_)::value;
        constexpr int u = 16 * c + i;
        acc[i & 3] = MFMA32(aq[u % 6], __builtin_bit_cast(bf16x8, xf[c % 3][i >> 2]), acc[i & 3]);
        if constexpr (u + 5 < 160) ARD(u + 5);
        if (c + 2 < 10) {
          if (i == 1) GLOAD1(xf[(c + 2) % 3][0], vl16, xn_, 0);
          if (i == 3) GLOAD1(xf[(c + 2) % 3][1], vl16, xn_, 1024);
          if (i == 5) GLOAD1(xf[(c + 2) % 3][2], vl16, xn_, 2048);
          if (i == 7) GLOAD1(xf[(c + 2) % 3][3], vl16, xn_, 3072);
        }
        if constexpr (c + 3 < 10) { if constexpr (i == 8) STAGE_Q1(c + 3, 0); if constexpr (i == 9) STAGE_Q1(c + 3, 1); }
        if constexpr (i >= 10) {
          static_for<4>([&](auto kk_) {
            constexpr int k = decltype(kk_)::value;
            constexpr int rel = c - qproj_st0(k);
            if constexpr (rel >= 0 && rel <= 2) {
              constexpr int cnt = rel == 0 ? 6 : 5, base = rel == 0 ? 0 : (rel == 1 ? 6 : 11);
              if constexpr (i - 10 < cnt) { if (cls == k) KVST(base + i - 10); }
            }
          });
        }
        if (c == 9) {
          if (i == 0) DMA_T(Kh + (size_t)TILE(0) * 16384, L_K);
          if (i == 2) DMA_T(Vh + (size_t)TILE(0) * 16384, L_V);
          if (i == 4) DMA_T(Kh + (size_t)TILE(1) * 16384, L_K + 16384);
          if (i == 6) DMA_T(Vh + (size_t)TILE(1) * 16384, L_V + 16384);
          if (i == 8) DMA_T(Kh + (size_t)TILE(2) * 16384, L_K + 32768);
          if (i == 9) DMA_T(Kh + (size_t)TILE(3) * 16384, L_K + 49152);
        }
        SBAR();
      });
    });
#pragma unroll
    for (int dt = 0; dt < 4; ++dt) { acc[dt] = acc[dt] * QSCALE; qr[2 * dt] = pack8(acc[dt], 0); qr[2 * dt + 1] = pack8(acc[dt], 1); }
    if (cls == 3) WAIT_V(15); else WAIT_V(10);
    BAR();
    { const char* qst = ws + WS_QF + (size_t)(h * 64 + 8 * j + wid) * 8192;
      static_for<8>([&](auto ss_) { constexpr int s = decltype(ss_)::value; gstore_sc1_c<s * 1024>(vl16, qr[s], qst); }); }
#undef KVST
#undef STAGE_Q1
#undef ARD
#undef TILE
#undef DMA_T
  }
  const PfW pfw = {(const char*)wo, (const char*)w1, (const char*)w2};
  if (wid < 4) attn_main<0>(lds, lds0, ws, flags, h, j, wid, lane, tid, qr, kvmask, pfw);
  else attn_main<1>(lds, lds0, ws, flags, h, j, wid, lane, tid, qr, kvmask, pfw);
}

constexpr int T_LDS = 163840;
constexpr int FL_P3 = 20480;
constexpr int FL_H = 22528;
constexpr int FL_U = 23552;
#define GLDS_SC1(voff, sbase, ldst) do { unsigned keep_; asm volatile("s_mov_b32 %0, m0\n\ts_mov_b32 m0, %3\n\ts_nop 2\n\tglobal_load_lds_dwordx4 %1, %2 sc1\n\ts_mov_b32 m0, %0" \
    : "=&s"(keep_) : "v"(voff), "s"(sbase), "s"(ldst) : "memory"); } while (0)
template <int OFFS> DI void gstore_sc1_uc(unsigned voff, const u32x4& v, const void* sbase) {
  unsigned t;
  asm volatile("v_add_u32 %0, %4, %1\n\ts_nop 3\n\tglobal_store_dwordx4 %0, %2, %3 sc1\n\ts_nop 1" : "=&v"(t) : "v"(voff), "v"(v), "s"(sbase), "i"(OFFS) : "memory");
}
DI void gstore_sc1_u(unsigned voff, const u32x4& v, const void* sbase) {
  asm volatile("s_nop 4\n\tglobal_store_dwordx4 %0, %1, %2 sc1\n\ts_nop 1" :: "v"(voff), "v"(v), "s"(sbase) : "memory");
}
template <bool FIRST> struct MlpCfg {
  static constexpr int PITCH = FIRST ? FF * 2 : SEQ * 2;
  static constexpr int NKT = FIRST ? SEQ / 64 : FF / 64;
  static constexpr int NST = (NKT + 1) / 2;
};
template <bool FIRST> DI unsigned mlp_voffw(int wid, int lane) {
  const int row = 8 * wid + (lane >> 3); const int f = ((row >> 1) & 1) | (((row >> 3) & 1) << 1);
  return (unsigned)(row * MlpCfg<FIRST>::PITCH + (((lane & 7) ^ (f << 1)) << 4));
}
template <bool FIRST, int KT0, int NK> DI void mlp_wstage(unsigned lds0, const char* wb, unsigned voffW, int wid, int reg) {
  static_for<NK>([&](auto kk_) {
    constexpr int k = decltype(kk_)::value; constexpr int kt = (KT0 + k) < MlpCfg<FIRST>::NKT ? (KT0 + k) : MlpCfg<FIRST>::NKT - 1;
    glds16_c<kt * 64 * MlpCfg<FIRST>::PITCH>(voffW, wb, lds0 + reg + k * 8192 + wid * 1024);
  });
}
struct MlpW { ldscp p[2]; };
DI MlpW mlp_wptr(ldsp lds, int wid, int lane) {
  const int cp = wid & 1, qq = wid >> 1, g = lane >> 4, q = (lane & 15) >> 2, p = lane & 3; const int f = (q >> 1) | ((g & 1) << 1);
  MlpW w;
#pragma unroll
  for (int e = 0; e < 2; ++e) w.p[e] = lds + (qq >> 1) * 8192 + 128 * (32 * (qq & 1) + 8 * g + q) + (((2 * (2 * cp + e) + (p >> 1)) ^ (f << 1)) << 4) + 8 * (p & 1);
  return w;
}
template <int P0, int NP, int NW> DI void mlp_wread(const MlpW& w, int reg, bf16x8 (&wf)[NW]) {
  static_for<NP>([&](auto kk_) {
    constexpr int k = decltype(kk_)::value;
#pragma unroll
    for (int e = 0; e < 2; ++e) wf[(P0 + k) * 2 + e] = cat8(vtr(w.p[e] + reg + k * 16384), vtr(w.p[e] + reg + k * 16384 + 512));
  });
}
constexpr int mlp_slot(int s) { return (s & 3) * 40960; }
template <bool FIRST> DI void mlp_astage(unsigned lds0, const char* ab, unsigned vl16, int wid, int stg) {
  constexpr int NT = 2 * MlpCfg<FIRST>::NKT;
#pragma unroll
  for (int k = 0; k < 5; ++k) {
    const int i = wid * 5 + k;
    const int T_ = 8 * stg + wid; const int Tc_ = T_ < NT ? T_ : NT - 1;
    GLDS_SC1(vl16, ab + ((size_t)Tc_ * 40 + k) * 1024, lds0 + mlp_slot(stg) + i * 1024);
  }
}
template <bool FIRST, int NW>
DI void mlp_main(ldsp lds, unsigned lds0, const char* __restrict__ ab, int wid, int lane, const bf16x8 (&wf)[NW], f32x4 (&acc)[2][5]) {
  constexpr int NKT = MlpCfg<FIRST>::NKT, NS = (NKT + 3) / 4;
  const int qq = wid >> 1;
  const unsigned vl16 = lane * 16;
  ldscp ap = lds + qq * 5120 + lane * 16;
#pragma unroll
  for (int e = 0; e < 2; ++e)
#pragma unroll
    for (int d = 0; d < 5; ++d) acc[e][d] = (f32x4){0.f, 0.f, 0.f, 0.f};
  mlp_astage<FIRST>(lds0, ab, vl16, wid, 0); mlp_astage<FIRST>(lds0, ab, vl16, wid, 1); mlp_astage<FIRST>(lds0, ab, vl16, wid, 2);
  static_for<NS>([&](auto ss_) {
    constexpr int s = decltype(ss_)::value;
    constexpr int ahead = (NS - 1 - s) < 2 ? (NS - 1 - s) : 2;
    WAITVN(5 * ahead);
    SBAR(); BAR();
    bf16x8 af[2][5];
#pragma unroll
    for (int e2 = 0; e2 < 2; ++e2)
#pragma unroll
      for (int d = 0; d < 5; ++d) af[e2][d] = ldsr128(ap + mlp_slot(s) + e2 * 20480 + d * 1024);
    SBAR();
    if constexpr (s + 3 < NS) mlp_astage<FIRST>(lds0, ab, vl16, wid, s + 3);
    SBAR();
#pragma unroll
    for (int e2 = 0; e2 < 2; ++e2) {
      const bool ok = (4 * s + 2 * e2 + 1 < NKT) || (4 * s + 2 * e2 + (qq >> 1) < NKT);
      if (ok) {
#pragma unroll
        for (int e = 0; e < 2; ++e)
#pragma unroll
          for (int d = 0; d < 5; ++d) acc[e][d] = FIRST ? MFMA16(wf[(2 * s + e2) * 2 + e], af[e2][d], acc[e][d]) : MFMA16(af[e2][d], wf[(2 * s + e2) * 2 + e], acc[e][d]);
      }
    }
    SBAR();
  });
}
DI void mlp_park(ldsp lds, int wid, int lane, const f32x4 (&acc)[2][5]) {
#pragma unroll
  for (int e = 0; e < 2; ++e)
#pragma unroll
    for (int d = 0; d < 5; ++d) *(LAS f32x4*)(lds + ((wid * 2 + e) * 5 + d) * 1024 + lane * 16) = acc[e][d];
  WAIT_L(0); BAR();
}
DI void mlp_gather(ldscp lds, int c2, int dt, int gD, int c15, float (&v)[8]) {
#pragma unroll
  for (int hf = 0; hf < 2; ++hf) {
    f32x4 a = {0.f, 0.f, 0.f, 0.f};
#pragma unroll
    for (int q = 0; q < 4; ++q) a += *(const LAS f32x4*)(lds + ((q * 4 + c2) * 5 + dt) * 1024 + ((gD + hf) * 16 + c15) * 16);
#pragma unroll
    for (int e = 0; e < 4; ++e) v[4 * hf + e] = a[e];
  }
}

__global__ __launch_bounds__(512, 2) void kT(const u16* __restrict__ x, const u16* __restrict__ wo, const u16* __restrict__ w1, const u16* __restrict__ b1,
                                             const u16* __restrict__ w2, const u16* __restrict__ b2, char* __restrict__ ws, u16* __restrict__ out) {
  extern __shared__ __attribute__((aligned(16))) unsigned char smem[];
  const int b = blockIdx.x, xcd = b & 7, r = b >> 3;
  const int wid = rfl(threadIdx.x >> 6);
  ldsp lds = (ldsp)smem;
  const unsigned lds0 = (unsigned)(uintptr_t)smem;
  unsigned* flags = (unsigned*)(ws + WS_FLAGS);
  const int ksp = xcd >> 1, panel = (xcd & 1) * 8 + (r >> 2), ctile = r & 3;
  const int idx5 = xcd * 27 + r;
  const int wt5 = idx5 >> 3, dg5 = idx5 & 7;
  const int wt6 = xcd * 4 + (r >> 3), dg6 = r & 7;
  const bool do5 = r < 27;
  constexpr int P4S = 131072;
  bf16x8 wf[32];
  {
    const int lane = threadIdx.x & 63;
    const unsigned vl16 = lane * 16;
    const int kh = wid >> 2, st = wid & 3, hh = lane >> 5;
    unsigned voff[5];
#pragma unroll
    for (int i = 0; i < 5; ++i) {
      const int id = 2 * (5 * wid + i) + (lane >> 5), half = id / 40, rem = id % 40, rg = rem / 5, c = rem % 5;
      const int row = 64 * half + 8 * rg + ((lane >> 2) & 7);
      voff[i] = (unsigned)(row * 1280 + (32 * c + 8 * ((lane & 3) ^ ((row >> 2) & 3))) * 2);
    }
    const char* wb = (const char*)wo + (size_t)(ksp * 1024) * 1280 + ctile * 320;
    const unsigned dstw = lds0 + wid * 5120;
#define OPIECE(hd, i) glds16_c<(hd) * 128 * 1280>(voff[i], wb, dstw + ((hd) & 3) * 40960 + (i) * 1024)
#define STAGE_O(hd) do { OPIECE(hd, 0); OPIECE(hd, 1); OPIECE(hd, 2); OPIECE(hd, 3); OPIECE(hd, 4); } while (0)
    const char* cb = ws + WS_CTX + ((size_t)((4 * panel + st) * 32 + 8 * ksp) * 8 + 4 * kh) * 1024;
    int boff[2];
    { const int blk = (lane >> 4) & 1, q = (lane & 15) >> 2, p = lane & 3;
#pragma unroll
      for (int t = 0; t < 2; ++t) boff[t] = kh * 20480 + 2560 * t + 64 * (4 * hh + q) + (((2 * blk + (p >> 1)) ^ ((2 * t + hh) & 3)) << 4) + 8 * (p & 1); }
    ldscp bp0 = lds + boff[0], bp1 = lds + boff[1];
    u32x4 xf[3][4];
    f32x16 acc[5] = {};
    bf16x8 aq[6];
#define ARD(U) aq[(U) % 6] = cat8(vtr(bp0 + (((U) / 20) & 3) * 40960 + (((U) % 20) / 5) * 5120 + ((U) % 5) * 512), vtr(bp1 + (((U) / 20) & 3) * 40960 + (((U) % 20) / 5) * 5120 + ((U) % 5) * 512))
    GLOAD4(xf[0], vl16, cb); STAGE_O(0);
    STAGE_O(1);
    GLOAD4(xf[1], vl16, cb + 8192); STAGE_O(2);
    WAIT_V(14); BAR();
#pragma unroll
    for (int U = 0; U < 5; ++U) ARD(U);
    static_for<8>([&](auto hh_) {
      constexpr int hd = decltype(hh_)::value;
      if constexpr (hd <= 5) { WAITV_TIE4(9, xf[hd % 3]); }
      else if constexpr (hd == 6) { WAITV_TIE4(4, xf[hd % 3]); }
      else { WAITV_TIE4(0, xf[hd % 3]); }
      SBAR(); BAR(); SBAR();
      const char* xn_ = cb + (size_t)(hd + 2) * 8192;
      static_for<20>([&](auto uu_) {
        constexpr int u = decltype(uu_)::value;
        constexpr int U = 20 * hd + u;
        acc[u % 5] = MFMA32(aq[U % 6], __builtin_bit_cast(bf16x8, xf[hd % 3][u / 5]), acc[u % 5]);
        if constexpr (U + 5 < 160) ARD(U + 5);
        if constexpr (hd + 2 < 8) {
          if constexpr (u == 1) GLOAD1(xf[(hd + 2) % 3][0], vl16, xn_, 0);
          if constexpr (u == 3) GLOAD1(xf[(hd + 2) % 3][1], vl16, xn_, 1024);
          if constexpr (u == 5) GLOAD1(xf[(hd + 2) % 3][2], vl16, xn_, 2048);
          if constexpr (u == 7) GLOAD1(xf[(hd + 2) % 3][3], vl16, xn_, 3072);
        }
        if constexpr (hd + 3 < 8) {
          if constexpr (u == 9) OPIECE(hd + 3, 0);
          if constexpr (u == 11) OPIECE(hd + 3, 1);
          if constexpr (u == 13) OPIECE(hd + 3, 2);
          if constexpr (u == 15) OPIECE(hd + 3, 3);
          if constexpr (u == 17) OPIECE(hd + 3, 4);
        }
        SBAR();
      });
    });
#undef OPIECE
#undef STAGE_O
#undef ARD
    BAR();
    ldsp xw = lds + (kh == 0 ? 49152 : 0) + st * 12288 + lane * 16;
    ldscp xr = lds + (kh == 0 ? 0 : 49152) + st * 12288 + lane * 16;
    if (kh == 0) {
#pragma unroll
      for (int i = 0; i < 2; ++i)
#pragma unroll
        for (int g = 0; g < 4; ++g) *(LAS f32x4*)(xw + (i * 4 + g) * 1024) = (f32x4){acc[3 + i][4 * g], acc[3 + i][4 * g + 1], acc[3 + i][4 * g + 2], acc[3 + i][4 * g + 3]};
    } else {
#pragma unroll
      for (int i = 0; i < 3; ++i)
#pragma unroll
        for (int g = 0; g < 4; ++g) *(LAS f32x4*)(xw + (i * 4 + g) * 1024) = (f32x4){acc[i][4 * g], acc[i][4 * g + 1], acc[i][4 * g + 2], acc[i][4 * g + 3]};
    }
    WAIT_L(0); BAR();
    const char* sb = ws + WS_SLAB + ((size_t)((ksp * 64 + 4 * panel + st) * 20 + 5 * ctile) * 2) * 1024;
#define SLABST(I, T) static_for<2>([&](auto pp_) { \
          constexpr int sp = decltype(pp_)::value; \
          const f32x4 o0 = *(const LAS f32x4*)(xr + ((I) * 4 + 2 * sp) * 1024), o1 = *(const LAS f32x4*)(xr + ((I) * 4 + 2 * sp + 1) * 1024); \
          const u32x4 w = {cvtpk(acc[T][8 * sp] + o0[0], acc[T][8 * sp + 1] + o0[1]), cvtpk(acc[T][8 * sp + 2] + o0[2], acc[T][8 * sp + 3] + o0[3]), \
                           cvtpk(acc[T][8 * sp + 4] + o1[0], acc[T][8 * sp + 5] + o1[1]), cvtpk(acc[T][8 * sp + 6] + o1[2], acc[T][8 * sp + 7] + o1[3])}; \
          gstore_sc1_uc<((T) * 2 + sp) * 1024>(vl16, w, sb); })
    if (kh == 0) { SLABST(0, 0); SLABST(1, 1); SLABST(2, 2); }
    else { SLABST(0, 3); SLABST(1, 4); }
#undef SLABST
    WAIT_V(0); WAIT_L(0); BAR();
    if (threadIdx.x == 0) __hip_atomic_fetch_add(flags + FL_P3 + 64 * panel, 1u, __ATOMIC_RELAXED, __HIP_MEMORY_SCOPE_AGENT);
  }
  {
    const int lane = lane_id(), tid = wid * 64 + lane;
    const MlpW wp = mlp_wptr(lds, wid, lane);
    const char* w1b = (const char*)w1 + wt5 * 128;
    const unsigned voffW = mlp_voffw<true>(wid, lane);
    ldsp tr = lds + P4S;
    LAS float* red = (LAS float*)(lds + P4S + 10240);
    const int rr = tid & 7, pc = tid >> 3;
    const int r8 = ksp * 4 + ctile;
    const int s = 128 * panel + 8 * r8 + rr, st = s >> 5;
    int n0[2]; unsigned so[2];
    u32x2 xv[2][2];
#pragma unroll
    for (int i = 0; i < 2; ++i) {
      const int pi = pc + 64 * i; const int pcl = pi < 80 ? pi : 0;
      const int nt = pcl >> 2, sp = (pcl >> 1) & 1, hh = pcl & 1;
      n0[i] = 32 * nt + 16 * sp + 4 * hh;
      so[i] = (unsigned)((((st * 20 + nt) * 2 + sp) * 64 + (s & 31) + 32 * hh) * 16);
      xv[i][0] = *(const u32x2*)(x + (size_t)s * DM + n0[i]); xv[i][1] = *(const u32x2*)(x + (size_t)s * DM + n0[i] + 8);
    }
    asm volatile("" : "+v"(xv[0][0]), "+v"(xv[0][1]), "+v"(xv[1][0]), "+v"(xv[1][1]));
    if (do5) mlp_wstage<true, 0, 16>(lds0, w1b, voffW, wid, 0);
    if (wid == 0) wait_word(flags + FL_P3 + 64 * panel, 16u, flags + FL_TMO);
    BAR();
    u32x4 sv[2][4];
    const char* sl = ws + WS_SLAB;
#pragma unroll
    for (int i = 0; i < 2; ++i)
#pragma unroll
      for (int k = 0; k < 4; ++k) GLOAD1_SC1(sv[i][k], so[i], sl + (size_t)k * (64 * 20 * 2 * 1024), 0);
    WAITV_TIE4(0, sv[0]); WAITV_TIE4(0, sv[1]);
    float hv[2][8];
    unsigned pk[2][4];
    float sum = 0.f, ssq = 0.f;
#pragma unroll
    for (int i = 0; i < 2; ++i) {
      const bool ok = pc + 64 * i < 80;
#pragma unroll
      for (int e = 0; e < 4; ++e) {
        const float lo = (bflo(sv[i][0][e]) + bflo(sv[i][1][e])) + (bflo(sv[i][2][e]) + bflo(sv[i][3][e]));
        const float hi = (bfhi(sv[i][0][e]) + bfhi(sv[i][1][e])) + (bfhi(sv[i][2][e]) + bfhi(sv[i][3][e]));
        const unsigned xw_ = xv[i][e >> 1][e & 1];
        pk[i][e] = cvtpk(lo + bflo(xw_), hi + bfhi(xw_));
        hv[i][2 * e] = ok ? bflo(pk[i][e]) : 0.f; hv[i][2 * e + 1] = ok ? bfhi(pk[i][e]) : 0.f;
        sum += hv[i][2 * e] + hv[i][2 * e + 1];
      }
    }
    sum += __shfl_xor(sum, 8); sum += __shfl_xor(sum, 16); sum += __shfl_xor(sum, 32);
    const float cntw = (wid < 2) ? 128.f : 64.f;
    const float mw = sum / cntw;
#pragma unroll
    for (int i = 0; i < 2; ++i) {
      if (pc + 64 * i < 80) {
#pragma unroll
        for (int e = 0; e < 8; ++e) { const float d = hv[i][e] - mw; ssq += d * d; }
      }
    }
    ssq += __shfl_xor(ssq, 8); ssq += __shfl_xor(ssq, 16); ssq += __shfl_xor(ssq, 32);
    if (lane < 8) { red[(wid * 8 + lane) * 2] = mw; red[(wid * 8 + lane) * 2 + 1] = ssq; }
    WAIT_L(0); BAR();
    float mean = 0.f;
#pragma unroll
    for (int w = 0; w < 8; ++w) mean += red[(w * 8 + rr) * 2] * (w < 2 ? 128.f : 64.f);
    mean *= (1.0f / DM);
    float m2 = 0.f;
#pragma unroll
    for (int w = 0; w < 8; ++w) { const float dm = red[(w * 8 + rr) * 2] - mean; m2 += red[(w * 8 + rr) * 2 + 1] + dm * dm * (w < 2 ? 128.f : 64.f); }
    const float rstd = 1.0f / sqrtf(m2 * (1.0f / DM) + LN_EPS);
#pragma unroll
    for (int i = 0; i < 2; ++i) {
      if (pc + 64 * i < 80) {
        LAS u16* t16 = (LAS u16*)tr;
#pragma unroll
        for (int e = 0; e < 4; ++e) {
          const unsigned p = cvtpk((hv[i][2 * e] - mean) * rstd, (hv[i][2 * e + 1] - mean) * rstd);
          const int n = n0[i] + 8 * (e >> 1) + 2 * (e & 1);
          t16[n * 8 + rr] = (u16)(p & 0xffff); t16[(n + 1) * 8 + rr] = (u16)(p >> 16);
        }
      }
    }
    WAIT_L(0); BAR();
    const char* hdst = ws + WS_HNF + ((size_t)st * 40 * 64 + ((s >> 3) & 3) * 16) * 16;
    { const int d = tid; const u32x4 tv4 = *(const LAS u32x4*)(tr + d * 16); gstore_sc1_u((unsigned)(((d >> 4) * 64 + (d & 15)) * 16), tv4, hdst); }
    if (tid < 128) { const int d = 512 + tid; const u32x4 tv4 = *(const LAS u32x4*)(tr + d * 16); gstore_sc1_u((unsigned)(((d >> 4) * 64 + (d & 15)) * 16), tv4, hdst); }
#pragma unroll
    for (int i = 0; i < 2; ++i) {
      if (pc + 64 * i < 80) {
        unsigned long long* hp = (unsigned long long*)(ws + WS_H + ((size_t)s * DM + n0[i]) * 2);
        __hip_atomic_store(hp, ((unsigned long long)pk[i][1] << 32) | pk[i][0], __ATOMIC_RELAXED, __HIP_MEMORY_SCOPE_AGENT);
        __hip_atomic_store(hp + 2, ((unsigned long long)pk[i][3] << 32) | pk[i][2], __ATOMIC_RELAXED, __HIP_MEMORY_SCOPE_AGENT);
      }
    }
    if (do5) mlp_wread<0, 8>(wp, 0, wf);
    WAIT_V(0); WAIT_L(0); BAR();
    if (tid == 0) __hip_atomic_fetch_add(flags + FL_H + 64 * (b & 7), 1u, __ATOMIC_RELAXED, __HIP_MEMORY_SCOPE_AGENT);
    if (do5) {
      mlp_wstage<true, 16, 16>(lds0, w1b, voffW, wid, 0);
      WAIT_V(0); BAR();
      mlp_wread<8, 8>(wp, 0, wf);
    }
  }
  constexpr int RHI = 81920;
  {
    const int lane = lane_id(), tid = wid * 64 + lane;
    const char* w2b = (const char*)w2 + wt6 * 128;
    const unsigned voffW2 = mlp_voffw<false>(wid, lane);
    if (do5) {
      if (wid == 0) {
        for (unsigned spins = 0;; ++spins) {
          const unsigned v = __hip_atomic_load(flags + FL_H + 64 * (lane & 7), __ATOMIC_RELAXED, __HIP_MEMORY_SCOPE_AGENT);
          if (__all(v >= 32u)) break;
          if (spins > (1u << 16)) { __hip_atomic_store(flags + FL_TMO, 1u, __ATOMIC_RELAXED, __HIP_MEMORY_SCOPE_AGENT); break; }
          __builtin_amdgcn_s_sleep(1);
        }
      }
      WAIT_L(0); BAR();
      f32x4 acc[2][5];
      mlp_main<true, 32>(lds, lds0, ws + WS_HNF + (size_t)(5 * dg5) * 1024, wid, lane, wf, acc);
      BAR();
      mlp_park(lds, wid, lane, acc);
      for (int it = tid; it < 640; it += 512) {
        float v[8];
        const int d15 = it & 15, gp = (it >> 4) & 3, fr = it >> 6, dt = fr % 5, Tp = fr / 5;
        mlp_gather(lds, 2 * Tp + (gp >> 1), dt, 2 * (gp & 1), d15, v);
        const int f0 = 64 * wt5 + 32 * Tp + 8 * gp;
        const u32x4 bv = *(const u32x4*)(b1 + f0);
        u32x4 o;
#pragma unroll
        for (int e = 0; e < 4; ++e) {
          float z[2] = {v[2 * e] + bflo(bv[e]), v[2 * e + 1] + bfhi(bv[e])};
#pragma unroll
          for (int k = 0; k < 2; ++k) {
            const float xx = z[k];
            const float a = xx * (1.0f + 0.044715f * xx * xx) * (-2.0f * 0.7978845608028654f * 1.4426950408889634f);
            z[k] = xx * __builtin_amdgcn_rcpf(1.0f + __builtin_amdgcn_exp2f(a));
          }
          o[e] = cvtpk(z[0], z[1]);
        }
        gstore_sc1_u((unsigned)((((2 * wt5 + Tp) * 40 + 5 * dg5 + dt) * 64 + gp * 16 + d15) * 16), o, ws + WS_UF);
      }
      mlp_wstage<false, 0, 10>(lds0, w2b, voffW2, wid, RHI);
      WAIT_V(10); WAIT_L(0); BAR();
      if (tid == 0) __hip_atomic_fetch_add(flags + FL_U + 64 * dg5, 1u, __ATOMIC_RELAXED, __HIP_MEMORY_SCOPE_AGENT);
    } else {
      WAIT_L(0); BAR();
      mlp_wstage<false, 0, 10>(lds0, w2b, voffW2, wid, RHI);
    }
  }
  {
    const int lane = lane_id(), tid = wid * 64 + lane;
    bf16x8 wg[28];
    const MlpW wp = mlp_wptr(lds, wid, lane);
    const char* w2b = (const char*)w2 + wt6 * 128;
    const unsigned voffW2 = mlp_voffw<false>(wid, lane);
    mlp_wstage<false, 10, 10>(lds0, w2b, voffW2, wid, 0);
    WAIT_V(0); BAR();
    mlp_wread<0, 5>(wp, RHI, wg); mlp_wread<5, 5>(wp, 0, wg);
    WAIT_L(0); BAR();
    mlp_wstage<false, 20, 8>(lds0, w2b, voffW2, wid, 0);
    WAIT_V(0); BAR();
    mlp_wread<10, 4>(wp, 0, wg);
    if (wid == 0) wait_word(flags + FL_U + 64 * dg6, 27u, flags + FL_TMO);
    WAIT_L(0); BAR();
    const char* ab = ws + WS_UF + (size_t)(5 * dg6) * 1024;
    f32x4 acc[2][5];
    mlp_main<false, 28>(lds, lds0, ab, wid, lane, wg, acc);
    BAR();
    mlp_park(lds, wid, lane, acc);
    for (int it = tid; it < 640; it += 512) {
      float v[8];
      const int sl = it / 10, c = it % 10;
      const int srow = 64 * wt6 + sl;
      const size_t o = (size_t)srow * DM + 80 * dg6 + 8 * c;
      u32x4 hv;
      GLOAD1_SC1(hv, (unsigned)(o * 2), ws + WS_H, 0);
      mlp_gather(lds, sl >> 4, c >> 1, 2 * (c & 1), sl & 15, v);
      const float bb = bflo((unsigned)b2[srow]);
      asm volatile("s_waitcnt vmcnt(0)" : "+v"(hv) :: "memory");
      u32x4 ov;
#pragma unroll
      for (int e = 0; e < 4; ++e) ov[e] = cvtpk(v[2 * e] + bb + bflo(hv[e]), v[2 * e + 1] + bb + bfhi(hv[e]));
      *(u32x4*)(out + o) = ov;
    }
  }
}

extern "C" void kernel_launch(void* const* d_in, const int* in_sizes, int n_in, void* d_out, int out_size, void* d_ws, size_t ws_size,
                              hipStream_t stream) {
  const u16* x = (const u16*)d_in[0];
  const u16* ckv = (const u16*)d_in[1];
  const u16* wuk = (const u16*)d_in[3];
  const u16* wuv = (const u16*)d_in[4];
  char* ws = (char*)d_ws;
  const u16* wq = (const u16*)d_in[2];
  const u16* wo = (const u16*)d_in[5];
  const u16* w1 = (const u16*)d_in[7];
  const u16* b1 = (const u16*)d_in[8];
  const u16* w2 = (const u16*)d_in[9];
  const u16* b2 = (const u16*)d_in[10];
  (void)hipFuncSetAttribute((const void*)kF1, hipFuncAttributeMaxDynamicSharedMemorySize, F1_LDS);
  (void)hipFuncSetAttribute((const void*)kT, hipFuncAttributeMaxDynamicSharedMemorySize, T_LDS);
  hipLaunchKernelGGL(k0_ln, dim3(256), dim3(512), 0, stream, x, (u16*)(ws + WS_XNF), (unsigned*)(ws + WS_FLAGS));
  hipLaunchKernelGGL(kF1, dim3(256), dim3(512), F1_LDS, stream, ckv, wuk, wuv, wq, wo, w1, w2, ws);
  hipLaunchKernelGGL(kT, dim3(256), dim3(512), T_LDS, stream, x, wo, w1, b1, w2, b2, ws, (u16*)d_out);
}
```

```cpp
#include <hip/hip_runtime.h>
#include <stdint.h>

#define LAS __attribute__((address_space(3)))
#define DI __device__ __forceinline__
typedef unsigned short u16;
typedef short bf16x8 __attribute__((ext_vector_type(8)));
typedef short s16x4 __attribute__((ext_vector_type(4)));
typedef float f32x2 __attribute__((ext_vector_type(2)));
typedef float f32x4 __attribute__((ext_vector_type(4)));
typedef float f32x16 __attribute__((ext_vector_type(16)));
typedef unsigned u32x2 __attribute__((ext_vector_type(2)));
typedef unsigned u32x4 __attribute__((ext_vector_type(4)));
typedef __bf16 bf16x2_t __attribute__((ext_vector_type(2)));
typedef LAS unsigned char* ldsp;
typedef LAS const unsigned char* ldscp;

constexpr int SEQ = 2048, DM = 640, DC = 512, NH = 32, HD = 128, FF = 1728;
constexpr float LN_EPS = 1e-5f;

constexpr size_t WS_FLAGS = 0;
constexpr size_t WS_XNF = 131072;
constexpr size_t WS_KF = WS_XNF + (size_t)SEQ * DM * 2;
constexpr size_t WS_VF = WS_KF + (size_t)NH * SEQ * HD * 2;
constexpr size_t WS_CTX = WS_VF + (size_t)NH * SEQ * HD * 2;
constexpr size_t WS_SLAB = WS_CTX + (size_t)NH * SEQ * HD * 2;
constexpr size_t WS_H = WS_SLAB + (size_t)4 * SEQ * DM * 4;
constexpr size_t WS_HNF = WS_H + (size_t)SEQ * DM * 2;
constexpr size_t WS_UF = WS_HNF + (size_t)SEQ * DM * 2;
constexpr size_t WS_QF = WS_UF + (size_t)DM * FF * 2;
constexpr size_t WS_PF = WS_QF + (size_t)NH * SEQ * HD * 2;
constexpr size_t WS_PS = WS_PF + (size_t)NH * SEQ * HD * 2;
constexpr size_t WS_END = WS_PS + (size_t)NH * 8 * 8 * 64 * 8;
constexpr int FL_TMO = 0;
constexpr int FL_KV = 24576;
constexpr int FL_Q = 4096;
constexpr int FL_P = 4096 + 8192;
constexpr int FL_WORDS = 32768;

#define SBAR() __builtin_amdgcn_sched_barrier(0)
#define BAR() asm volatile("s_barrier" ::: "memory")
#define WAIT_V(n) asm volatile("s_waitcnt vmcnt(" #n ")" ::: "memory")
#define WAIT_L(n) asm volatile("s_waitcnt lgkmcnt(" #n ")" ::: "memory")
#define MFMA32(a, b, c) __builtin_amdgcn_mfma_f32_32x32x16_bf16((a), (b), (c), 0, 0, 0)
#define MFMA16(a, b, c) __builtin_amdgcn_mfma_f32_16x16x32_bf16((a), (b), (c), 0, 0, 0)

DI unsigned cvtpk(float lo, float hi) { f32x2 v = {lo, hi}; bf16x2_t b = __builtin_convertvector(v, bf16x2_t); return __builtin_bit_cast(unsigned, b); }
DI float bflo(unsigned u) { return __uint_as_float(u << 16); }
DI float bfhi(unsigned u) { return __uint_as_float(u & 0xffff0000u); }
DI unsigned rfl(unsigned x) { return __builtin_amdgcn_readfirstlane(x); }
DI void glds16(unsigned voff, const void* sbase, unsigned lds_dst) {
  unsigned keep;
  asm volatile("s_mov_b32 %0, m0\n\ts_mov_b32 m0, %3\n\ts_nop 2\n\tglobal_load_lds_dwordx4 %1, %2\n\ts_mov_b32 m0, %0"
               : "=&s"(keep) : "v"(voff), "s"(sbase), "s"(lds_dst) : "memory");
}
DI void glds16x4(unsigned voff, const void* sbase, unsigned lds_dst) {
  unsigned keep;
  asm volatile("s_mov_b32 %0, m0\n\ts_mov_b32 m0, %3\n\ts_nop 2\n\tglobal_load_lds_dwordx4 %1, %2\n\tglobal_load_lds_dwordx4 %1, %2 offset:1024\n\t"
               "global_load_lds_dwordx4 %1, %2 offset:2048\n\tglobal_load_lds_dwordx4 %1, %2 offset:3072\n\ts_mov_b32 m0, %0"
               : "=&s"(keep) : "v"(voff), "s"(sbase), "s"(lds_dst) : "memory");
}
typedef short v4i16_t __attribute__((ext_vector_type(4)));
DI s16x4 vtr(ldscp p) { return __builtin_bit_cast(s16x4, __builtin_amdgcn_ds_read_tr16_b64_v4i16((LAS v4i16_t*)p)); }
DI bf16x8 cat8(s16x4 lo, s16x4 hi) { return __builtin_shufflevector(lo, hi, 0, 1, 2, 3, 4, 5, 6, 7); }
DI bf16x8 ldsr128(ldscp p) { return *(const LAS bf16x8*)p; }
DI bf16x8 pack8(const f32x16& x, int s) {
  u32x4 w = {cvtpk(x[8 * s], x[8 * s + 1]), cvtpk(x[8 * s + 2], x[8 * s + 3]), cvtpk(x[8 * s + 4], x[8 * s + 5]), cvtpk(x[8 * s + 6], x[8 * s + 7])};
  return __builtin_bit_cast(bf16x8, w);
}
DI float wave_sum(float v) {
#pragma unroll
  for (int o = 32; o >= 1; o >>= 1) v += __shfl_xor(v, o);
  return v;
}

__global__ __launch_bounds__(512) void k0_ln(const u16* __restrict__ x, u16* __restrict__ xnf, unsigned* __restrict__ flags) {
  const int w = threadIdx.x >> 6, lane = threadIdx.x & 63;
  const int q = blockIdx.x * 8 + w;
  if (threadIdx.x < 128) flags[blockIdx.x * 128 + threadIdx.x] = 0u;
  const u32x4* row = (const u32x4*)(x + (size_t)q * DM);
  u32x4 c0 = row[lane];
  u32x4 c1 = {0u, 0u, 0u, 0u};
  if (lane < 16) c1 = row[64 + lane];
  float v[16];
#pragma unroll
  for (int i = 0; i < 4; ++i) { v[2 * i] = bflo(c0[i]); v[2 * i + 1] = bfhi(c0[i]); v[8 + 2 * i] = bflo(c1[i]); v[8 + 2 * i + 1] = bfhi(c1[i]); }
  float s = 0.f;
#pragma unroll
  for (int i = 0; i < 16; ++i) s += v[i];
  const float mean = wave_sum(s) * (1.0f / DM);
  float ss = 0.f;
#pragma unroll
  for (int i = 0; i < 8; ++i) { const float d = v[i] - mean; ss += d * d; }
  if (lane < 16) {
#pragma unroll
    for (int i = 8; i < 16; ++i) { const float d = v[i] - mean; ss += d * d; }
  }
  const float rstd = 1.0f / sqrtf(wave_sum(ss) * (1.0f / DM) + LN_EPS);
  u32x4 o0, o1;
#pragma unroll
  for (int i = 0; i < 4; ++i) { o0[i] = cvtpk((v[2 * i] - mean) * rstd, (v[2 * i + 1] - mean) * rstd); o1[i] = cvtpk((v[8 + 2 * i] - mean) * rstd, (v[8 + 2 * i + 1] - mean) * rstd); }
  u32x4* dst = (u32x4*)xnf + (size_t)(q >> 5) * 40 * 64 + (q & 31);
  dst[(lane >> 1) * 64 + (lane & 1) * 32] = o0;
  if (lane < 16) dst[((64 + lane) >> 1) * 64 + (lane & 1) * 32] = o1;
}

constexpr int F1_LDS = 163840;
constexpr int L_K = 0, L_V = 65536, L_Q = 98304;
constexpr float QSCALE = 0.08838834764831845f * 1.4426950408889634f;
constexpr float THR = 16.f;
#define PIN(x) asm volatile("" : "+v"(x))
#define EX2(v) __builtin_amdgcn_exp2f(v)
#define MEMFENCE() asm volatile("" ::: "memory")
DI float max3f(float a, float b, float c) { return __builtin_fmaxf(__builtin_fmaxf(a, b), c); }
DI float xhalf_max(float m) { auto rr = __builtin_amdgcn_permlane32_swap(__float_as_uint(m), __float_as_uint(m), false, false); return __builtin_fmaxf(__uint_as_float(rr[0]), __uint_as_float(rr[1])); }
DI float xhalf_sum(float m) { auto rr = __builtin_amdgcn_permlane32_swap(__float_as_uint(m), __float_as_uint(m), false, false); return __uint_as_float(rr[0]) + __uint_as_float(rr[1]); }
DI float max16(const f32x16& p) {
  float a = max3f(p[0], p[1], p[2]), b = max3f(p[3], p[4], p[5]);
  a = max3f(a, p[6], p[7]); b = max3f(b, p[8], p[9]); a = max3f(a, p[10], p[11]); b = max3f(b, p[12], p[13]); a = max3f(a, p[14], p[15]);
  return __builtin_fmaxf(a, b);
}
DI void cmask(f32x16& p0, f32x16& p1, int dq) {
#pragma unroll
  for (int r = 0; r < 16; ++r) { const int c = (r & 3) + 8 * (r >> 2); if (c > dq) p0[r] = -__builtin_inff(); if (c + 32 > dq) p1[r] = -__builtin_inff(); }
}
#define GLOAD4(d, voff, sbase) asm volatile("s_nop 4\n\tglobal_load_dwordx4 %0, %4, %5\n\tglobal_load_dwordx4 %1, %4, %5 offset:1024\n\tglobal_load_dwordx4 %2, %4, %5 offset:2048\n\tglobal_load_dwordx4 %3, %4, %5 offset:3072" \
    : "=&v"(d[0]), "=&v"(d[1]), "=&v"(d[2]), "=&v"(d[3]) : "v"(voff), "s"(sbase) : "memory")
#define GLOAD1(d, voff, sbase, OFF) asm volatile("s_nop 4\n\tglobal_load_dwordx4 %0, %1, %2 offset:" #OFF : "=&v"(d) : "v"(voff), "s"(sbase) : "memory")
#define GLOAD1_SC1(d, voff, sbase, OFF) asm volatile("s_nop 4\n\tglobal_load_dwordx4 %0, %1, %2 offset:" #OFF " sc1" : "=&v"(d) : "v"(voff), "s"(sbase) : "memory")
#define WAITV_TIE4(n, d) asm volatile("s_waitcnt vmcnt(" #n ")" : "+v"(d[0]), "+v"(d[1]), "+v"(d[2]), "+v"(d[3]) :: "memory")
DI void gstore_sc1(unsigned voff, const bf16x8& v, const void* sbase) {
  asm volatile("s_nop 4\n\tglobal_store_dwordx4 %0, %1, %2 sc1\n\ts_nop 1" :: "v"(voff), "v"(v), "s"(sbase) : "memory");
}
template <int OFFS> DI void gstore_sc1_c(unsigned voff, const bf16x8& v, const void* sbase) {
  unsigned t;
  asm volatile("v_add_u32 %0, %4, %1\n\ts_nop 3\n\tglobal_store_dwordx4 %0, %2, %3 sc1\n\ts_nop 1" : "=&v"(t) : "v"(voff), "v"(v), "s"(sbase), "i"(OFFS) : "memory");
}
template <int OFFS> DI void glds16_c(unsigned voff, const void* sbase, unsigned lds_dst) {
  unsigned keep, t;
  asm volatile("s_mov_b32 %0, m0\n\ts_mov_b32 m0, %4\n\tv_add_u32 %1, %5, %2\n\ts_nop 1\n\tglobal_load_lds_dwordx4 %1, %3\n\ts_mov_b32 m0, %0"
               : "=&s"(keep), "=&v"(t) : "v"(voff), "s"(sbase), "s"(lds_dst), "i"(OFFS) : "memory");
}
#define GLDS_OFF(voff, sbase, ldst, OFF) do { unsigned keep_; asm volatile("s_mov_b32 %0, m0\n\ts_mov_b32 m0, %3\n\ts_nop 2\n\tglobal_load_lds_dwordx4 %1, %2 offset:" #OFF "\n\ts_mov_b32 m0, %0" \
    : "=&s"(keep_) : "v"(voff), "s"(sbase), "s"(ldst) : "memory"); } while (0)
#define WAITVN(n) asm volatile("s_waitcnt vmcnt(%0)" :: "i"(n) : "memory")
DI const char* upt(const char* p) {
  const unsigned long long v = (unsigned long long)p;
  return (const char*)(((unsigned long long)rfl((unsigned)(v >> 32)) << 32) | rfl((unsigned)v));
}
DI void wait_word(unsigned* word, unsigned want, unsigned* tmo) {
  for (unsigned spins = 0;; ++spins) {
    if ((unsigned)rfl(__hip_atomic_load(word, __ATOMIC_RELAXED, __HIP_MEMORY_SCOPE_AGENT)) >= want) break;
    if (spins > (1u << 16)) { __hip_atomic_store(tmo, 1u, __ATOMIC_RELAXED, __HIP_MEMORY_SCOPE_AGENT); break; }
    __builtin_amdgcn_s_sleep(1);
  }
}
#define ACQUIRE() __builtin_amdgcn_fence(__ATOMIC_ACQUIRE, "agent")

template <int V> struct IC { static constexpr int value = V; };
template <int N, int I = 0, class F> DI void static_for(F&& f) { if constexpr (I < N) { f(IC<I>{}); static_for<N, I + 1>(f); } }
DI int lane_id() { unsigned l; asm volatile("v_mbcnt_lo_u32_b32 %0, -1, 0\n\tv_mbcnt_hi_u32_b32 %0, -1, %0" : "=v"(l)); return (int)l; }
struct PfW { const char* wo; const char* w1; const char* w2; };
template <int GRP>
DI void attn_main(ldsp lds, unsigned lds0, char* __restrict__ ws, unsigned* flags, int h, int j, int wid, int lane, int tid, bf16x8 (&qr)[8], unsigned kvmask, const PfW& pfw) {
  const int l31 = lane & 31, hh = lane >> 5;
  const unsigned vl16 = lane * 16;
  const char* Kh = ws + WS_KF + (size_t)h * 64 * 8192;
  const char* Vh = ws + WS_VF + (size_t)h * 128 * 4096;
  const unsigned vt2 = (unsigned)(wid * 2048 + lane * 16);
  const unsigned dt2 = lds0 + wid * 2048;
  const int n1 = j >= 4 ? 16 : 4 * j + 2;
  const int t2 = j >= 4 ? 4 * (8 - j) - 2 : 16;
  const int jp = 7 - j;
#define TILE(g) ((g) < n1 ? (g) : ((g) < 18 ? t2 + (g) - n1 : 0))
#define KSRC(g) (Kh + (size_t)TILE(g) * 16384)
#define VSRC(g) (Vh + (size_t)TILE(g) * 16384)
#define GLDS_T(src, dst, OFF) do { unsigned keep_; asm volatile("s_mov_b32 %0, m0\n\ts_mov_b32 m0, %3\n\ts_nop 2\n\tglobal_load_lds_dwordx4 %1, %2 offset:" #OFF "\n\ts_mov_b32 m0, %0" \
    : "=&s"(keep_) : "v"(vt2), "s"(src), "s"(dst) : "memory"); } while (0)
#define ENSURE_KV(g) do { if (wid == 0) { const int bk_ = TILE(g) >> 2; if (!((kvmask >> bk_) & 1u)) { wait_word(flags + FL_KV + 16 * (8 * h + bk_), 1u, flags + FL_TMO); \
      const unsigned fv_ = __hip_atomic_load(flags + FL_KV + 16 * (8 * h + (lane & 7)), __ATOMIC_RELAXED, __HIP_MEMORY_SCOPE_AGENT); \
      kvmask = ((unsigned)__builtin_amdgcn_ballot_w64(fv_ != 0u) & 0xffu) | (1u << bk_); ACQUIRE(); WAIT_V(0); } } } while (0)
  const int rdoff = lane * 16;
  bf16x8 kq[4], vq[4], pw[4];
  f32x16 C0, C1, o[4], negm;
  float mhat, lsum;
  const f32x16 z = {};
  unsigned pfd = 0;
#pragma unroll
  for (int i = 0; i < 4; ++i) kq[i] = ldsr128(lds + L_K + rdoff + i * 1024);
  {
    ldscp kb = lds + L_K + rdoff;
#pragma unroll
    for (int u = 0; u < 16; ++u) {
      if (u < 8) C0 = MFMA32(kq[u & 3], qr[u & 7], u == 0 ? z : C0); else C1 = MFMA32(kq[u & 3], qr[u & 7], u == 8 ? z : C1);
      if (u + 4 < 16) kq[u & 3] = ldsr128(kb + (u + 4) * 1024);
      if (GRP == 1 && u == 11) {
        SBAR(); WAIT_V(0); WAIT_L(0); BAR();
        GLDS_T(VSRC(2), dt2 + L_V + 32768, 0); GLDS_T(VSRC(2), dt2 + L_V + 32768, 1024);
        GLDS_T(KSRC(4), dt2 + L_K, 0);
      }
      SBAR();
    }
    if (j == 0) cmask(C0, C1, 32 * wid + l31 - 4 * hh);
    float rm = xhalf_max(__builtin_fmaxf(max16(C0), max16(C1)));
    mhat = __builtin_fmaxf(rm, -1e30f);
#pragma unroll
    for (int r = 0; r < 16; ++r) { C0[r] -= mhat; C1[r] -= mhat; negm[r] = -mhat; }
    PIN(negm);
    if (GRP == 0) {
      ENSURE_KV(2); ENSURE_KV(4);
      SBAR(); WAIT_V(0); WAIT_L(0); BAR();
      if (tid == 0) {
        __hip_atomic_store(flags + FL_Q + 32 * (8 * h + j), 1u, __ATOMIC_RELAXED, __HIP_MEMORY_SCOPE_AGENT);
        if ((j & 3) >= 2) __hip_atomic_store(flags + FL_KV + 16 * (8 * h + j), 1u, __ATOMIC_RELAXED, __HIP_MEMORY_SCOPE_AGENT);
      }
      GLDS_T(VSRC(2), dt2 + L_V + 32768, 0);
    } else {
      GLDS_T(KSRC(4), dt2 + L_K, 1024);
    }
#pragma unroll
    for (int i = 0; i < 4; ++i) kq[i] = ldsr128(lds + L_K + 16384 + rdoff + i * 1024);
    float s0 = 0.f;
#pragma unroll
    for (int r = 0; r < 16; ++r) { C0[r] = EX2(C0[r]); s0 += C0[r]; }
#pragma unroll
    for (int r = 0; r < 8; ++r) C1[r] = EX2(C1[r]);
    lsum = s0;
    pw[0] = pack8(C0, 0); pw[1] = pack8(C0, 1);
#pragma unroll
    for (int d = 0; d < 4; ++d) o[d] = z;
  }
  for (int g = 1; g < 18; ++g) {
    const bool seam = (g == n1);
    const int X = g < n1 ? j : jp;
    const int dd = TILE(g) - 4 * X;
    ldscp kb = lds + L_K + (g & 3) * 16384 + rdoff;
    ldscp vb = lds + L_V + ((g - 1) & 3) * 16384 + rdoff;
    ldscp kn = lds + L_K + ((g + 1) & 3) * 16384 + rdoff;
    const char* vs_n = VSRC(g + 2); const char* ks_n = KSRC(g + 4);
    const char* vs_p = VSRC(g + 1); const char* ks_p = KSRC(g + 3);
    const unsigned vd_n = dt2 + L_V + ((g + 2) & 3) * 16384, kd_n = dt2 + L_K + (g & 3) * 16384;
    const unsigned vd_p = dt2 + L_V + ((g + 1) & 3) * 16384, kd_p = dt2 + L_K + ((g - 1) & 3) * 16384;
    const bool gq = (GRP == 0) || g > 1;
    float lsum1 = 0.f, mhat1 = 0.f;
    if (seam) {
      mhat1 = mhat; mhat = 0.f;
#pragma unroll
      for (int r = 0; r < 16; ++r) negm[r] = 0.f;
      PIN(negm);
    }
    if (g >= 2 && g < 12 && wid == ((g - 2) & 7)) {
      const unsigned lc = (unsigned)(g - 2) * 64u + lane, gid = blockIdx.x * 592u + lc;
      const char* p = gid < 40960u ? pfw.wo + (size_t)gid * 128 : (gid < 96256u ? pfw.w1 + (size_t)(gid - 40960u) * 128 : pfw.w2 + (size_t)(gid - 96256u) * 128);
      if (lc < 592u && gid < 151552u) asm volatile("global_load_dword %0, %1, off" : "+v"(pfd) : "v"(p) : "memory");
    }
    float sa = 0.f, sb = 0.f;
    SBAR();
#define KRD(u) kq[(u) & 3] = ldsr128(kb + ((u) + 4) * 1024)
#define VRD(v) vq[(v) & 3] = ldsr128(vb + (v) * 1024)
#define KPRE(i) kq[i] = ldsr128(kn + (i) * 1024)
#define BARRIER_G() do { if (GRP == 0) { ENSURE_KV(g + 4); if (wid == 0 && g == n1 - 2 && j != 0) wait_word(flags + FL_Q + 32 * (8 * h + jp), 1u, flags + FL_TMO); } \
      if (g < 17) WAIT_V(6); else WAIT_V(0); WAIT_L(0); BAR(); SBAR(); \
      if (GRP == 0 && (g == n1 + 2 || (g == 17 && n1 + 2 > 17)) && tid == 0) \
        __hip_atomic_store(flags + FL_P + 32 * (8 * h + j), 1u, __ATOMIC_RELAXED, __HIP_MEMORY_SCOPE_AGENT); } while (0)
    C0 = MFMA32(kq[0], qr[0], negm); KRD(0); C1[8] = EX2(C1[8]); sa += C1[0]; sb += C1[1]; sa += C1[2]; sb += C1[3]; PIN(C1); PIN(sa); PIN(sb); SBAR();
    C0 = MFMA32(kq[1], qr[1], C0); KRD(1); C1[9] = EX2(C1[9]); sa += C1[4]; sb += C1[5]; sa += C1[6]; sb += C1[7]; PIN(C1); PIN(sa); PIN(sb); SBAR();
    C0 = MFMA32(kq[2], qr[2], C0); KRD(2); if (GRP == 0) GLDS_T(vs_p, vd_p, 1024); else if (gq) GLDS_T(ks_p, kd_p, 1024);
        C1[10] = EX2(C1[10]); pw[2] = pack8(C1, 0); PIN(C1); PIN(pw[2]); SBAR();
    C0 = MFMA32(kq[3], qr[3], C0); KRD(3); C1[11] = EX2(C1[11]); C1[12] = EX2(C1[12]); sa += C1[8]; sb += C1[9]; PIN(C1); PIN(sa); PIN(sb); SBAR();
    C0 = MFMA32(kq[0], qr[4], C0); KRD(4); C1[13] = EX2(C1[13]); C1[14] = EX2(C1[14]); sa += C1[10]; sb += C1[11]; PIN(C1); PIN(sa); PIN(sb); SBAR();
    C0 = MFMA32(kq[1], qr[5], C0); KRD(5); C1[15] = EX2(C1[15]); sa += C1[12]; sb += C1[13]; sa += C1[14]; PIN(C1); PIN(sa); PIN(sb); SBAR();
    C0 = MFMA32(kq[2], qr[6], C0); KRD(6); sb += C1[15]; pw[3] = pack8(C1, 1); PIN(pw[3]); PIN(sb); SBAR();
    C0 = MFMA32(kq[3], qr[7], C0); KRD(7); lsum += sa + sb; PIN(lsum); SBAR();
    C1 = MFMA32(kq[0], qr[0], negm); KRD(8); if (GRP == 0) GLDS_T(ks_p, kd_p, 0); SBAR();
    C1 = MFMA32(kq[1], qr[1], C1); KRD(9); SBAR();
    C1 = MFMA32(kq[2], qr[2], C1); KRD(10); SBAR();
    float mxa, mxb;
    C1 = MFMA32(kq[3], qr[3], C1); KRD(11); mxa = max3f(C0[0], C0[1], C0[2]); mxb = max3f(C0[3], C0[4], C0[5]); PIN(mxa); PIN(mxb); SBAR();
    if (GRP == 1) BARRIER_G();
    C1 = MFMA32(kq[0], qr[4], C1); VRD(0); if (GRP == 1) GLDS_T(vs_n, vd_n, 0); mxa = max3f(mxa, C0[6], C0[7]); mxb = max3f(mxb, C0[8], C0[9]); PIN(mxa); PIN(mxb); SBAR();
    C1 = MFMA32(kq[1], qr[5], C1); VRD(1); mxa = max3f(mxa, C0[10], C0[11]); mxb = max3f(mxb, C0[12], C0[13]); PIN(mxa); PIN(mxb); SBAR();
    C1 = MFMA32(kq[2], qr[6], C1); VRD(2); mxa = max3f(mxa, C0[14], C0[15]); mxa = __builtin_fmaxf(mxa, mxb); PIN(mxa); SBAR();
    C1 = MFMA32(kq[3], qr[7], C1); VRD(3); SBAR();
    if (g == n1 - 1) {
      if (j == 0) wait_word(flags + FL_Q + 32 * (8 * h + jp), 1u, flags + FL_TMO);
      const char* qsrc = ws + WS_QF + (size_t)(h * 64 + 8 * jp + wid) * 8192;
      GLOAD1_SC1(qr[0], vl16, qsrc, 0); GLOAD1_SC1(qr[1], vl16, qsrc, 1024); GLOAD1_SC1(qr[2], vl16, qsrc, 2048); GLOAD1_SC1(qr[3], vl16, qsrc, 3072);
      GLOAD1_SC1(qr[4], vl16, qsrc + 4096, 0); GLOAD1_SC1(qr[5], vl16, qsrc + 4096, 1024); GLOAD1_SC1(qr[6], vl16, qsrc + 4096, 2048); GLOAD1_SC1(qr[7], vl16, qsrc + 4096, 3072);
    }
    SBAR();
    o[0] = MFMA32(vq[0], pw[0], o[0]); VRD(4); if (GRP == 1) GLDS_T(vs_n, vd_n, 1024); SBAR();
    o[1] = MFMA32(vq[1], pw[0], o[1]); VRD(5); SBAR();
    float mxc, mxd;
    o[2] = MFMA32(vq[2], pw[0], o[2]); VRD(6); if (GRP == 0) GLDS_T(ks_p, kd_p, 1024);
        mxc = max3f(C1[0], C1[1], C1[2]); mxd = max3f(C1[3], C1[4], C1[5]); mxc = max3f(mxc, C1[6], C1[7]); mxd = max3f(mxd, C1[8], C1[9]); PIN(mxc); PIN(mxd); SBAR();
    o[3] = MFMA32(vq[3], pw[0], o[3]); VRD(7); mxc = max3f(mxc, C1[10], C1[11]); mxd = max3f(mxd, C1[12], C1[13]); mxc = max3f(mxc, C1[14], C1[15]); mxa = max3f(mxa, mxc, mxd); PIN(mxa); SBAR();
    o[0] = MFMA32(vq[0], pw[1], o[0]); VRD(8);
    float rm = mxa;
    MEMFENCE(); SBAR();
    if (dd >= 0) { cmask(C0, C1, 32 * wid + l31 - 64 * dd - 4 * hh); rm = __builtin_fmaxf(max16(C0), max16(C1)); }
    rm = xhalf_max(rm);
    float fsc = 1.f; bool resc = false;
    if (__builtin_expect(seam || __any(rm > THR), 0)) {
      const float dl = seam ? __builtin_fmaxf(rm, -1e30f) : __builtin_fmaxf(rm, 0.f);
      mhat += dl;
#pragma unroll
      for (int r = 0; r < 16; ++r) { C0[r] -= dl; C1[r] -= dl; negm[r] = -mhat; }
      PIN(negm);
      if (seam) { lsum1 = lsum; lsum = 0.f; }
      else { fsc = EX2(-dl); lsum *= fsc; resc = true; }
    }
    SBAR();
    sa = 0.f; sb = 0.f;
    o[1] = MFMA32(vq[1], pw[1], o[1]); VRD(9); C0[0] = EX2(C0[0]); C0[1] = EX2(C0[1]); C0[2] = EX2(C0[2]); PIN(C0); SBAR();
    o[2] = MFMA32(vq[2], pw[1], o[2]); VRD(10); C0[3] = EX2(C0[3]); C0[4] = EX2(C0[4]); sa += C0[0]; sb += C0[1]; PIN(C0); PIN(sa); PIN(sb); SBAR();
    o[3] = MFMA32(vq[3], pw[1], o[3]); VRD(11); C0[5] = EX2(C0[5]); C0[6] = EX2(C0[6]); sa += C0[2]; sb += C0[3]; PIN(C0); PIN(sa); PIN(sb); SBAR();
    o[0] = MFMA32(vq[0], pw[2], o[0]); VRD(12); if (GRP == 1) GLDS_T(ks_n, kd_n, 0); C0[7] = EX2(C0[7]); C0[8] = EX2(C0[8]); sa += C0[4]; sb += C0[5]; PIN(C0); PIN(sa); PIN(sb); SBAR();
    o[1] = MFMA32(vq[1], pw[2], o[1]); VRD(13); pw[0] = pack8(C0, 0); C0[9] = EX2(C0[9]); PIN(pw[0]); PIN(C0); SBAR();
    o[2] = MFMA32(vq[2], pw[2], o[2]); VRD(14); C0[10] = EX2(C0[10]); C0[11] = EX2(C0[11]); sa += C0[6]; sb += C0[7]; PIN(C0); PIN(sa); PIN(sb); SBAR();
    o[3] = MFMA32(vq[3], pw[2], o[3]); VRD(15); C0[12] = EX2(C0[12]); C0[13] = EX2(C0[13]); sa += C0[8]; sb += C0[9]; PIN(C0); PIN(sa); PIN(sb); SBAR();
    if (GRP == 0) BARRIER_G();
    o[0] = MFMA32(vq[0], pw[3], o[0]); KPRE(0); if (GRP == 0) GLDS_T(vs_n, vd_n, 0); C0[14] = EX2(C0[14]); C0[15] = EX2(C0[15]); sa += C0[10]; sb += C0[11]; PIN(C0); PIN(sa); PIN(sb); SBAR();
    o[1] = MFMA32(vq[1], pw[3], o[1]); KPRE(1); C1[0] = EX2(C1[0]); C1[1] = EX2(C1[1]); sa += C0[12]; sb += C0[13]; PIN(C1); PIN(sa); PIN(sb); SBAR();
    o[2] = MFMA32(vq[2], pw[3], o[2]); KPRE(2); pw[1] = pack8(C0, 1); C1[2] = EX2(C1[2]); C1[3] = EX2(C1[3]); PIN(pw[1]); PIN(C1); SBAR();
    o[3] = MFMA32(vq[3], pw[3], o[3]); KPRE(3); C1[4] = EX2(C1[4]); C1[5] = EX2(C1[5]); sa += C0[14]; sb += C0[15]; PIN(C1); PIN(sa); PIN(sb); SBAR();
    C1[6] = EX2(C1[6]); C1[7] = EX2(C1[7]); lsum += sa + sb; PIN(C1); PIN(lsum);
    if (resc) {
#pragma unroll
      for (int d = 0; d < 4; ++d) o[d] = o[d] * fsc;
    }
    if (seam) {
      const float lt = xhalf_sum(lsum1);
      const char* pst = ws + WS_PF + (size_t)((h * 8 + j) * 8 + wid) * 8192;
      static_for<4>([&](auto dd_) { constexpr int d = decltype(dd_)::value; gstore_sc1_c<(2 * d) * 1024>(vl16, pack8(o[d], 0), pst); gstore_sc1_c<(2 * d + 1) * 1024>(vl16, pack8(o[d], 1), pst); o[d] = z; });
      const unsigned long long sv = ((unsigned long long)__float_as_uint(lt) << 32) | __float_as_uint(mhat1);
      __hip_atomic_store((unsigned long long*)(ws + WS_PS) + (size_t)((h * 8 + j) * 8 + wid) * 64 + lane, sv, __ATOMIC_RELAXED, __HIP_MEMORY_SCOPE_AGENT);
    }
    if (g == n1 - 1) {
      asm volatile("s_waitcnt vmcnt(1)" : "+v"(qr[0]), "+v"(qr[1]), "+v"(qr[2]), "+v"(qr[3]), "+v"(qr[4]), "+v"(qr[5]), "+v"(qr[6]), "+v"(qr[7]) :: "memory");
    }
  }
  asm volatile("" :: "v"(pfd));
  {
    ldscp vb = lds + L_V + (17 & 3) * 16384 + rdoff;
#pragma unroll
    for (int i = 0; i < 4; ++i) vq[i] = ldsr128(vb + i * 1024);
    float sa = 0.f;
#pragma unroll
    for (int r = 8; r < 16; ++r) C1[r] = EX2(C1[r]);
#pragma unroll
    for (int r = 0; r < 16; ++r) sa += C1[r];
    lsum += sa;
    pw[2] = pack8(C1, 0); pw[3] = pack8(C1, 1);
    SBAR();
#pragma unroll
    for (int v = 0; v < 16; ++v) {
      o[v & 3] = MFMA32(vq[v & 3], pw[v >> 2], o[v & 3]);
      if (v + 4 < 16) vq[v & 3] = ldsr128(vb + (v + 4) * 1024);
      SBAR();
    }
    lsum = xhalf_sum(lsum);
    wait_word(flags + FL_P + 32 * (8 * h + jp), 1u, flags + FL_TMO);
    const char* psrc = upt(ws + WS_PF + (size_t)((h * 8 + jp) * 8 + wid) * 8192);
    u32x4 qa[4], qb[4];
    GLOAD1_SC1(qa[0], vl16, psrc, 0); GLOAD1_SC1(qa[1], vl16, psrc, 1024); GLOAD1_SC1(qa[2], vl16, psrc, 2048); GLOAD1_SC1(qa[3], vl16, psrc, 3072);
    const char* psrc2 = upt(psrc + 4096);
    GLOAD1_SC1(qb[0], vl16, psrc2, 0); GLOAD1_SC1(qb[1], vl16, psrc2, 1024); GLOAD1_SC1(qb[2], vl16, psrc2, 2048); GLOAD1_SC1(qb[3], vl16, psrc2, 3072);
    const unsigned long long sv = __hip_atomic_load((const unsigned long long*)(ws + WS_PS) + (size_t)((h * 8 + jp) * 8 + wid) * 64 + lane, __ATOMIC_RELAXED, __HIP_MEMORY_SCOPE_AGENT);
    WAITV_TIE4(0, qa); WAITV_TIE4(0, qb);
    const float om = __uint_as_float((unsigned)sv), ol = __uint_as_float((unsigned)(sv >> 32));
    const float m = __builtin_fmaxf(mhat, om);
    const float f0 = EX2(mhat - m), f1 = EX2(om - m);
    const float inv = __builtin_amdgcn_rcpf(lsum * f0 + ol * f1);
    const float g0 = f0 * inv, g1 = f1 * inv;
    const char* cdst = upt(ws + WS_CTX + (size_t)((8 * jp + wid) * 32 + h) * 8192);
    static_for<8>([&](auto ii_) {
      constexpr int i = decltype(ii_)::value; constexpr int d = i >> 1, sp = i & 1;
      const u32x4 v4 = i < 4 ? qa[i & 3] : qb[i & 3];
      u32x4 w;
#pragma unroll
      for (int e = 0; e < 4; ++e) w[e] = cvtpk(o[d][8 * sp + 2 * e] * g0 + bflo(v4[e]) * g1, o[d][8 * sp + 2 * e + 1] * g0 + bfhi(v4[e]) * g1);
      gstore_sc1_c<i * 1024>(vl16, __builtin_bit_cast(bf16x8, w), cdst);
    });
  }
#undef KRD
#undef VRD
#undef KPRE
#undef BARRIER_G
#undef ENSURE_KV
#undef GLDS_T
#undef TILE
#undef KSRC
#undef VSRC
}

constexpr int T_LDS = 163840;
constexpr int FL_C = 28672;
constexpr int FL_P3 = 20480;
constexpr int FL_H = 22528;
constexpr int FL_U = 23552;
#define GLOAD4_SC1(d, voff, sbase) asm volatile("s_nop 4\n\tglobal_load_dwordx4 %0, %4, %5 sc1\n\tglobal_load_dwordx4 %1, %4, %5 offset:1024 sc1\n\tglobal_load_dwordx4 %2, %4, %5 offset:2048 sc1\n\tglobal_load_dwordx4 %3, %4, %5 offset:3072 sc1" \
    : "=&v"(d[0]), "=&v"(d[1]), "=&v"(d[2]), "=&v"(d[3]) : "v"(voff), "s"(sbase) : "memory")
#define GLDS_SC1(voff, sbase, ldst) do { unsigned keep_; asm volatile("s_mov_b32 %0, m0\n\ts_mov_b32 m0, %3\n\ts_nop 2\n\tglobal_load_lds_dwordx4 %1, %2 sc1\n\ts_mov_b32 m0, %0" \
    : "=&s"(keep_) : "v"(voff), "s"(sbase), "s"(ldst) : "memory"); } while (0)
template <int OFFS> DI void gstore_sc1_uc(unsigned voff, const u32x4& v, const void* sbase) {
  unsigned t;
  asm volatile("v_add_u32 %0, %4, %1\n\ts_nop 3\n\tglobal_store_dwordx4 %0, %2, %3 sc1\n\ts_nop 1" : "=&v"(t) : "v"(voff), "v"(v), "s"(sbase), "i"(OFFS) : "memory");
}
DI void gstore_sc1_u(unsigned voff, const u32x4& v, const void* sbase) {
  asm volatile("s_nop 4\n\tglobal_store_dwordx4 %0, %1, %2 sc1\n\ts_nop 1" :: "v"(voff), "v"(v), "s"(sbase) : "memory");
}
template <bool FIRST> struct MlpCfg {
  static constexpr int PITCH = FIRST ? FF * 2 : SEQ * 2;
  static constexpr int NKT = FIRST ? SEQ / 64 : FF / 64;
  static constexpr int NST = (NKT + 1) / 2;
};
template <bool FIRST> DI unsigned mlp_voffw(int wid, int lane) {
  const int row = 8 * wid + (lane >> 3); const int f = ((row >> 1) & 1) | (((row >> 3) & 1) << 1);
  return (unsigned)(row * MlpCfg<FIRST>::PITCH + (((lane & 7) ^ (f << 1)) << 4));
}
template <bool FIRST, int KT0, int NK> DI void mlp_wstage(unsigned lds0, const char* wb, unsigned voffW, int wid, int reg) {
  static_for<NK>([&](auto kk_) {
    constexpr int k = decltype(kk_)::value; constexpr int kt = (KT0 + k) < MlpCfg<FIRST>::NKT ? (KT0 + k) : MlpCfg<FIRST>::NKT - 1;
    glds16_c<kt * 64 * MlpCfg<FIRST>::PITCH>(voffW, wb, lds0 + reg + k * 8192 + wid * 1024);
  });
}
struct MlpW { ldscp p[2]; };
DI MlpW mlp_wptr(ldsp lds, int wid, int lane) {
  const int cp = wid & 1, qq = wid >> 1, g = lane >> 4, q = (lane & 15) >> 2, p = lane & 3; const int f = (q >> 1) | ((g & 1) << 1);
  MlpW w;
#pragma unroll
  for (int e = 0; e < 2; ++e) w.p[e] = lds + (qq >> 1) * 8192 + 128 * (32 * (qq & 1) + 8 * g + q) + (((2 * (2 * cp + e) + (p >> 1)) ^ (f << 1)) << 4) + 8 * (p & 1);
  return w;
}
template <int P0, int NP, int NW> DI void mlp_wread(const MlpW& w, int reg, bf16x8 (&wf)[NW]) {
  static_for<NP>([&](auto kk_) {
    constexpr int k = decltype(kk_)::value;
#pragma unroll
    for (int e = 0; e < 2; ++e) wf[(P0 + k) * 2 + e] = cat8(vtr(w.p[e] + reg + k * 16384), vtr(w.p[e] + reg + k * 16384 + 512));
  });
}
constexpr int mlp_slot(int s) { return (s & 3) * 40960; }
template <bool FIRST> DI void mlp_astage(unsigned lds0, const char* ab, unsigned vl16, int wid, int stg) {
  constexpr int NT = 2 * MlpCfg<FIRST>::NKT;
#pragma unroll
  for (int k = 0; k < 5; ++k) {
    const int i = wid * 5 + k;
    const int T_ = 8 * stg + wid; const int Tc_ = T_ < NT ? T_ : NT - 1;
    GLDS_SC1(vl16, ab + ((size_t)Tc_ * 40 + k) * 1024, lds0 + mlp_slot(stg) + i * 1024);
  }
}
template <bool FIRST, int NW>
DI void mlp_main(ldsp lds, unsigned lds0, const char* __restrict__ ab, int wid, int lane, const bf16x8 (&wf)[NW], f32x4 (&acc)[2][5]) {
  constexpr int NKT = MlpCfg<FIRST>::NKT, NS = (NKT + 3) / 4;
  const int qq = wid >> 1;
  const unsigned vl16 = lane * 16;
  ldscp ap = lds + qq * 5120 + lane * 16;
#pragma unroll
  for (int e = 0; e < 2; ++e)
#pragma unroll
    for (int d = 0; d < 5; ++d) acc[e][d] = (f32x4){0.f, 0.f, 0.f, 0.f};
  mlp_astage<FIRST>(lds0, ab, vl16, wid, 0); mlp_astage<FIRST>(lds0, ab, vl16, wid, 1); mlp_astage<FIRST>(lds0, ab, vl16, wid, 2);
  static_for<NS>([&](auto ss_) {
    constexpr int s = decltype(ss_)::value;
    constexpr int ahead = (NS - 1 - s) < 2 ? (NS - 1 - s) : 2;
    WAITVN(5 * ahead);
    SBAR(); BAR();
    bf16x8 af[2][5];
#pragma unroll
    for (int e2 = 0; e2 < 2; ++e2)
#pragma unroll
      for (int d = 0; d < 5; ++d) af[e2][d] = ldsr128(ap + mlp_slot(s) + e2 * 20480 + d * 1024);
    SBAR();
    if constexpr (s + 3 < NS) mlp_astage<FIRST>(lds0, ab, vl16, wid, s + 3);
    SBAR();
#pragma unroll
    for (int e2 = 0; e2 < 2; ++e2) {
      const bool ok = (4 * s + 2 * e2 + 1 < NKT) || (4 * s + 2 * e2 + (qq >> 1) < NKT);
      if (ok) {
#pragma unroll
        for (int e = 0; e < 2; ++e)
#pragma unroll
          for (int d = 0; d < 5; ++d) acc[e][d] = FIRST ? MFMA16(wf[(2 * s + e2) * 2 + e], af[e2][d], acc[e][d]) : MFMA16(af[e2][d], wf[(2 * s + e2) * 2 + e], acc[e][d]);
      }
    }
    SBAR();
  });
}
DI void mlp_park(ldsp lds, int wid, int lane, const f32x4 (&acc)[2][5]) {
#pragma unroll
  for (int e = 0; e < 2; ++e)
#pragma unroll
    for (int d = 0; d < 5; ++d) *(LAS f32x4*)(lds + ((wid * 2 + e) * 5 + d) * 1024 + lane * 16) = acc[e][d];
  WAIT_L(0); BAR();
}
DI void mlp_gather(ldscp lds, int c2, int dt, int gD, int c15, float (&v)[8]) {
#pragma unroll
  for (int hf = 0; hf < 2; ++hf) {
    f32x4 a = {0.f, 0.f, 0.f, 0.f};
#pragma unroll
    for (int q = 0; q < 4; ++q) a += *(const LAS f32x4*)(lds + ((q * 4 + c2) * 5 + dt) * 1024 + ((gD + hf) * 16 + c15) * 16);
#pragma unroll
    for (int e = 0; e < 4; ++e) v[4 * hf + e] = a[e];
  }
}

DI void tail_phase(unsigned char* smem, const u16* __restrict__ x, const u16* __restrict__ wo, const u16* __restrict__ w1, const u16* __restrict__ b1,
                   const u16* __restrict__ w2, const u16* __restrict__ b2, char* __restrict__ ws, u16* __restrict__ out, int wid) {
  const int b = blockIdx.x, xcd = b & 7, r = b >> 3;
  ldsp lds = (ldsp)smem;
  const unsigned lds0 = (unsigned)(uintptr_t)smem;
  unsigned* flags = (unsigned*)(ws + WS_FLAGS);
  const int ksp = xcd >> 1, panel = (xcd & 1) * 8 + (r >> 2), ctile = r & 3;
  const int idx5 = xcd * 27 + r;
  const int wt5 = idx5 >> 3, dg5 = idx5 & 7;
  const int wt6 = xcd * 4 + (r >> 3), dg6 = r & 7;
  const bool do5 = r < 27;
  constexpr int P4S = 131072;
  bf16x8 wf[32];
  {
    const int lane = lane_id();
    const unsigned vl16 = lane * 16;
    const int kh = wid >> 2, st = wid & 3, hh = lane >> 5;
    unsigned voff[5];
#pragma unroll
    for (int i = 0; i < 5; ++i) {
      const int id = 2 * (5 * wid + i) + (lane >> 5), half = id / 40, rem = id % 40, rg = rem / 5, c = rem % 5;
      const int row = 64 * half + 8 * rg + ((lane >> 2) & 7);
      voff[i] = (unsigned)(row * 1280 + (32 * c + 8 * ((lane & 3) ^ ((row >> 2) & 3))) * 2);
    }
    const char* wb = (const char*)wo + (size_t)(ksp * 1024) * 1280 + ctile * 320;
    const unsigned dstw = lds0 + wid * 5120;
#define OPIECE(hd, i) glds16_c<(hd) * 128 * 1280>(voff[i], wb, dstw + ((hd) & 3) * 40960 + (i) * 1024)
#define STAGE_O(hd) do { OPIECE(hd, 0); OPIECE(hd, 1); OPIECE(hd, 2); OPIECE(hd, 3); OPIECE(hd, 4); } while (0)
    const char* cb = ws + WS_CTX + ((size_t)((4 * panel + st) * 32 + 8 * ksp) * 8 + 4 * kh) * 1024;
    int boff[2];
    { const int blk = (lane >> 4) & 1, q = (lane & 15) >> 2, p = lane & 3;
#pragma unroll
      for (int t = 0; t < 2; ++t) boff[t] = kh * 20480 + 2560 * t + 64 * (4 * hh + q) + (((2 * blk + (p >> 1)) ^ ((2 * t + hh) & 3)) << 4) + 8 * (p & 1); }
    ldscp bp0 = lds + boff[0], bp1 = lds + boff[1];
    u32x4 xf[3][4];
    f32x16 acc[5] = {};
    bf16x8 aq[6];
#define ARD(U) aq[(U) % 6] = cat8(vtr(bp0 + (((U) / 20) & 3) * 40960 + (((U) % 20) / 5) * 5120 + ((U) % 5) * 512), vtr(bp1 + (((U) / 20) & 3) * 40960 + (((U) % 20) / 5) * 5120 + ((U) % 5) * 512))
    STAGE_O(0); STAGE_O(1); STAGE_O(2);
    if (wid == 0) wait_word(flags + FL_C + 16 * ((panel >> 1) * 4 + ksp), 8u, flags + FL_TMO);
    BAR();
    GLOAD4_SC1(xf[0], vl16, cb); GLOAD4_SC1(xf[1], vl16, cb + 8192);
    WAIT_V(8); BAR();
#pragma unroll
    for (int U = 0; U < 5; ++U) ARD(U);
    static_for<8>([&](auto hh_) {
      constexpr int hd = decltype(hh_)::value;
      if constexpr (hd == 0) { WAITV_TIE4(4, xf[0]); }
      else if constexpr (hd <= 5) { WAITV_TIE4(9, xf[hd % 3]); }
      else if constexpr (hd == 6) { WAITV_TIE4(4, xf[hd % 3]); }
      else { WAITV_TIE4(0, xf[hd % 3]); }
      SBAR(); BAR(); SBAR();
      const char* xn_ = cb + (size_t)(hd + 2) * 8192;
      static_for<20>([&](auto uu_) {
        constexpr int u = decltype(uu_)::value;
        constexpr int U = 20 * hd + u;
        acc[u % 5] = MFMA32(aq[U % 6], __builtin_bit_cast(bf16x8, xf[hd % 3][u / 5]), acc[u % 5]);
        if constexpr (U + 5 < 160) ARD(U + 5);
        if constexpr (hd + 2 < 8) {
          if constexpr (u == 1) GLOAD1_SC1(xf[(hd + 2) % 3][0], vl16, xn_, 0);
          if constexpr (u == 3) GLOAD1_SC1(xf[(hd + 2) % 3][1], vl16, xn_, 1024);
          if constexpr (u == 5) GLOAD1_SC1(xf[(hd + 2) % 3][2], vl16, xn_, 2048);
          if constexpr (u == 7) GLOAD1_SC1(xf[(hd + 2) % 3][3], vl16, xn_, 3072);
        }
        if constexpr (hd + 3 < 8) {
          if constexpr (u == 9) OPIECE(hd + 3, 0);
          if constexpr (u == 11) OPIECE(hd + 3, 1);
          if constexpr (u == 13) OPIECE(hd + 3, 2);
          if constexpr (u == 15) OPIECE(hd + 3, 3);
          if constexpr (u == 17) OPIECE(hd + 3, 4);
        }
        SBAR();
      });
    });
#undef OPIECE
#undef STAGE_O
#undef ARD
    BAR();
    ldsp xw = lds + (kh == 0 ? 49152 : 0) + st * 12288 + lane * 16;
    ldscp xr = lds + (kh == 0 ? 0 : 49152) + st * 12288 + lane * 16;
    if (kh == 0) {
#pragma unroll
      for (int i = 0; i < 2; ++i)
#pragma unroll
        for (int g = 0; g < 4; ++g) *(LAS f32x4*)(xw + (i * 4 + g) * 1024) = (f32x4){acc[3 + i][4 * g], acc[3 + i][4 * g + 1], acc[3 + i][4 * g + 2], acc[3 + i][4 * g + 3]};
    } else {
#pragma unroll
      for (int i = 0; i < 3; ++i)
#pragma unroll
        for (int g = 0; g < 4; ++g) *(LAS f32x4*)(xw + (i * 4 + g) * 1024) = (f32x4){acc[i][4 * g], acc[i][4 * g + 1], acc[i][4 * g + 2], acc[i][4 * g + 3]};
    }
    WAIT_L(0); BAR();
    const char* sb = ws + WS_SLAB + ((size_t)((ksp * 64 + 4 * panel + st) * 20 + 5 * ctile) * 2) * 1024;
#define SLABST(I, T) static_for<2>([&](auto pp_) { \
          constexpr int sp = decltype(pp_)::value; \
          const f32x4 o0 = *(const LAS f32x4*)(xr + ((I) * 4 + 2 * sp) * 1024), o1 = *(const LAS f32x4*)(xr + ((I) * 4 + 2 * sp + 1) * 1024); \
          const u32x4 w = {cvtpk(acc[T][8 * sp] + o0[0], acc[T][8 * sp + 1] + o0[1]), cvtpk(acc[T][8 * sp + 2] + o0[2], acc[T][8 * sp + 3] + o0[3]), \
                           cvtpk(acc[T][8 * sp + 4] + o1[0], acc[T][8 * sp + 5] + o1[1]), cvtpk(acc[T][8 * sp + 6] + o1[2], acc[T][8 * sp + 7] + o1[3])}; \
          gstore_sc1_uc<((T) * 2 + sp) * 1024>(vl16, w, sb); })
    if (kh == 0) { SLABST(0, 0); SLABST(1, 1); SLABST(2, 2); }
    else { SLABST(0, 3); SLABST(1, 4); }
#undef SLABST
    WAIT_V(0); WAIT_L(0); BAR();
    if (wid == 0 && lane == 0) __hip_atomic_fetch_add(flags + FL_P3 + 64 * panel, 1u, __ATOMIC_RELAXED, __HIP_MEMORY_SCOPE_AGENT);
  }
  {
    const int lane = lane_id(), tid = wid * 64 + lane;
    const MlpW wp = mlp_wptr(lds, wid, lane);
    const char* w1b = (const char*)w1 + wt5 * 128;
    const unsigned voffW = mlp_voffw<true>(wid, lane);
    ldsp tr = lds + P4S;
    LAS float* red = (LAS float*)(lds + P4S + 10240);
    const int rr = tid & 7, pc = tid >> 3;
    const int r8 = ksp * 4 + ctile;
    const int s = 128 * panel + 8 * r8 + rr, st = s >> 5;
    int n0[2]; unsigned so[2];
    u32x2 xv[2][2];
#pragma unroll
    for (int i = 0; i < 2; ++i) {
      const int pi = pc + 64 * i; const int pcl = pi < 80 ? pi : 0;
      const int nt = pcl >> 2, sp = (pcl >> 1) & 1, hh = pcl & 1;
      n0[i] = 32 * nt + 16 * sp + 4 * hh;
      so[i] = (unsigned)((((st * 20 + nt) * 2 + sp) * 64 + (s & 31) + 32 * hh) * 16);
      xv[i][0] = *(const u32x2*)(x + (size_t)s * DM + n0[i]); xv[i][1] = *(const u32x2*)(x + (size_t)s * DM + n0[i] + 8);
    }
    asm volatile("" : "+v"(xv[0][0]), "+v"(xv[0][1]), "+v"(xv[1][0]), "+v"(xv[1][1]));
    if (do5) mlp_wstage<true, 0, 16>(lds0, w1b, voffW, wid, 0);
    if (wid == 0) wait_word(flags + FL_P3 + 64 * panel, 16u, flags + FL_TMO);
    BAR();
    u32x4 sv[2][4];
    const char* sl = ws + WS_SLAB;
#pragma unroll
    for (int i = 0; i < 2; ++i)
#pragma unroll
      for (int k = 0; k < 4; ++k) GLOAD1_SC1(sv[i][k], so[i], sl + (size_t)k * (64 * 20 * 2 * 1024), 0);
    WAITV_TIE4(0, sv[0]); WAITV_TIE4(0, sv[1]);
    float hv[2][8];
    unsigned pk[2][4];
    float sum = 0.f, ssq = 0.f;
#pragma unroll
    for (int i = 0; i < 2; ++i) {
      const bool ok = pc + 64 * i < 80;
#pragma unroll
      for (int e = 0; e < 4; ++e) {
        const float lo = (bflo(sv[i][0][e]) + bflo(sv[i][1][e])) + (bflo(sv[i][2][e]) + bflo(sv[i][3][e]));
        const float hi = (bfhi(sv[i][0][e]) + bfhi(sv[i][1][e])) + (bfhi(sv[i][2][e]) + bfhi(sv[i][3][e]));
        const unsigned xw_ = xv[i][e >> 1][e & 1];
        pk[i][e] = cvtpk(lo + bflo(xw_), hi + bfhi(xw_));
        hv[i][2 * e] = ok ? bflo(pk[i][e]) : 0.f; hv[i][2 * e + 1] = ok ? bfhi(pk[i][e]) : 0.f;
        sum += hv[i][2 * e] + hv[i][2 * e + 1];
      }
    }
    sum += __shfl_xor(sum, 8); sum += __shfl_xor(sum, 16); sum += __shfl_xor(sum, 32);
    const float cntw = (wid < 2) ? 128.f : 64.f;
    const float mw = sum / cntw;
#pragma unroll
    for (int i = 0; i < 2; ++i) {
      if (pc + 64 * i < 80) {
#pragma unroll
        for (int e = 0; e < 8; ++e) { const float d = hv[i][e] - mw; ssq += d * d; }
      }
    }
    ssq += __shfl_xor(ssq, 8); ssq += __shfl_xor(ssq, 16); ssq += __shfl_xor(ssq, 32);
    if (lane < 8) { red[(wid * 8 + lane) * 2] = mw; red[(wid * 8 + lane) * 2 + 1] = ssq; }
    WAIT_L(0); BAR();
    float mean = 0.f;
#pragma unroll
    for (int w = 0; w < 8; ++w) mean += red[(w * 8 + rr) * 2] * (w < 2 ? 128.f : 64.f);
    mean *= (1.0f / DM);
    float m2 = 0.f;
#pragma unroll
    for (int w = 0; w < 8; ++w) { const float dm = red[(w * 8 + rr) * 2] - mean; m2 += red[(w * 8 + rr) * 2 + 1] + dm * dm * (w < 2 ? 128.f : 64.f); }
    const float rstd = 1.0f / sqrtf(m2 * (1.0f / DM) + LN_EPS);
#pragma unroll
    for (int i = 0; i < 2; ++i) {
      if (pc + 64 * i < 80) {
        LAS u16* t16 = (LAS u16*)tr;
#pragma unroll
        for (int e = 0; e < 4; ++e) {
          const unsigned p = cvtpk((hv[i][2 * e] - mean) * rstd, (hv[i][2 * e + 1] - mean) * rstd);
          const int n = n0[i] + 8 * (e >> 1) + 2 * (e & 1);
          t16[n * 8 + rr] = (u16)(p & 0xffff); t16[(n + 1) * 8 + rr] = (u16)(p >> 16);
        }
      }
    }
    WAIT_L(0); BAR();
    const char* hdst = ws + WS_HNF + ((size_t)st * 40 * 64 + ((s >> 3) & 3) * 16) * 16;
    { const int d = tid; const u32x4 tv4 = *(const LAS u32x4*)(tr + d * 16); gstore_sc1_u((unsigned)(((d >> 4) * 64 + (d & 15)) * 16), tv4, hdst); }
    if (tid < 128) { const int d = 512 + tid; const u32x4 tv4 = *(const LAS u32x4*)(tr + d * 16); gstore_sc1_u((unsigned)(((d >> 4) * 64 + (d & 15)) * 16), tv4, hdst); }
#pragma unroll
    for (int i = 0; i < 2; ++i) {
      if (pc + 64 * i < 80) {
        unsigned long long* hp = (unsigned long long*)(ws + WS_H + ((size_t)s * DM + n0[i]) * 2);
        __hip_atomic_store(hp, ((unsigned long long)pk[i][1] << 32) | pk[i][0], __ATOMIC_RELAXED, __HIP_MEMORY_SCOPE_AGENT);
        __hip_atomic_store(hp + 2, ((unsigned long long)pk[i][3] << 32) | pk[i][2], __ATOMIC_RELAXED, __HIP_MEMORY_SCOPE_AGENT);
      }
    }
    if (do5) mlp_wread<0, 8>(wp, 0, wf);
    WAIT_V(0); WAIT_L(0); BAR();
    if (tid == 0) __hip_atomic_fetch_add(flags + FL_H + 64 * (b & 7), 1u, __ATOMIC_RELAXED, __HIP_MEMORY_SCOPE_AGENT);
    if (do5) {
      mlp_wstage<true, 16, 16>(lds0, w1b, voffW, wid, 0);
      WAIT_V(0); BAR();
      mlp_wread<8, 8>(wp, 0, wf);
    }
  }
  constexpr int RHI = 81920;
  {
    const int lane = lane_id(), tid = wid * 64 + lane;
    const char* w2b = (const char*)w2 + wt6 * 128;
    const unsigned voffW2 = mlp_voffw<false>(wid, lane);
    if (do5) {
      if (wid == 0) {
        for (unsigned spins = 0;; ++spins) {
          const unsigned v = __hip_atomic_load(flags + FL_H + 64 * (lane & 7), __ATOMIC_RELAXED, __HIP_MEMORY_SCOPE_AGENT);
          if (__all(v >= 32u)) break;
          if (spins > (1u << 16)) { __hip_atomic_store(flags + FL_TMO, 1u, __ATOMIC_RELAXED, __HIP_MEMORY_SCOPE_AGENT); break; }
          __builtin_amdgcn_s_sleep(1);
        }
      }
      WAIT_L(0); BAR();
      f32x4 acc[2][5];
      mlp_main<true, 32>(lds, lds0, ws + WS_HNF + (size_t)(5 * dg5) * 1024, wid, lane, wf, acc);
      BAR();
      mlp_park(lds, wid, lane, acc);
      for (int it = tid; it < 640; it += 512) {
        float v[8];
        const int d15 = it & 15, gp = (it >> 4) & 3, fr = it >> 6, dt = fr % 5, Tp = fr / 5;
        mlp_gather(lds, 2 * Tp + (gp >> 1), dt, 2 * (gp & 1), d15, v);
        const int f0 = 64 * wt5 + 32 * Tp + 8 * gp;
        const u32x4 bv = *(const u32x4*)(b1 + f0);
        u32x4 o;
#pragma unroll
        for (int e = 0; e < 4; ++e) {
          float z[2] = {v[2 * e] + bflo(bv[e]), v[2 * e + 1] + bfhi(bv[e])};
#pragma unroll
          for (int k = 0; k < 2; ++k) {
            const float xx = z[k];
            const float a = xx * (1.0f + 0.044715f * xx * xx) * (-2.0f * 0.7978845608028654f * 1.4426950408889634f);
            z[k] = xx * __builtin_amdgcn_rcpf(1.0f + __builtin_amdgcn_exp2f(a));
          }
          o[e] = cvtpk(z[0], z[1]);
        }
        gstore_sc1_u((unsigned)((((2 * wt5 + Tp) * 40 + 5 * dg5 + dt) * 64 + gp * 16 + d15) * 16), o, ws + WS_UF);
      }
      mlp_wstage<false, 0, 10>(lds0, w2b, voffW2, wid, RHI);
      WAIT_V(10); WAIT_L(0); BAR();
      if (tid == 0) __hip_atomic_fetch_add(flags + FL_U + 64 * dg5, 1u, __ATOMIC_RELAXED, __HIP_MEMORY_SCOPE_AGENT);
    } else {
      WAIT_L(0); BAR();
      mlp_wstage<false, 0, 10>(lds0, w2b, voffW2, wid, RHI);
    }
  }
  {
    const int lane = lane_id(), tid = wid * 64 + lane;
    bf16x8 wg[28];
    const MlpW wp = mlp_wptr(lds, wid, lane);
    const char* w2b = (const char*)w2 + wt6 * 128;
    const unsigned voffW2 = mlp_voffw<false>(wid, lane);
    mlp_wstage<false, 10, 10>(lds0, w2b, voffW2, wid, 0);
    WAIT_V(0); BAR();
    mlp_wread<0, 5>(wp, RHI, wg); mlp_wread<5, 5>(wp, 0, wg);
    WAIT_L(0); BAR();
    mlp_wstage<false, 20, 8>(lds0, w2b, voffW2, wid, 0);
    WAIT_V(0); BAR();
    mlp_wread<10, 4>(wp, 0, wg);
    if (wid == 0) wait_word(flags + FL_U + 64 * dg6, 27u, flags + FL_TMO);
    WAIT_L(0); BAR();
    const char* ab = ws + WS_UF + (size_t)(5 * dg6) * 1024;
    f32x4 acc[2][5];
    mlp_main<false, 28>(lds, lds0, ab, wid, lane, wg, acc);
    BAR();
    mlp_park(lds, wid, lane, acc);
    for (int it = tid; it < 640; it += 512) {
      float v[8];
      const int sl = it / 10, c = it % 10;
      const int srow = 64 * wt6 + sl;
      const size_t o = (size_t)srow * DM + 80 * dg6 + 8 * c;
      u32x4 hv;
      GLOAD1_SC1(hv, (unsigned)(o * 2), ws + WS_H, 0);
      mlp_gather(lds, sl >> 4, c >> 1, 2 * (c & 1), sl & 15, v);
      const float bb = bflo((unsigned)b2[srow]);
      asm volatile("s_waitcnt vmcnt(0)" : "+v"(hv) :: "memory");
      u32x4 ov;
#pragma unroll
      for (int e = 0; e < 4; ++e) ov[e] = cvtpk(v[2 * e] + bb + bflo(hv[e]), v[2 * e + 1] + bb + bfhi(hv[e]));
      *(u32x4*)(out + o) = ov;
    }
  }
}

constexpr int qproj_st0(int cls) { return cls == 0 ? 0 : (cls == 1 ? 2 : (cls == 2 ? 5 : 7)); }
constexpr int qproj_ns(int c, int cls) { const int rel = c - qproj_st0(cls); return (rel < 0 || rel > 2) ? 0 : (rel == 0 ? 6 : 5); }
constexpr int qproj_nl(int c) { return (c + 2 < 10 ? 4 : 0) + (c + 3 < 10 ? 2 : 0); }
constexpr int qproj_allowed(int c, int cls) {
  return c == 0 ? 6 : (c == 1 ? qproj_nl(0) + qproj_ns(0, cls) : qproj_ns(c - 2, cls) + qproj_nl(c - 1) + qproj_ns(c - 1, cls));
}
__global__ __launch_bounds__(512, 2) void kF1(const u16* __restrict__ ckv, const u16* __restrict__ wuk, const u16* __restrict__ wuv, const u16* __restrict__ wq,
                                              const u16* __restrict__ wo, const u16* __restrict__ w1, const u16* __restrict__ w2,
                                              const u16* __restrict__ x, const u16* __restrict__ b1, const u16* __restrict__ b2, char* __restrict__ ws, u16* __restrict__ out) {
  extern __shared__ __attribute__((aligned(16))) unsigned char smem[];
  const int bidx = blockIdx.x, xcd = bidx & 7, rr_ = bidx >> 3;
  const int h = xcd * 4 + (rr_ >> 3), j = rr_ & 7;
  const int wid = rfl(threadIdx.x >> 6);
  ldsp lds = (ldsp)smem;
  const unsigned lds0 = (unsigned)(uintptr_t)smem;
  unsigned* flags = (unsigned*)(ws + WS_FLAGS);
  bf16x8 kvp[16];
  {
    const int wr = wid >> 2, wc = wid & 3;
    const int lane = threadIdx.x & 63, l31 = lane & 31, hh = lane >> 5;
    unsigned voffC, voffW;
    { const int row = 8 * wid + (lane >> 3); voffC = (unsigned)(row * 1024 + (((lane & 7) ^ ((row >> 1) & 7)) << 4)); }
    { const int row = 8 * (wid >> 1) + ((lane >> 2) & 7); const int chq = 2 * (wid & 1) + (lane >> 5);
      const int ch = 4 * chq + ((lane & 3) ^ ((row >> 2) & 3)); voffW = (unsigned)(row * 8192 + ch * 16); }
    const char* cbase = (const char*)ckv + (size_t)(256 * j) * 1024;
    const char* wkb = (const char*)wuk + h * 256;
    const char* wvb = (const char*)wuv + h * 256;
    const unsigned dstw = lds0 + wid * 1024;
#define CBUF(b, a) (((b) * 2 + (a)) * 16384)
#define WBUF(b, a) (65536 + ((b) * 2 + (a)) * 16384)
#define STAGE_C(b, a, kt) do { const char* s_ = cbase + (a) * 131072 + (kt) * 128; glds16(voffC, s_, dstw + CBUF(b, a)); glds16(voffC, s_ + 65536, dstw + CBUF(b, a) + 8192); } while (0)
#define STAGE_W(b, a, kt) do { const char* s_ = ((a) ? wvb : wkb) + (size_t)(kt) * 64 * 8192; glds16(voffW, s_, dstw + WBUF(b, a)); glds16(voffW, s_ + 32 * 8192, dstw + WBUF(b, a) + 8192); } while (0)
    int aoff[4], boff[2];
#pragma unroll
    for (int s = 0; s < 4; ++s) aoff[s] = 128 * (wr * 64 + l31) + (((2 * s + hh) ^ ((l31 >> 1) & 7)) << 4);
    { const int blk = (lane >> 4) & 1, q = (lane & 15) >> 2, p = lane & 3;
#pragma unroll
      for (int t = 0; t < 2; ++t) boff[t] = 2048 * hh + 512 * wc + 64 * (4 * t + q) + (((2 * blk + (p >> 1)) ^ ((2 * hh + t) & 3)) << 4) + 8 * (p & 1); }
#define LDA(dst, b, a) do { _Pragma("unroll") for (int m = 0; m < 2; ++m) _Pragma("unroll") for (int s = 0; s < 4; ++s) dst[m][s] = ldsr128(lds + CBUF(b, a) + m * 4096 + aoff[s]); } while (0)
#define LDB(dst, b, a) do { _Pragma("unroll") for (int s = 0; s < 4; ++s) dst[s] = cat8(vtr(lds + WBUF(b, a) + s * 4096 + boff[0]), vtr(lds + WBUF(b, a) + s * 4096 + boff[1])); } while (0)
#define MMAK(ai, At, Bt) do { __builtin_amdgcn_s_setprio(1); _Pragma("unroll") for (int s = 0; s < 4; ++s) _Pragma("unroll") for (int m = 0; m < 2; ++m) \
    accK[ai][m] = MFMA32(Bt[s], At[m][s], accK[ai][m]); __builtin_amdgcn_s_setprio(0); } while (0)
#define MMAV(ai, At, Bt) do { __builtin_amdgcn_s_setprio(1); _Pragma("unroll") for (int s = 0; s < 4; ++s) _Pragma("unroll") for (int m = 0; m < 2; ++m) \
    accV[ai][m] = MFMA32(At[m][s], Bt[s], accV[ai][m]); __builtin_amdgcn_s_setprio(0); } while (0)
    f32x16 accK[2][2] = {}, accV[2][2] = {};
    bf16x8 At[2][4], B0[4], B1[4];
    constexpr int nt = DC / 64;
    STAGE_W(0, 0, 0); STAGE_C(0, 0, 0); STAGE_W(0, 1, 0); STAGE_C(0, 1, 0);
    if (wr == 1) BAR();
    WAIT_V(4); BAR();
    STAGE_W(1, 0, 1); STAGE_C(1, 0, 1); STAGE_W(1, 1, 1);
    WAIT_V(6); BAR();
    unsigned pfd = 0;
    if (wid < 3) {
      const int ln = wid * 64 + lane;
      if (ln < 160) asm volatile("global_load_dword %0, %1, off" : "+v"(pfd) : "v"((const char*)wq + (size_t)(80 * j + (ln >> 1)) * 8192 + h * 256 + (ln & 1) * 128) : "memory");
    }
    for (int t = 0; t < nt - 2; t += 2) {
      LDB(B0, 0, 0); SBAR(); LDA(At, 0, 0); STAGE_C(1, 1, t + 1);
      WAIT_L(8); BAR(); WAIT_L(0); MMAK(0, At, B0); BAR(); SBAR();
      LDB(B1, 0, 1); STAGE_W(0, 0, t + 2);
      BAR(); WAIT_L(0); MMAV(0, At, B1); BAR();
      LDA(At, 0, 1); STAGE_C(0, 0, t + 2);
      BAR(); WAIT_L(0); MMAK(1, At, B0); BAR(); SBAR();
      STAGE_W(0, 1, t + 2);
      WAIT_V(6); BAR(); MMAV(1, At, B1); BAR();
      LDB(B0, 1, 0); SBAR(); LDA(At, 1, 0); STAGE_C(0, 1, t + 2);
      WAIT_L(8); BAR(); WAIT_L(0); MMAK(0, At, B0); BAR(); SBAR();
      LDB(B1, 1, 1); STAGE_W(1, 0, t + 3);
      BAR(); WAIT_L(0); MMAV(0, At, B1); BAR();
      LDA(At, 1, 1); STAGE_C(1, 0, t + 3);
      BAR(); WAIT_L(0); MMAK(1, At, B0); BAR(); SBAR();
      STAGE_W(1, 1, t + 3);
      WAIT_V(6); BAR(); MMAV(1, At, B1); BAR();
    }
    { LDB(B0, 0, 0); WAIT_V(0); LDA(At, 0, 0); STAGE_C(1, 1, nt - 1);
      BAR(); WAIT_L(0); MMAK(0, At, B0); BAR();
      LDB(B1, 0, 1); BAR(); WAIT_L(0); MMAV(0, At, B1); BAR();
      LDA(At, 0, 1); WAIT_V(4); BAR(); WAIT_L(0); MMAK(1, At, B0); MMAV(1, At, B1); BAR(); }
    { LDB(B0, 1, 0); LDA(At, 1, 0); WAIT_V(2); BAR(); WAIT_L(0); MMAK(0, At, B0); BAR();
      LDB(B1, 1, 1); WAIT_V(0); BAR(); WAIT_L(0); MMAV(0, At, B1); BAR();
      LDA(At, 1, 1); BAR(); WAIT_L(0); MMAK(1, At, B0); MMAV(1, At, B1); BAR(); }
    if (wr == 0) BAR();
    asm volatile("" :: "v"(pfd));
#pragma unroll
    for (int ai = 0; ai < 2; ++ai)
#pragma unroll
      for (int m = 0; m < 2; ++m)
#pragma unroll
        for (int sp = 0; sp < 2; ++sp) {
          kvp[((ai * 2 + m) * 2 + sp) * 2] = pack8(accK[ai][m], sp);
          kvp[((ai * 2 + m) * 2 + sp) * 2 + 1] = pack8(accV[ai][m], sp);
          PIN(kvp[((ai * 2 + m) * 2 + sp) * 2]); PIN(kvp[((ai * 2 + m) * 2 + sp) * 2 + 1]);
        }
#undef CBUF
#undef WBUF
#undef STAGE_C
#undef STAGE_W
#undef LDA
#undef LDB
#undef MMAK
#undef MMAV
  }
  SBAR();
  bf16x8 qr[8];
  unsigned kvmask = 0;
  const int lane = lane_id(), tid = wid * 64 + lane;
  const unsigned vl16 = lane * 16;
  {
    const int hh = lane >> 5;
    const char* Kh = ws + WS_KF + (size_t)h * 64 * 8192;
    const char* Vh = ws + WS_VF + (size_t)h * 128 * 4096;
    const unsigned vt2 = (unsigned)(wid * 2048 + lane * 16);
    const unsigned dt2 = lds0 + wid * 2048;
    const int n1 = j >= 4 ? 16 : 4 * j + 2;
#define TILE(g) ((g) < n1 ? (g) : 16 + (g) - n1)
#define DMA_T(src, ldsoff) do { unsigned keep_; asm volatile("s_mov_b32 %0, m0\n\ts_mov_b32 m0, %3\n\ts_nop 2\n\tglobal_load_lds_dwordx4 %1, %2\n\tglobal_load_lds_dwordx4 %1, %2 offset:1024\n\ts_mov_b32 m0, %0" \
    : "=&s"(keep_) : "v"(vt2), "s"(src), "s"(dt2 + (ldsoff)) : "memory"); } while (0)
    const int wr = wid >> 2, wc = wid & 3, cls = j & 3;
    const char* kst = ws + WS_KF + ((size_t)(h * 64 + 8 * j + 2 * wr) * 8 + 2 * wc) * 1024;
    const char* vst = ws + WS_VF + ((size_t)(h * 128 + 2 * (8 * j + 2 * wr)) * 4 + wc) * 1024;
#define KVST(i) gstore_sc1_c<(((i) & 1) ? (((i) >> 3) * 8 + (((i) >> 2) & 1) * 2 + (((i) >> 1) & 1)) * 4096 : (((i) >> 3) * 4 + (((i) >> 2) & 1)) * 8192 + (((i) >> 1) & 1) * 1024)>(vl16, kvp[i], ((i) & 1) ? vst : kst)
    unsigned voffW;
    { const int row = 8 * (wid >> 1) + ((lane >> 2) & 7); const int chq = 2 * (wid & 1) + (lane >> 5);
      const int ch = 4 * chq + ((lane & 3) ^ ((row >> 2) & 3)); voffW = (unsigned)(row * 8192 + ch * 16); }
    const char* wqb = (const char*)wq + h * 256;
    const unsigned dstw = lds0 + L_Q + wid * 1024;
#define STAGE_Q1(c, i) glds16_c<(c) * 64 * 8192 + (i) * 32 * 8192>(voffW, wqb, dstw + ((c) & 3) * 16384 + (i) * 8192)
    const char* xb = ws + WS_XNF + (size_t)(8 * j + wid) * 40 * 1024;
    int boff[2];
    { const int blk = (lane >> 4) & 1, q = (lane & 15) >> 2, p = lane & 3;
#pragma unroll
      for (int t = 0; t < 2; ++t) boff[t] = L_Q + 2048 * hh + 64 * (4 * t + q) + (((2 * blk + (p >> 1)) ^ ((2 * hh + t) & 3)) << 4) + 8 * (p & 1); }
    u32x4 xf[3][4];
    f32x16 acc[4] = {};
    bf16x8 aq[6];
    ldscp bp0 = lds + boff[0], bp1 = lds + boff[1];
#define ARD(u) aq[(u) % 6] = cat8(vtr(bp0 + (((u) >> 4) & 3) * 16384 + (((u) >> 2) & 3) * 4096 + ((u) & 3) * 512), vtr(bp1 + (((u) >> 4) & 3) * 16384 + (((u) >> 2) & 3) * 4096 + ((u) & 3) * 512))
    GLOAD4(xf[0], vl16, xb); STAGE_Q1(0, 0); STAGE_Q1(0, 1);
    STAGE_Q1(1, 0); STAGE_Q1(1, 1);
    GLOAD4(xf[1], vl16, xb + 4096); STAGE_Q1(2, 0); STAGE_Q1(2, 1);
    WAIT_V(8); BAR();
#pragma unroll
    for (int u = 0; u < 5; ++u) ARD(u);
    static_for<10>([&](auto cc_) {
      constexpr int c = decltype(cc_)::value;
#define QW(K) WAITVN(qproj_allowed(c, K))
      if (c == 9 && wid == 0) {
        const unsigned fv = __hip_atomic_load(flags + FL_KV + 16 * (8 * h + (lane & 7)), __ATOMIC_RELAXED, __HIP_MEMORY_SCOPE_AGENT);
        kvmask = (unsigned)__builtin_amdgcn_ballot_w64(fv != 0u) & 0xffu;
        if (!(kvmask & 1u)) { wait_word(flags + FL_KV + 16 * (8 * h), 1u, flags + FL_TMO); kvmask |= 1u; }
        if (j == 0 && !(kvmask & 16u)) { wait_word(flags + FL_KV + 16 * (8 * h + 4), 1u, flags + FL_TMO); kvmask |= 16u; }
        ACQUIRE(); WAIT_V(0);
      }
      if (cls == 0) QW(0); else if (cls == 1) QW(1); else if (cls == 2) QW(2); else QW(3);
      asm volatile("" : "+v"(xf[c % 3][0]), "+v"(xf[c % 3][1]), "+v"(xf[c % 3][2]), "+v"(xf[c % 3][3]) :: "memory");
#undef QW
      SBAR(); BAR();
      if (tid == 0 && ((cls == 0 && c == 5) || (cls == 1 && c == 7)))
        __hip_atomic_store(flags + FL_KV + 16 * (8 * h + j), 1u, __ATOMIC_RELAXED, __HIP_MEMORY_SCOPE_AGENT);
      SBAR();
      const char* xn_ = xb + (c + 2) * 4096;
      static_for<16>([&](auto ii_) {
        constexpr int i = decltype(ii_)::value;
        constexpr int u = 16 * c + i;
        acc[i & 3] = MFMA32(aq[u % 6], __builtin_bit_cast(bf16x8, xf[c % 3][i >> 2]), acc[i & 3]);
        if constexpr (u + 5 < 160) ARD(u + 5);
        if (c + 2 < 10) {
          if (i == 1) GLOAD1(xf[(c + 2) % 3][0], vl16, xn_, 0);
          if (i == 3) GLOAD1(xf[(c + 2) % 3][1], vl16, xn_, 1024);
          if (i == 5) GLOAD1(xf[(c + 2) % 3][2], vl16, xn_, 2048);
          if (i == 7) GLOAD1(xf[(c + 2) % 3][3], vl16, xn_, 3072);
        }
        if constexpr (c + 3 < 10) { if constexpr (i == 8) STAGE_Q1(c + 3, 0); if constexpr (i == 9) STAGE_Q1(c + 3, 1); }
        if constexpr (i >= 10) {
          static_for<4>([&](auto kk_) {
            constexpr int k = decltype(kk_)::value;
            constexpr int rel = c - qproj_st0(k);
            if constexpr (rel >= 0 && rel <= 2) {
              constexpr int cnt = rel == 0 ? 6 : 5, base = rel == 0 ? 0 : (rel == 1 ? 6 : 11);
              if constexpr (i - 10 < cnt) { if (cls == k) KVST(base + i - 10); }
            }
          });
        }
        if (c == 9) {
          if (i == 0) DMA_T(Kh + (size_t)TILE(0) * 16384, L_K);
          if (i == 2) DMA_T(Vh + (size_t)TILE(0) * 16384, L_V);
          if (i == 4) DMA_T(Kh + (size_t)TILE(1) * 16384, L_K + 16384);
          if (i == 6) DMA_T(Vh + (size_t)TILE(1) * 16384, L_V + 16384);
          if (i == 8) DMA_T(Kh + (size_t)TILE(2) * 16384, L_K + 32768);
          if (i == 9) DMA_T(Kh + (size_t)TILE(3) * 16384, L_K + 49152);
        }
        SBAR();
      });
    });
#pragma unroll
    for (int dt = 0; dt < 4; ++dt) { acc[dt] = acc[dt] * QSCALE; qr[2 * dt] = pack8(acc[dt], 0); qr[2 * dt + 1] = pack8(acc[dt], 1); }
    if (cls == 3) WAIT_V(15); else WAIT_V(10);
    BAR();
    { const char* qst = ws + WS_QF + (size_t)(h * 64 + 8 * j + wid) * 8192;
      static_for<8>([&](auto ss_) { constexpr int s = decltype(ss_)::value; gstore_sc1_c<s * 1024>(vl16, qr[s], qst); }); }
#undef KVST
#undef STAGE_Q1
#undef ARD
#undef TILE
#undef DMA_T
  }
  const PfW pfw = {(const char*)wo, (const char*)w1, (const char*)w2};
  if (wid < 4) attn_main<0>(lds, lds0, ws, flags, h, j, wid, lane, tid, qr, kvmask, pfw);
  else attn_main<1>(lds, lds0, ws, flags, h, j, wid, lane, tid, qr, kvmask, pfw);
  WAIT_V(0); WAIT_L(0); BAR();
  if (tid == 0) __hip_atomic_fetch_add(flags + FL_C + 16 * ((7 - j) * 4 + (h >> 3)), 1u, __ATOMIC_RELAXED, __HIP_MEMORY_SCOPE_AGENT);
  tail_phase(smem, x, wo, w1, b1, w2, b2, ws, out, wid);
}

extern "C" void kernel_launch(void* const* d_in, const int* in_sizes, int n_in, void* d_out, int out_size, void* d_ws, size_t ws_size,
                              hipStream_t stream) {
  const u16* x = (const u16*)d_in[0];
  const u16* ckv = (const u16*)d_in[1];
  const u16* wuk = (const u16*)d_in[3];
  const u16* wuv = (const u16*)d_in[4];
  char* ws = (char*)d_ws;
  const u16* wq = (const u16*)d_in[2];
  const u16* wo = (const u16*)d_in[5];
  const u16* w1 = (const u16*)d_in[7];
  const u16* b1 = (const u16*)d_in[8];
  const u16* w2 = (const u16*)d_in[9];
  const u16* b2 = (const u16*)d_in[10];
  (void)hipFuncSetAttribute((const void*)kF1, hipFuncAttributeMaxDynamicSharedMemorySize, F1_LDS);
  hipLaunchKernelGGL(k0_ln, dim3(256), dim3(512), 0, stream, x, (u16*)(ws + WS_XNF), (unsigned*)(ws + WS_FLAGS));
  hipLaunchKernelGGL(kF1, dim3(256), dim3(512), F1_LDS, stream, ckv, wuk, wuv, wq, wo, w1, w2, x, b1, b2, ws, (u16*)d_out);
}
```

```cpp
#include <hip/hip_runtime.h>
#include <stdint.h>

#define LAS __attribute__((address_space(3)))
#define DI __device__ __forceinline__
typedef unsigned short u16;
typedef short bf16x8 __attribute__((ext_vector_type(8)));
typedef short s16x4 __attribute__((ext_vector_type(4)));
typedef float f32x2 __attribute__((ext_vector_type(2)));
typedef float f32x4 __attribute__((ext_vector_type(4)));
typedef float f32x16 __attribute__((ext_vector_type(16)));
typedef unsigned u32x2 __attribute__((ext_vector_type(2)));
typedef unsigned u32x4 __attribute__((ext_vector_type(4)));
typedef __bf16 bf16x2_t __attribute__((ext_vector_type(2)));
typedef LAS unsigned char* ldsp;
typedef LAS const unsigned char* ldscp;

constexpr int SEQ = 2048, DM = 640, DC = 512, NH = 32, HD = 128, FF = 1728;
constexpr float LN_EPS = 1e-5f;

constexpr size_t WS_FLAGS = 0;
constexpr size_t WS_XNF = 131072;
constexpr size_t WS_KF = WS_XNF + (size_t)SEQ * DM * 2;
constexpr size_t WS_VF = WS_KF + (size_t)NH * SEQ * HD * 2;
constexpr size_t WS_CTX = WS_VF + (size_t)NH * SEQ * HD * 2;
constexpr size_t WS_SLAB = WS_CTX + (size_t)NH * SEQ * HD * 2;
constexpr size_t WS_H = WS_SLAB + (size_t)4 * SEQ * DM * 4;
constexpr size_t WS_HNF = WS_H + (size_t)SEQ * DM * 2;
constexpr size_t WS_UF = WS_HNF + (size_t)SEQ * DM * 2;
constexpr size_t WS_QF = WS_UF + (size_t)DM * FF * 2;
constexpr size_t WS_PF = WS_QF + (size_t)NH * SEQ * HD * 2;
constexpr size_t WS_PS = WS_PF + (size_t)NH * SEQ * HD * 2;
constexpr size_t WS_END = WS_PS + (size_t)NH * 8 * 8 * 64 * 8;
constexpr int FL_TMO = 0;
constexpr int FL_KV = 24576;
constexpr int FL_Q = 4096;
constexpr int FL_P = 4096 + 8192;
constexpr int FL_WORDS = 32768;

#define SBAR() __builtin_amdgcn_sched_barrier(0)
#define BAR() asm volatile("s_barrier" ::: "memory")
#define WAIT_V(n) asm volatile("s_waitcnt vmcnt(" #n ")" ::: "memory")
#define WAIT_L(n) asm volatile("s_waitcnt lgkmcnt(" #n ")" ::: "memory")
#define MFMA32(a, b, c) __builtin_amdgcn_mfma_f32_32x32x16_bf16((a), (b), (c), 0, 0, 0)
#define MFMA16(a, b, c) __builtin_amdgcn_mfma_f32_16x16x32_bf16((a), (b), (c), 0, 0, 0)

DI unsigned cvtpk(float lo, float hi) { f32x2 v = {lo, hi}; bf16x2_t b = __builtin_convertvector(v, bf16x2_t); return __builtin_bit_cast(unsigned, b); }
DI float bflo(unsigned u) { return __uint_as_float(u << 16); }
DI float bfhi(unsigned u) { return __uint_as_float(u & 0xffff0000u); }
DI unsigned rfl(unsigned x) { return __builtin_amdgcn_readfirstlane(x); }
DI void glds16(unsigned voff, const void* sbase, unsigned lds_dst) {
  unsigned keep;
  asm volatile("s_mov_b32 %0, m0\n\ts_mov_b32 m0, %3\n\ts_nop 2\n\tglobal_load_lds_dwordx4 %1, %2\n\ts_mov_b32 m0, %0"
               : "=&s"(keep) : "v"(voff), "s"(sbase), "s"(lds_dst) : "memory");
}
DI void glds16x4(unsigned voff, const void* sbase, unsigned lds_dst) {
  unsigned keep;
  asm volatile("s_mov_b32 %0, m0\n\ts_mov_b32 m0, %3\n\ts_nop 2\n\tglobal_load_lds_dwordx4 %1, %2\n\tglobal_load_lds_dwordx4 %1, %2 offset:1024\n\t"
               "global_load_lds_dwordx4 %1, %2 offset:2048\n\tglobal_load_lds_dwordx4 %1, %2 offset:3072\n\ts_mov_b32 m0, %0"
               : "=&s"(keep) : "v"(voff), "s"(sbase), "s"(lds_dst) : "memory");
}
typedef short v4i16_t __attribute__((ext_vector_type(4)));
DI s16x4 vtr(ldscp p) { return __builtin_bit_cast(s16x4, __builtin_amdgcn_ds_read_tr16_b64_v4i16((LAS v4i16_t*)p)); }
DI bf16x8 cat8(s16x4 lo, s16x4 hi) { return __builtin_shufflevector(lo, hi, 0, 1, 2, 3, 4, 5, 6, 7); }
DI bf16x8 ldsr128(ldscp p) { return *(const LAS bf16x8*)p; }
DI bf16x8 pack8(const f32x16& x, int s) {
  u32x4 w = {cvtpk(x[8 * s], x[8 * s + 1]), cvtpk(x[8 * s + 2], x[8 * s + 3]), cvtpk(x[8 * s + 4], x[8 * s + 5]), cvtpk(x[8 * s + 6], x[8 * s + 7])};
  return __builtin_bit_cast(bf16x8, w);
}
DI float wave_sum(float v) {
#pragma unroll
  for (int o = 32; o >= 1; o >>= 1) v += __shfl_xor(v, o);
  return v;
}

__global__ __launch_bounds__(512) void k0_ln(const u16* __restrict__ x, u16* __restrict__ xnf, unsigned* __restrict__ flags) {
  const int w = threadIdx.x >> 6, lane = threadIdx.x & 63;
  const int q = blockIdx.x * 8 + w;
  if (threadIdx.x < 128) flags[blockIdx.x * 128 + threadIdx.x] = 0u;
  const u32x4* row = (const u32x4*)(x + (size_t)q * DM);
  u32x4 c0 = row[lane];
  u32x4 c1 = {0u, 0u, 0u, 0u};
  if (lane < 16) c1 = row[64 + lane];
  float v[16];
#pragma unroll
  for (int i = 0; i < 4; ++i) { v[2 * i] = bflo(c0[i]); v[2 * i + 1] = bfhi(c0[i]); v[8 + 2 * i] = bflo(c1[i]); v[8 + 2 * i + 1] = bfhi(c1[i]); }
  float s = 0.f;
#pragma unroll
  for (int i = 0; i < 16; ++i) s += v[i];
  const float mean = wave_sum(s) * (1.0f / DM);
  float ss = 0.f;
#pragma unroll
  for (int i = 0; i < 8; ++i) { const float d = v[i] - mean; ss += d * d; }
  if (lane < 16) {
#pragma unroll
    for (int i = 8; i < 16; ++i) { const float d = v[i] - mean; ss += d * d; }
  }
  const float rstd = 1.0f / sqrtf(wave_sum(ss) * (1.0f / DM) + LN_EPS);
  u32x4 o0, o1;
#pragma unroll
  for (int i = 0; i < 4; ++i) { o0[i] = cvtpk((v[2 * i] - mean) * rstd, (v[2 * i + 1] - mean) * rstd); o1[i] = cvtpk((v[8 + 2 * i] - mean) * rstd, (v[8 + 2 * i + 1] - mean) * rstd); }
  u32x4* dst = (u32x4*)xnf + (size_t)(q >> 5) * 40 * 64 + (q & 31);
  dst[(lane >> 1) * 64 + (lane & 1) * 32] = o0;
  if (lane < 16) dst[((64 + lane) >> 1) * 64 + (lane & 1) * 32] = o1;
}

constexpr int F1_LDS = 163840;
constexpr int L_K = 0, L_V = 65536, L_Q = 98304;
constexpr float QSCALE = 0.08838834764831845f * 1.4426950408889634f;
constexpr float THR = 16.f;
#define PIN(x) asm volatile("" : "+v"(x))
#define EX2(v) __builtin_amdgcn_exp2f(v)
#define MEMFENCE() asm volatile("" ::: "memory")
DI float max3f(float a, float b, float c) { return __builtin_fmaxf(__builtin_fmaxf(a, b), c); }
DI float xhalf_max(float m) { auto rr = __builtin_amdgcn_permlane32_swap(__float_as_uint(m), __float_as_uint(m), false, false); return __builtin_fmaxf(__uint_as_float(rr[0]), __uint_as_float(rr[1])); }
DI float xhalf_sum(float m) { auto rr = __builtin_amdgcn_permlane32_swap(__float_as_uint(m), __float_as_uint(m), false, false); return __uint_as_float(rr[0]) + __uint_as_float(rr[1]); }
DI float max16(const f32x16& p) {
  float a = max3f(p[0], p[1], p[2]), b = max3f(p[3], p[4], p[5]);
  a = max3f(a, p[6], p[7]); b = max3f(b, p[8], p[9]); a = max3f(a, p[10], p[11]); b = max3f(b, p[12], p[13]); a = max3f(a, p[14], p[15]);
  return __builtin_fmaxf(a, b);
}
DI void cmask(f32x16& p0, f32x16& p1, int dq) {
#pragma unroll
  for (int r = 0; r < 16; ++r) { const int c = (r & 3) + 8 * (r >> 2); if (c > dq) p0[r] = -__builtin_inff(); if (c + 32 > dq) p1[r] = -__builtin_inff(); }
}
#define GLOAD4(d, voff, sbase) asm volatile("s_nop 4\n\tglobal_load_dwordx4 %0, %4, %5\n\tglobal_load_dwordx4 %1, %4, %5 offset:1024\n\tglobal_load_dwordx4 %2, %4, %5 offset:2048\n\tglobal_load_dwordx4 %3, %4, %5 offset:3072" \
    : "=&v"(d[0]), "=&v"(d[1]), "=&v"(d[2]), "=&v"(d[3]) : "v"(voff), "s"(sbase) : "memory")
#define GLOAD1(d, voff, sbase, OFF) asm volatile("s_nop 4\n\tglobal_load_dwordx4 %0, %1, %2 offset:" #OFF : "=&v"(d) : "v"(voff), "s"(sbase) : "memory")
#define GLOAD1_SC1(d, voff, sbase, OFF) asm volatile("s_nop 4\n\tglobal_load_dwordx4 %0, %1, %2 offset:" #OFF " sc1" : "=&v"(d) : "v"(voff), "s"(sbase) : "memory")
#define WAITV_TIE4(n, d) asm volatile("s_waitcnt vmcnt(" #n ")" : "+v"(d[0]), "+v"(d[1]), "+v"(d[2]), "+v"(d[3]) :: "memory")
DI void gstore_sc1(unsigned voff, const bf16x8& v, const void* sbase) {
  asm volatile("s_nop 4\n\tglobal_store_dwordx4 %0, %1, %2 sc1\n\ts_nop 1" :: "v"(voff), "v"(v), "s"(sbase) : "memory");
}
template <int OFFS> DI void gstore_sc1_c(unsigned voff, const bf16x8& v, const void* sbase) {
  unsigned t;
  asm volatile("v_add_u32 %0, %4, %1\n\ts_nop 3\n\tglobal_store_dwordx4 %0, %2, %3 sc1\n\ts_nop 1" : "=&v"(t) : "v"(voff), "v"(v), "s"(sbase), "i"(OFFS) : "memory");
}
template <int OFFS> DI void glds16_c(unsigned voff, const void* sbase, unsigned lds_dst) {
  unsigned keep, t;
  asm volatile("s_mov_b32 %0, m0\n\ts_mov_b32 m0, %4\n\tv_add_u32 %1, %5, %2\n\ts_nop 1\n\tglobal_load_lds_dwordx4 %1, %3\n\ts_mov_b32 m0, %0"
               : "=&s"(keep), "=&v"(t) : "v"(voff), "s"(sbase), "s"(lds_dst), "i"(OFFS) : "memory");
}
#define GLDS_OFF(voff, sbase, ldst, OFF) do { unsigned keep_; asm volatile("s_mov_b32 %0, m0\n\ts_mov_b32 m0, %3\n\ts_nop 2\n\tglobal_load_lds_dwordx4 %1, %2 offset:" #OFF "\n\ts_mov_b32 m0, %0" \
    : "=&s"(keep_) : "v"(voff), "s"(sbase), "s"(ldst) : "memory"); } while (0)
#define WAITVN(n) asm volatile("s_waitcnt vmcnt(%0)" :: "i"(n) : "memory")
DI const char* upt(const char* p) {
  const unsigned long long v = (unsigned long long)p;
  return (const char*)(((unsigned long long)rfl((unsigned)(v >> 32)) << 32) | rfl((unsigned)v));
}
DI void wait_word(unsigned* word, unsigned want, unsigned* tmo) {
  for (unsigned spins = 0;; ++spins) {
    if ((unsigned)rfl(__hip_atomic_load(word, __ATOMIC_RELAXED, __HIP_MEMORY_SCOPE_AGENT)) >= want) break;
    if (spins > (1u << 16)) { __hip_atomic_store(tmo, 1u, __ATOMIC_RELAXED, __HIP_MEMORY_SCOPE_AGENT); break; }
    __builtin_amdgcn_s_sleep(1);
  }
}
#define ACQUIRE() __builtin_amdgcn_fence(__ATOMIC_ACQUIRE, "agent")

template <int V> struct IC { static constexpr int value = V; };
template <int N, int I = 0, class F> DI void static_for(F&& f) { if constexpr (I < N) { f(IC<I>{}); static_for<N, I + 1>(f); } }
DI int lane_id() { unsigned l; asm volatile("v_mbcnt_lo_u32_b32 %0, -1, 0\n\tv_mbcnt_hi_u32_b32 %0, -1, %0" : "=v"(l)); return (int)l; }
struct PfW { const char* wo; const char* w1; const char* w2; };
template <int GRP>
DI void attn_main(ldsp lds, unsigned lds0, char* __restrict__ ws, unsigned* flags, int h, int j, int wid, int lane, int tid, bf16x8 (&qr)[8], unsigned kvmask, const PfW& pfw) {
  const int l31 = lane & 31, hh = lane >> 5;
  const unsigned vl16 = lane * 16;
  const char* Kh = ws + WS_KF + (size_t)h * 64 * 8192;
  const char* Vh = ws + WS_VF + (size_t)h * 128 * 4096;
  const unsigned vt2 = (unsigned)(wid * 2048 + lane * 16);
  const unsigned dt2 = lds0 + wid * 2048;
  const int n1 = j >= 4 ? 16 : 4 * j + 2;
  const int t2 = j >= 4 ? 4 * (8 - j) - 2 : 16;
  const int jp = 7 - j;
#define TILE(g) ((g) < n1 ? (g) : ((g) < 18 ? t2 + (g) - n1 : 0))
#define KSRC(g) (Kh + (size_t)TILE(g) * 16384)
#define VSRC(g) (Vh + (size_t)TILE(g) * 16384)
#define GLDS_T(src, dst, OFF) do { unsigned keep_; asm volatile("s_mov_b32 %0, m0\n\ts_mov_b32 m0, %3\n\ts_nop 2\n\tglobal_load_lds_dwordx4 %1, %2 offset:" #OFF " sc1\n\ts_mov_b32 m0, %0" \
    : "=&s"(keep_) : "v"(vt2), "s"(src), "s"(dst) : "memory"); } while (0)
  const unsigned stoff = lane < 8 ? (unsigned)(FL_KV + 16 * (8 * h + lane)) * 4u : (lane == 8 ? (unsigned)(FL_Q + 32 * (8 * h + jp)) * 4u : (unsigned)(FL_P + 32 * (8 * h + jp)) * 4u);
  unsigned fv = 0;
#define ST_ISSUE() do { if (wid == 0 && kvmask != 0x3ffu) asm volatile("s_nop 4\n\tglobal_load_dword %0, %1, %2 sc1" : "+v"(fv) : "v"(stoff), "s"(flags) : "memory"); } while (0)
#define ST_READ(N) do { if (wid == 0 && kvmask != 0x3ffu) { asm volatile("s_waitcnt vmcnt(" #N ")" : "+v"(fv) :: "memory"); kvmask |= (unsigned)__builtin_amdgcn_ballot_w64(fv != 0u) & 0x3ffu; } } while (0)
#define ENSURE_KV(g) do { if (wid == 0) { const int bk_ = TILE(g) >> 2; if (!((kvmask >> bk_) & 1u)) { wait_word(flags + FL_KV + 16 * (8 * h + bk_), 1u, flags + FL_TMO); kvmask |= 1u << bk_; } } } while (0)
  const int rdoff = lane * 16;
  bf16x8 kq[4], vq[4], pw[4];
  f32x16 C0, C1, o[4], negm;
  float mhat, lsum;
  const f32x16 z = {};
  unsigned pfd = 0;
#pragma unroll
  for (int i = 0; i < 4; ++i) kq[i] = ldsr128(lds + L_K + rdoff + i * 1024);
  {
    ST_ISSUE();
    ldscp kb = lds + L_K + rdoff;
#pragma unroll
    for (int u = 0; u < 16; ++u) {
      if (u < 8) C0 = MFMA32(kq[u & 3], qr[u & 7], u == 0 ? z : C0); else C1 = MFMA32(kq[u & 3], qr[u & 7], u == 8 ? z : C1);
      if (u + 4 < 16) kq[u & 3] = ldsr128(kb + (u + 4) * 1024);
      if (GRP == 1 && u == 11) {
        SBAR(); WAIT_V(0); WAIT_L(0); BAR();
        GLDS_T(VSRC(2), dt2 + L_V + 32768, 0); GLDS_T(VSRC(2), dt2 + L_V + 32768, 1024);
        GLDS_T(KSRC(4), dt2 + L_K, 0);
      }
      SBAR();
    }
    if (j == 0) cmask(C0, C1, 32 * wid + l31 - 4 * hh);
    float rm = xhalf_max(__builtin_fmaxf(max16(C0), max16(C1)));
    mhat = __builtin_fmaxf(rm, -1e30f);
#pragma unroll
    for (int r = 0; r < 16; ++r) { C0[r] -= mhat; C1[r] -= mhat; negm[r] = -mhat; }
    PIN(negm);
    if (GRP == 0) {
      SBAR(); WAIT_V(0); ST_READ(0); ENSURE_KV(2); ENSURE_KV(4);
      WAIT_L(0); BAR();
      if (tid == 0) {
        __hip_atomic_store(flags + FL_Q + 32 * (8 * h + j), 1u, __ATOMIC_RELAXED, __HIP_MEMORY_SCOPE_AGENT);
        if ((j & 3) >= 2) __hip_atomic_store(flags + FL_KV + 16 * (8 * h + j), 1u, __ATOMIC_RELAXED, __HIP_MEMORY_SCOPE_AGENT);
      }
      GLDS_T(VSRC(2), dt2 + L_V + 32768, 0);
    } else {
      GLDS_T(KSRC(4), dt2 + L_K, 1024);
    }
#pragma unroll
    for (int i = 0; i < 4; ++i) kq[i] = ldsr128(lds + L_K + 16384 + rdoff + i * 1024);
    float s0 = 0.f;
#pragma unroll
    for (int r = 0; r < 16; ++r) { C0[r] = EX2(C0[r]); s0 += C0[r]; }
#pragma unroll
    for (int r = 0; r < 8; ++r) C1[r] = EX2(C1[r]);
    lsum = s0;
    pw[0] = pack8(C0, 0); pw[1] = pack8(C0, 1);
#pragma unroll
    for (int d = 0; d < 4; ++d) o[d] = z;
  }
  for (int g = 1; g < 18; ++g) {
    const bool seam = (g == n1);
    const int X = g < n1 ? j : jp;
    const int dd = TILE(g) - 4 * X;
    ldscp kb = lds + L_K + (g & 3) * 16384 + rdoff;
    ldscp vb = lds + L_V + ((g - 1) & 3) * 16384 + rdoff;
    ldscp kn = lds + L_K + ((g + 1) & 3) * 16384 + rdoff;
    const char* vs_n = VSRC(g + 2); const char* ks_n = KSRC(g + 4);
    const char* vs_p = VSRC(g + 1); const char* ks_p = KSRC(g + 3);
    const unsigned vd_n = dt2 + L_V + ((g + 2) & 3) * 16384, kd_n = dt2 + L_K + (g & 3) * 16384;
    const unsigned vd_p = dt2 + L_V + ((g + 1) & 3) * 16384, kd_p = dt2 + L_K + ((g - 1) & 3) * 16384;
    const bool gq = (GRP == 0) || g > 1;
    float lsum1 = 0.f, mhat1 = 0.f;
    if (seam) {
      mhat1 = mhat; mhat = 0.f;
#pragma unroll
      for (int r = 0; r < 16; ++r) negm[r] = 0.f;
      PIN(negm);
    }
    if (g >= 2 && g < 12 && wid == ((g - 2) & 7)) {
      const unsigned lc = (unsigned)(g - 2) * 64u + lane, gid = blockIdx.x * 592u + lc;
      const char* p = gid < 40960u ? pfw.wo + (size_t)gid * 128 : (gid < 96256u ? pfw.w1 + (size_t)(gid - 40960u) * 128 : pfw.w2 + (size_t)(gid - 96256u) * 128);
      if (lc < 592u && gid < 151552u) asm volatile("global_load_dword %0, %1, off" : "+v"(pfd) : "v"(p) : "memory");
    }
    ST_ISSUE();
    float sa = 0.f, sb = 0.f;
    SBAR();
#define KRD(u) kq[(u) & 3] = ldsr128(kb + ((u) + 4) * 1024)
#define VRD(v) vq[(v) & 3] = ldsr128(vb + (v) * 1024)
#define KPRE(i) kq[i] = ldsr128(kn + (i) * 1024)
#define BARRIER_G() do { if (GRP == 0) { ST_READ(3); ENSURE_KV(g + 4); if (wid == 0 && g == n1 - 2 && j != 0 && !(kvmask & 256u)) wait_word(flags + FL_Q + 32 * (8 * h + jp), 1u, flags + FL_TMO); \
        if (g == 17 && wid == 0 && lane == 0) *(LAS unsigned*)(lds + 131072) = kvmask; } \
      if (g < 17) WAIT_V(6); else WAIT_V(0); WAIT_L(0); BAR(); SBAR(); \
      if (GRP == 0 && (g == n1 + 2 || (g == 17 && n1 + 2 > 17)) && tid == 0) \
        __hip_atomic_store(flags + FL_P + 32 * (8 * h + j), 1u, __ATOMIC_RELAXED, __HIP_MEMORY_SCOPE_AGENT); } while (0)
    C0 = MFMA32(kq[0], qr[0], negm); KRD(0); C1[8] = EX2(C1[8]); sa += C1[0]; sb += C1[1]; sa += C1[2]; sb += C1[3]; PIN(C1); PIN(sa); PIN(sb); SBAR();
    C0 = MFMA32(kq[1], qr[1], C0); KRD(1); C1[9] = EX2(C1[9]); sa += C1[4]; sb += C1[5]; sa += C1[6]; sb += C1[7]; PIN(C1); PIN(sa); PIN(sb); SBAR();
    C0 = MFMA32(kq[2], qr[2], C0); KRD(2); if (GRP == 0) GLDS_T(vs_p, vd_p, 1024); else if (gq) GLDS_T(ks_p, kd_p, 1024);
        C1[10] = EX2(C1[10]); pw[2] = pack8(C1, 0); PIN(C1); PIN(pw[2]); SBAR();
    C0 = MFMA32(kq[3], qr[3], C0); KRD(3); C1[11] = EX2(C1[11]); C1[12] = EX2(C1[12]); sa += C1[8]; sb += C1[9]; PIN(C1); PIN(sa); PIN(sb); SBAR();
    C0 = MFMA32(kq[0], qr[4], C0); KRD(4); C1[13] = EX2(C1[13]); C1[14] = EX2(C1[14]); sa += C1[10]; sb += C1[11]; PIN(C1); PIN(sa); PIN(sb); SBAR();
    C0 = MFMA32(kq[1], qr[5], C0); KRD(5); C1[15] = EX2(C1[15]); sa += C1[12]; sb += C1[13]; sa += C1[14]; PIN(C1); PIN(sa); PIN(sb); SBAR();
    C0 = MFMA32(kq[2], qr[6], C0); KRD(6); sb += C1[15]; pw[3] = pack8(C1, 1); PIN(pw[3]); PIN(sb); SBAR();
    C0 = MFMA32(kq[3], qr[7], C0); KRD(7); lsum += sa + sb; PIN(lsum); SBAR();
    C1 = MFMA32(kq[0], qr[0], negm); KRD(8); if (GRP == 0) GLDS_T(ks_p, kd_p, 0); SBAR();
    C1 = MFMA32(kq[1], qr[1], C1); KRD(9); SBAR();
    C1 = MFMA32(kq[2], qr[2], C1); KRD(10); SBAR();
    float mxa, mxb;
    C1 = MFMA32(kq[3], qr[3], C1); KRD(11); mxa = max3f(C0[0], C0[1], C0[2]); mxb = max3f(C0[3], C0[4], C0[5]); PIN(mxa); PIN(mxb); SBAR();
    if (GRP == 1) BARRIER_G();
    C1 = MFMA32(kq[0], qr[4], C1); VRD(0); if (GRP == 1) GLDS_T(vs_n, vd_n, 0); mxa = max3f(mxa, C0[6], C0[7]); mxb = max3f(mxb, C0[8], C0[9]); PIN(mxa); PIN(mxb); SBAR();
    C1 = MFMA32(kq[1], qr[5], C1); VRD(1); mxa = max3f(mxa, C0[10], C0[11]); mxb = max3f(mxb, C0[12], C0[13]); PIN(mxa); PIN(mxb); SBAR();
    C1 = MFMA32(kq[2], qr[6], C1); VRD(2); mxa = max3f(mxa, C0[14], C0[15]); mxa = __builtin_fmaxf(mxa, mxb); PIN(mxa); SBAR();
    C1 = MFMA32(kq[3], qr[7], C1); VRD(3); SBAR();
    if (g == n1 - 1) {
      if (j == 0) wait_word(flags + FL_Q + 32 * (8 * h + jp), 1u, flags + FL_TMO);
      const char* qsrc = ws + WS_QF + (size_t)(h * 64 + 8 * jp + wid) * 8192;
      GLOAD1_SC1(qr[0], vl16, qsrc, 0); GLOAD1_SC1(qr[1], vl16, qsrc, 1024); GLOAD1_SC1(qr[2], vl16, qsrc, 2048); GLOAD1_SC1(qr[3], vl16, qsrc, 3072);
      GLOAD1_SC1(qr[4], vl16, qsrc + 4096, 0); GLOAD1_SC1(qr[5], vl16, qsrc + 4096, 1024); GLOAD1_SC1(qr[6], vl16, qsrc + 4096, 2048); GLOAD1_SC1(qr[7], vl16, qsrc + 4096, 3072);
    }
    SBAR();
    o[0] = MFMA32(vq[0], pw[0], o[0]); VRD(4); if (GRP == 1) GLDS_T(vs_n, vd_n, 1024); SBAR();
    o[1] = MFMA32(vq[1], pw[0], o[1]); VRD(5); SBAR();
    float mxc, mxd;
    o[2] = MFMA32(vq[2], pw[0], o[2]); VRD(6); if (GRP == 0) GLDS_T(ks_p, kd_p, 1024);
        mxc = max3f(C1[0], C1[1], C1[2]); mxd = max3f(C1[3], C1[4], C1[5]); mxc = max3f(mxc, C1[6], C1[7]); mxd = max3f(mxd, C1[8], C1[9]); PIN(mxc); PIN(mxd); SBAR();
    o[3] = MFMA32(vq[3], pw[0], o[3]); VRD(7); mxc = max3f(mxc, C1[10], C1[11]); mxd = max3f(mxd, C1[12], C1[13]); mxc = max3f(mxc, C1[14], C1[15]); mxa = max3f(mxa, mxc, mxd); PIN(mxa); SBAR();
    o[0] = MFMA32(vq[0], pw[1], o[0]); VRD(8);
    float rm = mxa;
    MEMFENCE(); SBAR();
    if (dd >= 0) { cmask(C0, C1, 32 * wid + l31 - 64 * dd - 4 * hh); rm = __builtin_fmaxf(max16(C0), max16(C1)); }
    rm = xhalf_max(rm);
    float fsc = 1.f; bool resc = false;
    if (__builtin_expect(seam || __any(rm > THR), 0)) {
      const float dl = seam ? __builtin_fmaxf(rm, -1e30f) : __builtin_fmaxf(rm, 0.f);
      mhat += dl;
#pragma unroll
      for (int r = 0; r < 16; ++r) { C0[r] -= dl; C1[r] -= dl; negm[r] = -mhat; }
      PIN(negm);
      if (seam) { lsum1 = lsum; lsum = 0.f; }
      else { fsc = EX2(-dl); lsum *= fsc; resc = true; }
    }
    SBAR();
    sa = 0.f; sb = 0.f;
    o[1] = MFMA32(vq[1], pw[1], o[1]); VRD(9); C0[0] = EX2(C0[0]); C0[1] = EX2(C0[1]); C0[2] = EX2(C0[2]); PIN(C0); SBAR();
    o[2] = MFMA32(vq[2], pw[1], o[2]); VRD(10); C0[3] = EX2(C0[3]); C0[4] = EX2(C0[4]); sa += C0[0]; sb += C0[1]; PIN(C0); PIN(sa); PIN(sb); SBAR();
    o[3] = MFMA32(vq[3], pw[1], o[3]); VRD(11); C0[5] = EX2(C0[5]); C0[6] = EX2(C0[6]); sa += C0[2]; sb += C0[3]; PIN(C0); PIN(sa); PIN(sb); SBAR();
    o[0] = MFMA32(vq[0], pw[2], o[0]); VRD(12); if (GRP == 1) GLDS_T(ks_n, kd_n, 0); C0[7] = EX2(C0[7]); C0[8] = EX2(C0[8]); sa += C0[4]; sb += C0[5]; PIN(C0); PIN(sa); PIN(sb); SBAR();
    o[1] = MFMA32(vq[1], pw[2], o[1]); VRD(13); pw[0] = pack8(C0, 0); C0[9] = EX2(C0[9]); PIN(pw[0]); PIN(C0); SBAR();
    o[2] = MFMA32(vq[2], pw[2], o[2]); VRD(14); C0[10] = EX2(C0[10]); C0[11] = EX2(C0[11]); sa += C0[6]; sb += C0[7]; PIN(C0); PIN(sa); PIN(sb); SBAR();
    o[3] = MFMA32(vq[3], pw[2], o[3]); VRD(15); C0[12] = EX2(C0[12]); C0[13] = EX2(C0[13]); sa += C0[8]; sb += C0[9]; PIN(C0); PIN(sa); PIN(sb); SBAR();
    if (GRP == 0) BARRIER_G();
    o[0] = MFMA32(vq[0], pw[3], o[0]); KPRE(0); if (GRP == 0) GLDS_T(vs_n, vd_n, 0); C0[14] = EX2(C0[14]); C0[15] = EX2(C0[15]); sa += C0[10]; sb += C0[11]; PIN(C0); PIN(sa); PIN(sb); SBAR();
    o[1] = MFMA32(vq[1], pw[3], o[1]); KPRE(1); C1[0] = EX2(C1[0]); C1[1] = EX2(C1[1]); sa += C0[12]; sb += C0[13]; PIN(C1); PIN(sa); PIN(sb); SBAR();
    o[2] = MFMA32(vq[2], pw[3], o[2]); KPRE(2); pw[1] = pack8(C0, 1); C1[2] = EX2(C1[2]); C1[3] = EX2(C1[3]); PIN(pw[1]); PIN(C1); SBAR();
    o[3] = MFMA32(vq[3], pw[3], o[3]); KPRE(3); C1[4] = EX2(C1[4]); C1[5] = EX2(C1[5]); sa += C0[14]; sb += C0[15]; PIN(C1); PIN(sa); PIN(sb); SBAR();
    C1[6] = EX2(C1[6]); C1[7] = EX2(C1[7]); lsum += sa + sb; PIN(C1); PIN(lsum);
    if (resc) {
#pragma unroll
      for (int d = 0; d < 4; ++d) o[d] = o[d] * fsc;
    }
    if (seam) {
      const float lt = xhalf_sum(lsum1);
      const char* pst = ws + WS_PF + (size_t)((h * 8 + j) * 8 + wid) * 8192;
      static_for<4>([&](auto dd_) { constexpr int d = decltype(dd_)::value; gstore_sc1_c<(2 * d) * 1024>(vl16, pack8(o[d], 0), pst); gstore_sc1_c<(2 * d + 1) * 1024>(vl16, pack8(o[d], 1), pst); o[d] = z; });
      const unsigned long long sv = ((unsigned long long)__float_as_uint(lt) << 32) | __float_as_uint(mhat1);
      __hip_atomic_store((unsigned long long*)(ws + WS_PS) + (size_t)((h * 8 + j) * 8 + wid) * 64 + lane, sv, __ATOMIC_RELAXED, __HIP_MEMORY_SCOPE_AGENT);
    }
    if (g == n1 - 1) {
      asm volatile("s_waitcnt vmcnt(1)" : "+v"(qr[0]), "+v"(qr[1]), "+v"(qr[2]), "+v"(qr[3]), "+v"(qr[4]), "+v"(qr[5]), "+v"(qr[6]), "+v"(qr[7]) :: "memory");
    }
  }
  asm volatile("" :: "v"(pfd));
  {
    const bool early = (rfl(*(const LAS unsigned*)(lds + 131072)) & 512u) != 0u;
    const char* psrc = upt(ws + WS_PF + (size_t)((h * 8 + jp) * 8 + wid) * 8192);
    const char* psrc2 = upt(psrc + 4096);
    const unsigned long long* svp = (const unsigned long long*)(ws + WS_PS) + (size_t)((h * 8 + jp) * 8 + wid) * 64 + lane;
    u32x4 qa[4], qb[4];
    u32x2 svv;
#define PLOADS() do { GLOAD1_SC1(qa[0], vl16, psrc, 0); GLOAD1_SC1(qa[1], vl16, psrc, 1024); GLOAD1_SC1(qa[2], vl16, psrc, 2048); GLOAD1_SC1(qa[3], vl16, psrc, 3072); \
      GLOAD1_SC1(qb[0], vl16, psrc2, 0); GLOAD1_SC1(qb[1], vl16, psrc2, 1024); GLOAD1_SC1(qb[2], vl16, psrc2, 2048); GLOAD1_SC1(qb[3], vl16, psrc2, 3072); \
      asm volatile("global_load_dwordx2 %0, %1, off sc1" : "=&v"(svv) : "v"(svp) : "memory"); } while (0)
    if (early) PLOADS();
    ldscp vb = lds + L_V + (17 & 3) * 16384 + rdoff;
#pragma unroll
    for (int i = 0; i < 4; ++i) vq[i] = ldsr128(vb + i * 1024);
    float sa = 0.f;
#pragma unroll
    for (int r = 8; r < 16; ++r) C1[r] = EX2(C1[r]);
#pragma unroll
    for (int r = 0; r < 16; ++r) sa += C1[r];
    lsum += sa;
    pw[2] = pack8(C1, 0); pw[3] = pack8(C1, 1);
    SBAR();
#pragma unroll
    for (int v = 0; v < 16; ++v) {
      o[v & 3] = MFMA32(vq[v & 3], pw[v >> 2], o[v & 3]);
      if (v + 4 < 16) vq[v & 3] = ldsr128(vb + (v + 4) * 1024);
      SBAR();
    }
    lsum = xhalf_sum(lsum);
    if (!early) { wait_word(flags + FL_P + 32 * (8 * h + jp), 1u, flags + FL_TMO); PLOADS(); }
    WAITV_TIE4(0, qa); WAITV_TIE4(0, qb);
    asm volatile("" : "+v"(svv));
    const unsigned long long sv = ((unsigned long long)svv[1] << 32) | svv[0];
#undef PLOADS
    const float om = __uint_as_float((unsigned)sv), ol = __uint_as_float((unsigned)(sv >> 32));
    const float m = __builtin_fmaxf(mhat, om);
    const float f0 = EX2(mhat - m), f1 = EX2(om - m);
    const float inv = __builtin_amdgcn_rcpf(lsum * f0 + ol * f1);
    const float g0 = f0 * inv, g1 = f1 * inv;
    const char* cdst = upt(ws + WS_CTX + (size_t)((8 * jp + wid) * 32 + h) * 8192);
    static_for<8>([&](auto ii_) {
      constexpr int i = decltype(ii_)::value; constexpr int d = i >> 1, sp = i & 1;
      const u32x4 v4 = i < 4 ? qa[i & 3] : qb[i & 3];
      u32x4 w;
#pragma unroll
      for (int e = 0; e < 4; ++e) w[e] = cvtpk(o[d][8 * sp + 2 * e] * g0 + bflo(v4[e]) * g1, o[d][8 * sp + 2 * e + 1] * g0 + bfhi(v4[e]) * g1);
      gstore_sc1_c<i * 1024>(vl16, __builtin_bit_cast(bf16x8, w), cdst);
    });
  }
#undef KRD
#undef VRD
#undef KPRE
#undef BARRIER_G
#undef ENSURE_KV
#undef GLDS_T
#undef TILE
#undef KSRC
#undef VSRC
}

constexpr int T_LDS = 163840;
constexpr int FL_C = 28672;
constexpr int FL_P3 = 20480;
constexpr int FL_H = 22528;
constexpr int FL_U = 23552;
#define GLOAD4_SC1(d, voff, sbase) asm volatile("s_nop 4\n\tglobal_load_dwordx4 %0, %4, %5 sc1\n\tglobal_load_dwordx4 %1, %4, %5 offset:1024 sc1\n\tglobal_load_dwordx4 %2, %4, %5 offset:2048 sc1\n\tglobal_load_dwordx4 %3, %4, %5 offset:3072 sc1" \
    : "=&v"(d[0]), "=&v"(d[1]), "=&v"(d[2]), "=&v"(d[3]) : "v"(voff), "s"(sbase) : "memory")
#define GLDS_SC1(voff, sbase, ldst) do { unsigned keep_; asm volatile("s_mov_b32 %0, m0\n\ts_mov_b32 m0, %3\n\ts_nop 2\n\tglobal_load_lds_dwordx4 %1, %2 sc1\n\ts_mov_b32 m0, %0" \
    : "=&s"(keep_) : "v"(voff), "s"(sbase), "s"(ldst) : "memory"); } while (0)
template <int OFFS> DI void gstore_sc1_uc(unsigned voff, const u32x4& v, const void* sbase) {
  unsigned t;
  asm volatile("v_add_u32 %0, %4, %1\n\ts_nop 3\n\tglobal_store_dwordx4 %0, %2, %3 sc1\n\ts_nop 1" : "=&v"(t) : "v"(voff), "v"(v), "s"(sbase), "i"(OFFS) : "memory");
}
DI void gstore_sc1_u(unsigned voff, const u32x4& v, const void* sbase) {
  asm volatile("s_nop 4\n\tglobal_store_dwordx4 %0, %1, %2 sc1\n\ts_nop 1" :: "v"(voff), "v"(v), "s"(sbase) : "memory");
}
template <bool FIRST> struct MlpCfg {
  static constexpr int PITCH = FIRST ? FF * 2 : SEQ * 2;
  static constexpr int NKT = FIRST ? SEQ / 64 : FF / 64;
  static constexpr int NST = (NKT + 1) / 2;
};
template <bool FIRST> DI unsigned mlp_voffw(int wid, int lane) {
  const int row = 8 * wid + (lane >> 3); const int f = ((row >> 1) & 1) | (((row >> 3) & 1) << 1);
  return (unsigned)(row * MlpCfg<FIRST>::PITCH + (((lane & 7) ^ (f << 1)) << 4));
}
template <bool FIRST, int KT0, int NK> DI void mlp_wstage(unsigned lds0, const char* wb, unsigned voffW, int wid, int reg) {
  static_for<NK>([&](auto kk_) {
    constexpr int k = decltype(kk_)::value; constexpr int kt = (KT0 + k) < MlpCfg<FIRST>::NKT ? (KT0 + k) : MlpCfg<FIRST>::NKT - 1;
    glds16_c<kt * 64 * MlpCfg<FIRST>::PITCH>(voffW, wb, lds0 + reg + k * 8192 + wid * 1024);
  });
}
struct MlpW { ldscp p[2]; };
DI MlpW mlp_wptr(ldsp lds, int wid, int lane) {
  const int cp = wid & 1, qq = wid >> 1, g = lane >> 4, q = (lane & 15) >> 2, p = lane & 3; const int f = (q >> 1) | ((g & 1) << 1);
  MlpW w;
#pragma unroll
  for (int e = 0; e < 2; ++e) w.p[e] = lds + (qq >> 1) * 8192 + 128 * (32 * (qq & 1) + 8 * g + q) + (((2 * (2 * cp + e) + (p >> 1)) ^ (f << 1)) << 4) + 8 * (p & 1);
  return w;
}
template <int P0, int NP, int NW> DI void mlp_wread(const MlpW& w, int reg, bf16x8 (&wf)[NW]) {
  static_for<NP>([&](auto kk_) {
    constexpr int k = decltype(kk_)::value;
#pragma unroll
    for (int e = 0; e < 2; ++e) wf[(P0 + k) * 2 + e] = cat8(vtr(w.p[e] + reg + k * 16384), vtr(w.p[e] + reg + k * 16384 + 512));
  });
}
constexpr int mlp_slot(int s) { return (s & 3) * 40960; }
template <bool FIRST> DI void mlp_astage(unsigned lds0, const char* ab, unsigned vl16, int wid, int stg) {
  constexpr int NT = 2 * MlpCfg<FIRST>::NKT;
#pragma unroll
  for (int k = 0; k < 5; ++k) {
    const int i = wid * 5 + k;
    const int T_ = 8 * stg + wid; const int Tc_ = T_ < NT ? T_ : NT - 1;
    GLDS_SC1(vl16, ab + ((size_t)Tc_ * 40 + k) * 1024, lds0 + mlp_slot(stg) + i * 1024);
  }
}
template <bool FIRST, int NW>
DI void mlp_main(ldsp lds, unsigned lds0, const char* __restrict__ ab, int wid, int lane, const bf16x8 (&wf)[NW], f32x4 (&acc)[2][5]) {
  constexpr int NKT = MlpCfg<FIRST>::NKT, NS = (NKT + 3) / 4;
  const int qq = wid >> 1;
  const unsigned vl16 = lane * 16;
  ldscp ap = lds + qq * 5120 + lane * 16;
#pragma unroll
  for (int e = 0; e < 2; ++e)
#pragma unroll
    for (int d = 0; d < 5; ++d) acc[e][d] = (f32x4){0.f, 0.f, 0.f, 0.f};
  mlp_astage<FIRST>(lds0, ab, vl16, wid, 0); mlp_astage<FIRST>(lds0, ab, vl16, wid, 1); mlp_astage<FIRST>(lds0, ab, vl16, wid, 2);
  static_for<NS>([&](auto ss_) {
    constexpr int s = decltype(ss_)::value;
    constexpr int ahead = (NS - 1 - s) < 2 ? (NS - 1 - s) : 2;
    WAITVN(5 * ahead);
    SBAR(); BAR();
    bf16x8 af[2][5];
#pragma unroll
    for (int e2 = 0; e2 < 2; ++e2)
#pragma unroll
      for (int d = 0; d < 5; ++d) af[e2][d] = ldsr128(ap + mlp_slot(s) + e2 * 20480 + d * 1024);
    SBAR();
    if constexpr (s + 3 < NS) mlp_astage<FIRST>(lds0, ab, vl16, wid, s + 3);
    SBAR();
#pragma unroll
    for (int e2 = 0; e2 < 2; ++e2) {
      const bool ok = (4 * s + 2 * e2 + 1 < NKT) || (4 * s + 2 * e2 + (qq >> 1) < NKT);
      if (ok) {
#pragma unroll
        for (int e = 0; e < 2; ++e)
#pragma unroll
          for (int d = 0; d < 5; ++d) acc[e][d] = FIRST ? MFMA16(wf[(2 * s + e2) * 2 + e], af[e2][d], acc[e][d]) : MFMA16(af[e2][d], wf[(2 * s + e2) * 2 + e], acc[e][d]);
      }
    }
    SBAR();
  });
}
DI void mlp_park(ldsp lds, int wid, int lane, const f32x4 (&acc)[2][5]) {
#pragma unroll
  for (int e = 0; e < 2; ++e)
#pragma unroll
    for (int d = 0; d < 5; ++d) *(LAS f32x4*)(lds + ((wid * 2 + e) * 5 + d) * 1024 + lane * 16) = acc[e][d];
  WAIT_L(0); BAR();
}
DI void mlp_gather(ldscp lds, int c2, int dt, int gD, int c15, float (&v)[8]) {
#pragma unroll
  for (int hf = 0; hf < 2; ++hf) {
    f32x4 a = {0.f, 0.f, 0.f, 0.f};
#pragma unroll
    for (int q = 0; q < 4; ++q) a += *(const LAS f32x4*)(lds + ((q * 4 + c2) * 5 + dt) * 1024 + ((gD + hf) * 16 + c15) * 16);
#pragma unroll
    for (int e = 0; e < 4; ++e) v[4 * hf + e] = a[e];
  }
}

DI void tail_phase(unsigned char* smem, const u16* __restrict__ x, const u16* __restrict__ wo, const u16* __restrict__ w1, const u16* __restrict__ b1,
                   const u16* __restrict__ w2, const u16* __restrict__ b2, char* __restrict__ ws, u16* __restrict__ out, int wid) {
  const int b = blockIdx.x, xcd = b & 7, r = b >> 3;
  ldsp lds = (ldsp)smem;
  const unsigned lds0 = (unsigned)(uintptr_t)smem;
  unsigned* flags = (unsigned*)(ws + WS_FLAGS);
  const int ksp = xcd >> 1, panel = (xcd & 1) * 8 + (r >> 2), ctile = r & 3;
  const int idx5 = xcd * 27 + r;
  const int wt5 = idx5 >> 3, dg5 = idx5 & 7;
  const int wt6 = xcd * 4 + (r >> 3), dg6 = r & 7;
  const bool do5 = r < 27;
  constexpr int P4S = 131072;
  bf16x8 wf[32];
  {
    const int lane = lane_id();
    const unsigned vl16 = lane * 16;
    const int kh = wid >> 2, st = wid & 3, hh = lane >> 5;
    unsigned voff[5];
#pragma unroll
    for (int i = 0; i < 5; ++i) {
      const int id = 2 * (5 * wid + i) + (lane >> 5), half = id / 40, rem = id % 40, rg = rem / 5, c = rem % 5;
      const int row = 64 * half + 8 * rg + ((lane >> 2) & 7);
      voff[i] = (unsigned)(row * 1280 + (32 * c + 8 * ((lane & 3) ^ ((row >> 2) & 3))) * 2);
    }
    const char* wb = (const char*)wo + (size_t)(ksp * 1024) * 1280 + ctile * 320;
    const unsigned dstw = lds0 + wid * 5120;
#define OPIECE(hd, i) glds16_c<(hd) * 128 * 1280>(voff[i], wb, dstw + ((hd) & 3) * 40960 + (i) * 1024)
#define STAGE_O(hd) do { OPIECE(hd, 0); OPIECE(hd, 1); OPIECE(hd, 2); OPIECE(hd, 3); OPIECE(hd, 4); } while (0)
    const char* cb = ws + WS_CTX + ((size_t)((4 * panel + st) * 32 + 8 * ksp) * 8 + 4 * kh) * 1024;
    int boff[2];
    { const int blk = (lane >> 4) & 1, q = (lane & 15) >> 2, p = lane & 3;
#pragma unroll
      for (int t = 0; t < 2; ++t) boff[t] = kh * 20480 + 2560 * t + 64 * (4 * hh + q) + (((2 * blk + (p >> 1)) ^ ((2 * t + hh) & 3)) << 4) + 8 * (p & 1); }
    ldscp bp0 = lds + boff[0], bp1 = lds + boff[1];
    u32x4 xf[3][4];
    f32x16 acc[5] = {};
    bf16x8 aq[6];
#define ARD(U) aq[(U) % 6] = cat8(vtr(bp0 + (((U) / 20) & 3) * 40960 + (((U) % 20) / 5) * 5120 + ((U) % 5) * 512), vtr(bp1 + (((U) / 20) & 3) * 40960 + (((U) % 20) / 5) * 5120 + ((U) % 5) * 512))
    STAGE_O(0); STAGE_O(1); STAGE_O(2);
    if (wid == 0) wait_word(flags + FL_C + 16 * ((panel >> 1) * 4 + ksp), 8u, flags + FL_TMO);
    BAR();
    GLOAD4_SC1(xf[0], vl16, cb); GLOAD4_SC1(xf[1], vl16, cb + 8192);
    WAIT_V(8); BAR();
#pragma unroll
    for (int U = 0; U < 5; ++U) ARD(U);
    static_for<8>([&](auto hh_) {
      constexpr int hd = decltype(hh_)::value;
      if constexpr (hd == 0) { WAITV_TIE4(4, xf[0]); }
      else if constexpr (hd <= 5) { WAITV_TIE4(9, xf[hd % 3]); }
      else if constexpr (hd == 6) { WAITV_TIE4(4, xf[hd % 3]); }
      else { WAITV_TIE4(0, xf[hd % 3]); }
      SBAR(); BAR(); SBAR();
      const char* xn_ = cb + (size_t)(hd + 2) * 8192;
      static_for<20>([&](auto uu_) {
        constexpr int u = decltype(uu_)::value;
        constexpr int U = 20 * hd + u;
        acc[u % 5] = MFMA32(aq[U % 6], __builtin_bit_cast(bf16x8, xf[hd % 3][u / 5]), acc[u % 5]);
        if constexpr (U + 5 < 160) ARD(U + 5);
        if constexpr (hd + 2 < 8) {
          if constexpr (u == 1) GLOAD1_SC1(xf[(hd + 2) % 3][0], vl16, xn_, 0);
          if constexpr (u == 3) GLOAD1_SC1(xf[(hd + 2) % 3][1], vl16, xn_, 1024);
          if constexpr (u == 5) GLOAD1_SC1(xf[(hd + 2) % 3][2], vl16, xn_, 2048);
          if constexpr (u == 7) GLOAD1_SC1(xf[(hd + 2) % 3][3], vl16, xn_, 3072);
        }
        if constexpr (hd + 3 < 8) {
          if constexpr (u == 9) OPIECE(hd + 3, 0);
          if constexpr (u == 11) OPIECE(hd + 3, 1);
          if constexpr (u == 13) OPIECE(hd + 3, 2);
          if constexpr (u == 15) OPIECE(hd + 3, 3);
          if constexpr (u == 17) OPIECE(hd + 3, 4);
        }
        SBAR();
      });
    });
#undef OPIECE
#undef STAGE_O
#undef ARD
    BAR();
    ldsp xw = lds + (kh == 0 ? 49152 : 0) + st * 12288 + lane * 16;
    ldscp xr = lds + (kh == 0 ? 0 : 49152) + st * 12288 + lane * 16;
    if (kh == 0) {
#pragma unroll
      for (int i = 0; i < 2; ++i)
#pragma unroll
        for (int g = 0; g < 4; ++g) *(LAS f32x4*)(xw + (i * 4 + g) * 1024) = (f32x4){acc[3 + i][4 * g], acc[3 + i][4 * g + 1], acc[3 + i][4 * g + 2], acc[3 + i][4 * g + 3]};
    } else {
#pragma unroll
      for (int i = 0; i < 3; ++i)
#pragma unroll
        for (int g = 0; g < 4; ++g) *(LAS f32x4*)(xw + (i * 4 + g) * 1024) = (f32x4){acc[i][4 * g], acc[i][4 * g + 1], acc[i][4 * g + 2], acc[i][4 * g + 3]};
    }
    WAIT_L(0); BAR();
    const char* sb = ws + WS_SLAB + ((size_t)((ksp * 64 + 4 * panel + st) * 20 + 5 * ctile) * 2) * 1024;
#define SLABST(I, T) static_for<2>([&](auto pp_) { \
          constexpr int sp = decltype(pp_)::value; \
          const f32x4 o0 = *(const LAS f32x4*)(xr + ((I) * 4 + 2 * sp) * 1024), o1 = *(const LAS f32x4*)(xr + ((I) * 4 + 2 * sp + 1) * 1024); \
          const u32x4 w = {cvtpk(acc[T][8 * sp] + o0[0], acc[T][8 * sp + 1] + o0[1]), cvtpk(acc[T][8 * sp + 2] + o0[2], acc[T][8 * sp + 3] + o0[3]), \
                           cvtpk(acc[T][8 * sp + 4] + o1[0], acc[T][8 * sp + 5] + o1[1]), cvtpk(acc[T][8 * sp + 6] + o1[2], acc[T][8 * sp + 7] + o1[3])}; \
          gstore_sc1_uc<((T) * 2 + sp) * 1024>(vl16, w, sb); })
    if (kh == 0) { SLABST(0, 0); SLABST(1, 1); SLABST(2, 2); }
    else { SLABST(0, 3); SLABST(1, 4); }
#undef SLABST
    WAIT_V(0); WAIT_L(0); BAR();
    if (wid == 0 && lane == 0) __hip_atomic_fetch_add(flags + FL_P3 + 64 * panel, 1u, __ATOMIC_RELAXED, __HIP_MEMORY_SCOPE_AGENT);
  }
  {
    const int lane = lane_id(), tid = wid * 64 + lane;
    const MlpW wp = mlp_wptr(lds, wid, lane);
    const char* w1b = (const char*)w1 + wt5 * 128;
    const unsigned voffW = mlp_voffw<true>(wid, lane);
    ldsp tr = lds + P4S;
    LAS float* red = (LAS float*)(lds + P4S + 10240);
    const int rr = tid & 7, pc = tid >> 3;
    const int r8 = ksp * 4 + ctile;
    const int s = 128 * panel + 8 * r8 + rr, st = s >> 5;
    int n0[2]; unsigned so[2];
    u32x2 xv[2][2];
#pragma unroll
    for (int i = 0; i < 2; ++i) {
      const int pi = pc + 64 * i; const int pcl = pi < 80 ? pi : 0;
      const int nt = pcl >> 2, sp = (pcl >> 1) & 1, hh = pcl & 1;
      n0[i] = 32 * nt + 16 * sp + 4 * hh;
      so[i] = (unsigned)((((st * 20 + nt) * 2 + sp) * 64 + (s & 31) + 32 * hh) * 16);
      xv[i][0] = *(const u32x2*)(x + (size_t)s * DM + n0[i]); xv[i][1] = *(const u32x2*)(x + (size_t)s * DM + n0[i] + 8);
    }
    asm volatile("" : "+v"(xv[0][0]), "+v"(xv[0][1]), "+v"(xv[1][0]), "+v"(xv[1][1]));
    if (do5) mlp_wstage<true, 0, 16>(lds0, w1b, voffW, wid, 0);
    if (wid == 0) wait_word(flags + FL_P3 + 64 * panel, 16u, flags + FL_TMO);
    BAR();
    u32x4 sv[2][4];
    const char* sl = ws + WS_SLAB;
#pragma unroll
    for (int i = 0; i < 2; ++i)
#pragma unroll
      for (int k = 0; k < 4; ++k) GLOAD1_SC1(sv[i][k], so[i], sl + (size_t)k * (64 * 20 * 2 * 1024), 0);
    WAITV_TIE4(0, sv[0]); WAITV_TIE4(0, sv[1]);
    float hv[2][8];
    unsigned pk[2][4];
    float sum = 0.f, ssq = 0.f;
#pragma unroll
    for (int i = 0; i < 2; ++i) {
      const bool ok = pc + 64 * i < 80;
#pragma unroll
      for (int e = 0; e < 4; ++e) {
        const float lo = (bflo(sv[i][0][e]) + bflo(sv[i][1][e])) + (bflo(sv[i][2][e]) + bflo(sv[i][3][e]));
        const float hi = (bfhi(sv[i][0][e]) + bfhi(sv[i][1][e])) + (bfhi(sv[i][2][e]) + bfhi(sv[i][3][e]));
        const unsigned xw_ = xv[i][e >> 1][e & 1];
        pk[i][e] = cvtpk(lo + bflo(xw_), hi + bfhi(xw_));
        hv[i][2 * e] = ok ? bflo(pk[i][e]) : 0.f; hv[i][2 * e + 1] = ok ? bfhi(pk[i][e]) : 0.f;
        sum += hv[i][2 * e] + hv[i][2 * e + 1];
      }
    }
    sum += __shfl_xor(sum, 8); sum += __shfl_xor(sum, 16); sum += __shfl_xor(sum, 32);
    const float cntw = (wid < 2) ? 128.f : 64.f;
    const float mw = sum / cntw;
#pragma unroll
    for (int i = 0; i < 2; ++i) {
      if (pc + 64 * i < 80) {
#pragma unroll
        for (int e = 0; e < 8; ++e) { const float d = hv[i][e] - mw; ssq += d * d; }
      }
    }
    ssq += __shfl_xor(ssq, 8); ssq += __shfl_xor(ssq, 16); ssq += __shfl_xor(ssq, 32);
    if (lane < 8) { red[(wid * 8 + lane) * 2] = mw; red[(wid * 8 + lane) * 2 + 1] = ssq; }
    WAIT_L(0); BAR();
    float mean = 0.f;
#pragma unroll
    for (int w = 0; w < 8; ++w) mean += red[(w * 8 + rr) * 2] * (w < 2 ? 128.f : 64.f);
    mean *= (1.0f / DM);
    float m2 = 0.f;
#pragma unroll
    for (int w = 0; w < 8; ++w) { const float dm = red[(w * 8 + rr) * 2] - mean; m2 += red[(w * 8 + rr) * 2 + 1] + dm * dm * (w < 2 ? 128.f : 64.f); }
    const float rstd = 1.0f / sqrtf(m2 * (1.0f / DM) + LN_EPS);
#pragma unroll
    for (int i = 0; i < 2; ++i) {
      if (pc + 64 * i < 80) {
        LAS u16* t16 = (LAS u16*)tr;
#pragma unroll
        for (int e = 0; e < 4; ++e) {
          const unsigned p = cvtpk((hv[i][2 * e] - mean) * rstd, (hv[i][2 * e + 1] - mean) * rstd);
          const int n = n0[i] + 8 * (e >> 1) + 2 * (e & 1);
          t16[n * 8 + rr] = (u16)(p & 0xffff); t16[(n + 1) * 8 + rr] = (u16)(p >> 16);
        }
      }
    }
    WAIT_L(0); BAR();
    const char* hdst = ws + WS_HNF + ((size_t)st * 40 * 64 + ((s >> 3) & 3) * 16) * 16;
    { const int d = tid; const u32x4 tv4 = *(const LAS u32x4*)(tr + d * 16); gstore_sc1_u((unsigned)(((d >> 4) * 64 + (d & 15)) * 16), tv4, hdst); }
    if (tid < 128) { const int d = 512 + tid; const u32x4 tv4 = *(const LAS u32x4*)(tr + d * 16); gstore_sc1_u((unsigned)(((d >> 4) * 64 + (d & 15)) * 16), tv4, hdst); }
#pragma unroll
    for (int i = 0; i < 2; ++i) {
      if (pc + 64 * i < 80) {
        unsigned long long* hp = (unsigned long long*)(ws + WS_H + ((size_t)s * DM + n0[i]) * 2);
        __hip_atomic_store(hp, ((unsigned long long)pk[i][1] << 32) | pk[i][0], __ATOMIC_RELAXED, __HIP_MEMORY_SCOPE_AGENT);
        __hip_atomic_store(hp + 2, ((unsigned long long)pk[i][3] << 32) | pk[i][2], __ATOMIC_RELAXED, __HIP_MEMORY_SCOPE_AGENT);
      }
    }
    if (do5) mlp_wread<0, 8>(wp, 0, wf);
    WAIT_V(0); WAIT_L(0); BAR();
    if (tid == 0) __hip_atomic_fetch_add(flags + FL_H + 64 * (b & 7), 1u, __ATOMIC_RELAXED, __HIP_MEMORY_SCOPE_AGENT);
    if (do5) {
      mlp_wstage<true, 16, 16>(lds0, w1b, voffW, wid, 0);
      WAIT_V(0); BAR();
      mlp_wread<8, 8>(wp, 0, wf);
    }
  }
  constexpr int RHI = 81920;
  {
    const int lane = lane_id(), tid = wid * 64 + lane;
    const char* w2b = (const char*)w2 + wt6 * 128;
    const unsigned voffW2 = mlp_voffw<false>(wid, lane);
    if (do5) {
      if (wid == 0) {
        for (unsigned spins = 0;; ++spins) {
          const unsigned v = __hip_atomic_load(flags + FL_H + 64 * (lane & 7), __ATOMIC_RELAXED, __HIP_MEMORY_SCOPE_AGENT);
          if (__all(v >= 32u)) break;
          if (spins > (1u << 16)) { __hip_atomic_store(flags + FL_TMO, 1u, __ATOMIC_RELAXED, __HIP_MEMORY_SCOPE_AGENT); break; }
          __builtin_amdgcn_s_sleep(1);
        }
      }
      WAIT_L(0); BAR();
      f32x4 acc[2][5];
      mlp_main<true, 32>(lds, lds0, ws + WS_HNF + (size_t)(5 * dg5) * 1024, wid, lane, wf, acc);
      BAR();
      mlp_park(lds, wid, lane, acc);
      {
        float v[2][8]; u32x4 bv[2];
#pragma unroll
        for (int k2 = 0; k2 < 2; ++k2) {
          const int it = k2 == 0 ? tid : (tid < 128 ? 512 + tid : tid);
          const int d15 = it & 15, gp = (it >> 4) & 3, fr = it >> 6, dt = fr % 5, Tp = fr / 5;
          bv[k2] = *(const u32x4*)(b1 + 64 * wt5 + 32 * Tp + 8 * gp);
          mlp_gather(lds, 2 * Tp + (gp >> 1), dt, 2 * (gp & 1), d15, v[k2]);
        }
#pragma unroll
        for (int k2 = 0; k2 < 2; ++k2) {
          if (k2 == 0 || tid < 128) {
            const int it = k2 == 0 ? tid : 512 + tid;
            const int d15 = it & 15, gp = (it >> 4) & 3, fr = it >> 6, dt = fr % 5, Tp = fr / 5;
            u32x4 o;
#pragma unroll
            for (int e = 0; e < 4; ++e) {
              float z[2] = {v[k2][2 * e] + bflo(bv[k2][e]), v[k2][2 * e + 1] + bfhi(bv[k2][e])};
#pragma unroll
              for (int k = 0; k < 2; ++k) {
                const float xx = z[k];
                const float a = xx * (1.0f + 0.044715f * xx * xx) * (-2.0f * 0.7978845608028654f * 1.4426950408889634f);
                z[k] = xx * __builtin_amdgcn_rcpf(1.0f + __builtin_amdgcn_exp2f(a));
              }
              o[e] = cvtpk(z[0], z[1]);
            }
            gstore_sc1_u((unsigned)((((2 * wt5 + Tp) * 40 + 5 * dg5 + dt) * 64 + gp * 16 + d15) * 16), o, ws + WS_UF);
          }
        }
      }
      mlp_wstage<false, 0, 10>(lds0, w2b, voffW2, wid, RHI);
      WAIT_V(10); WAIT_L(0); BAR();
      if (tid == 0) __hip_atomic_fetch_add(flags + FL_U + 64 * dg5, 1u, __ATOMIC_RELAXED, __HIP_MEMORY_SCOPE_AGENT);
    } else {
      WAIT_L(0); BAR();
      mlp_wstage<false, 0, 10>(lds0, w2b, voffW2, wid, RHI);
    }
  }
  {
    const int lane = lane_id(), tid = wid * 64 + lane;
    bf16x8 wg[28];
    const MlpW wp = mlp_wptr(lds, wid, lane);
    const char* w2b = (const char*)w2 + wt6 * 128;
    const unsigned voffW2 = mlp_voffw<false>(wid, lane);
    mlp_wstage<false, 10, 10>(lds0, w2b, voffW2, wid, 0);
    WAIT_V(0); BAR();
    mlp_wread<0, 5>(wp, RHI, wg); mlp_wread<5, 5>(wp, 0, wg);
    WAIT_L(0); BAR();
    mlp_wstage<false, 20, 8>(lds0, w2b, voffW2, wid, 0);
    WAIT_V(0); BAR();
    mlp_wread<10, 4>(wp, 0, wg);
    if (wid == 0) wait_word(flags + FL_U + 64 * dg6, 27u, flags + FL_TMO);
    WAIT_L(0); BAR();
    const char* ab = ws + WS_UF + (size_t)(5 * dg6) * 1024;
    f32x4 acc[2][5];
    mlp_main<false, 28>(lds, lds0, ab, wid, lane, wg, acc);
    u32x4 hv[2]; float bb[2]; size_t oo[2];
#pragma unroll
    for (int k2 = 0; k2 < 2; ++k2) {
      const int it = k2 == 0 ? tid : (tid < 128 ? 512 + tid : tid);
      const int sl = it / 10, c = it % 10, srow = 64 * wt6 + sl;
      oo[k2] = (size_t)srow * DM + 80 * dg6 + 8 * c;
      GLOAD1_SC1(hv[k2], (unsigned)(oo[k2] * 2), ws + WS_H, 0);
      bb[k2] = bflo((unsigned)b2[srow]);
    }
    BAR();
    mlp_park(lds, wid, lane, acc);
    float v[2][8];
#pragma unroll
    for (int k2 = 0; k2 < 2; ++k2) {
      const int it = k2 == 0 ? tid : (tid < 128 ? 512 + tid : tid);
      const int sl = it / 10, c = it % 10;
      mlp_gather(lds, sl >> 4, c >> 1, 2 * (c & 1), sl & 15, v[k2]);
    }
    asm volatile("s_waitcnt vmcnt(0)" : "+v"(hv[0]), "+v"(hv[1]) :: "memory");
#pragma unroll
    for (int k2 = 0; k2 < 2; ++k2) {
      if (k2 == 0 || tid < 128) {
        u32x4 ov;
#pragma unroll
        for (int e = 0; e < 4; ++e) ov[e] = cvtpk(v[k2][2 * e] + bb[k2] + bflo(hv[k2][e]), v[k2][2 * e + 1] + bb[k2] + bfhi(hv[k2][e]));
        *(u32x4*)(out + oo[k2]) = ov;
      }
    }
  }
}

constexpr int qproj_ns(int c, int cls) {
  constexpr int tab[4][10] = {{3, 3, 3, 3, 2, 2, 0, 0, 0, 0}, {1, 2, 2, 2, 3, 3, 3, 0, 0, 0}, {0, 1, 1, 2, 2, 2, 2, 3, 3, 0}, {0, 0, 1, 1, 1, 2, 2, 3, 3, 3}};
  return (c < 0 || c > 9) ? 0 : tab[cls][c];
}
constexpr int qproj_sbase(int c, int cls) { int n = 0; for (int i = 0; i < c; ++i) n += qproj_ns(i, cls); return n; }
constexpr int qproj_nl(int c) { return (c + 2 < 10 ? 4 : 0) + (c + 3 < 10 ? 2 : 0); }
constexpr int qproj_allowed(int c, int cls) {
  return c == 0 ? 6 : (c == 1 ? qproj_nl(0) + qproj_ns(0, cls) : qproj_ns(c - 2, cls) + qproj_nl(c - 1) + qproj_ns(c - 1, cls));
}
__global__ __launch_bounds__(512, 2) void kF1(const u16* __restrict__ ckv, const u16* __restrict__ wuk, const u16* __restrict__ wuv, const u16* __restrict__ wq,
                                              const u16* __restrict__ wo, const u16* __restrict__ w1, const u16* __restrict__ w2,
                                              const u16* __restrict__ x, const u16* __restrict__ b1, const u16* __restrict__ b2, char* __restrict__ ws, u16* __restrict__ out) {
  extern __shared__ __attribute__((aligned(16))) unsigned char smem[];
  const int bidx = blockIdx.x, xcd = bidx & 7, rr_ = bidx >> 3;
  const int h = xcd * 4 + (rr_ >> 3), j = rr_ & 7;
  const int wid = rfl(threadIdx.x >> 6);
  ldsp lds = (ldsp)smem;
  const unsigned lds0 = (unsigned)(uintptr_t)smem;
  unsigned* flags = (unsigned*)(ws + WS_FLAGS);
  bf16x8 kvp[16];
  {
    const int wr = wid >> 2, wc = wid & 3;
    const int lane = threadIdx.x & 63, l31 = lane & 31, hh = lane >> 5;
    unsigned voffC, voffW;
    { const int row = 8 * wid + (lane >> 3); voffC = (unsigned)(row * 1024 + (((lane & 7) ^ ((row >> 1) & 7)) << 4)); }
    { const int row = 8 * (wid >> 1) + ((lane >> 2) & 7); const int chq = 2 * (wid & 1) + (lane >> 5);
      const int ch = 4 * chq + ((lane & 3) ^ ((row >> 2) & 3)); voffW = (unsigned)(row * 8192 + ch * 16); }
    const char* cbase = (const char*)ckv + (size_t)(256 * j) * 1024;
    const char* wkb = (const char*)wuk + h * 256;
    const char* wvb = (const char*)wuv + h * 256;
    const unsigned dstw = lds0 + wid * 1024;
#define CBUF(b, a) (((b) * 2 + (a)) * 16384)
#define WBUF(b, a) (65536 + ((b) * 2 + (a)) * 16384)
#define STAGE_C(b, a, kt) do { const char* s_ = cbase + (a) * 131072 + (kt) * 128; glds16(voffC, s_, dstw + CBUF(b, a)); glds16(voffC, s_ + 65536, dstw + CBUF(b, a) + 8192); } while (0)
#define STAGE_W(b, a, kt) do { const char* s_ = ((a) ? wvb : wkb) + (size_t)(kt) * 64 * 8192; glds16(voffW, s_, dstw + WBUF(b, a)); glds16(voffW, s_ + 32 * 8192, dstw + WBUF(b, a) + 8192); } while (0)
    int aoff[4], boff[2];
#pragma unroll
    for (int s = 0; s < 4; ++s) aoff[s] = 128 * (wr * 64 + l31) + (((2 * s + hh) ^ ((l31 >> 1) & 7)) << 4);
    { const int blk = (lane >> 4) & 1, q = (lane & 15) >> 2, p = lane & 3;
#pragma unroll
      for (int t = 0; t < 2; ++t) boff[t] = 2048 * hh + 512 * wc + 64 * (4 * t + q) + (((2 * blk + (p >> 1)) ^ ((2 * hh + t) & 3)) << 4) + 8 * (p & 1); }
#define LDA(dst, b, a) do { _Pragma("unroll") for (int m = 0; m < 2; ++m) _Pragma("unroll") for (int s = 0; s < 4; ++s) dst[m][s] = ldsr128(lds + CBUF(b, a) + m * 4096 + aoff[s]); } while (0)
#define LDB(dst, b, a) do { _Pragma("unroll") for (int s = 0; s < 4; ++s) dst[s] = cat8(vtr(lds + WBUF(b, a) + s * 4096 + boff[0]), vtr(lds + WBUF(b, a) + s * 4096 + boff[1])); } while (0)
#define MMAK(ai, At, Bt) do { __builtin_amdgcn_s_setprio(1); _Pragma("unroll") for (int s = 0; s < 4; ++s) _Pragma("unroll") for (int m = 0; m < 2; ++m) \
    accK[ai][m] = MFMA32(Bt[s], At[m][s], accK[ai][m]); __builtin_amdgcn_s_setprio(0); } while (0)
#define MMAV(ai, At, Bt) do { __builtin_amdgcn_s_setprio(1); _Pragma("unroll") for (int s = 0; s < 4; ++s) _Pragma("unroll") for (int m = 0; m < 2; ++m) \
    accV[ai][m] = MFMA32(At[m][s], Bt[s], accV[ai][m]); __builtin_amdgcn_s_setprio(0); } while (0)
    f32x16 accK[2][2] = {}, accV[2][2] = {};
    bf16x8 At[2][4], B0[4], B1[4];
    constexpr int nt = DC / 64;
    STAGE_W(0, 0, 0); STAGE_C(0, 0, 0); STAGE_W(0, 1, 0); STAGE_C(0, 1, 0);
    if (wr == 1) BAR();
    WAIT_V(4); BAR();
    STAGE_W(1, 0, 1); STAGE_C(1, 0, 1); STAGE_W(1, 1, 1);
    WAIT_V(6); BAR();
    unsigned pfd = 0;
    if (wid < 3) {
      const int ln = wid * 64 + lane;
      if (ln < 160) asm volatile("global_load_dword %0, %1, off" : "+v"(pfd) : "v"((const char*)wq + (size_t)(80 * j + (ln >> 1)) * 8192 + h * 256 + (ln & 1) * 128) : "memory");
    }
    for (int t = 0; t < nt - 2; t += 2) {
      LDB(B0, 0, 0); SBAR(); LDA(At, 0, 0); STAGE_C(1, 1, t + 1);
      WAIT_L(8); BAR(); WAIT_L(0); MMAK(0, At, B0); BAR(); SBAR();
      LDB(B1, 0, 1); STAGE_W(0, 0, t + 2);
      BAR(); WAIT_L(0); MMAV(0, At, B1); BAR();
      LDA(At, 0, 1); STAGE_C(0, 0, t + 2);
      BAR(); WAIT_L(0); MMAK(1, At, B0); BAR(); SBAR();
      STAGE_W(0, 1, t + 2);
      WAIT_V(6); BAR(); MMAV(1, At, B1); BAR();
      LDB(B0, 1, 0); SBAR(); LDA(At, 1, 0); STAGE_C(0, 1, t + 2);
      WAIT_L(8); BAR(); WAIT_L(0); MMAK(0, At, B0); BAR(); SBAR();
      LDB(B1, 1, 1); STAGE_W(1, 0, t + 3);
      BAR(); WAIT_L(0); MMAV(0, At, B1); BAR();
      LDA(At, 1, 1); STAGE_C(1, 0, t + 3);
      BAR(); WAIT_L(0); MMAK(1, At, B0); BAR(); SBAR();
      STAGE_W(1, 1, t + 3);
      WAIT_V(6); BAR(); MMAV(1, At, B1); BAR();
    }
    {
      const char* wqb_ = (const char*)wq + h * 256;
      glds16_c<0>(voffW, wqb_, lds0 + 131072 + wid * 1024); glds16_c<32 * 8192>(voffW, wqb_, lds0 + 131072 + 8192 + wid * 1024);
      glds16_c<64 * 8192>(voffW, wqb_, lds0 + 147456 + wid * 1024); glds16_c<96 * 8192>(voffW, wqb_, lds0 + 147456 + 8192 + wid * 1024); }
    { LDB(B0, 0, 0); WAIT_V(0); LDA(At, 0, 0); STAGE_C(1, 1, nt - 1);
      BAR(); WAIT_L(0); MMAK(0, At, B0); BAR();
      LDB(B1, 0, 1); BAR(); WAIT_L(0); MMAV(0, At, B1); BAR();
      LDA(At, 0, 1); WAIT_V(4); BAR(); WAIT_L(0); MMAK(1, At, B0); MMAV(1, At, B1); BAR(); }
    { LDB(B0, 1, 0); LDA(At, 1, 0); WAIT_V(2); BAR(); WAIT_L(0); MMAK(0, At, B0); BAR();
      LDB(B1, 1, 1); WAIT_V(0); BAR(); WAIT_L(0); MMAV(0, At, B1); BAR();
      LDA(At, 1, 1); BAR(); WAIT_L(0); MMAK(1, At, B0); MMAV(1, At, B1); BAR(); }
    if (wr == 0) BAR();
    asm volatile("" :: "v"(pfd));
#pragma unroll
    for (int ai = 0; ai < 2; ++ai)
#pragma unroll
      for (int m = 0; m < 2; ++m)
#pragma unroll
        for (int sp = 0; sp < 2; ++sp) {
          kvp[((ai * 2 + m) * 2 + sp) * 2] = pack8(accK[ai][m], sp);
          kvp[((ai * 2 + m) * 2 + sp) * 2 + 1] = pack8(accV[ai][m], sp);
          PIN(kvp[((ai * 2 + m) * 2 + sp) * 2]); PIN(kvp[((ai * 2 + m) * 2 + sp) * 2 + 1]);
        }
#undef CBUF
#undef WBUF
#undef STAGE_C
#undef STAGE_W
#undef LDA
#undef LDB
#undef MMAK
#undef MMAV
  }
  SBAR();
  bf16x8 qr[8];
  unsigned kvmask = 0;
  const int lane = lane_id(), tid = wid * 64 + lane;
  const unsigned vl16 = lane * 16;
  {
    const int hh = lane >> 5;
    const char* Kh = ws + WS_KF + (size_t)h * 64 * 8192;
    const char* Vh = ws + WS_VF + (size_t)h * 128 * 4096;
    const unsigned vt2 = (unsigned)(wid * 2048 + lane * 16);
    const unsigned dt2 = lds0 + wid * 2048;
    const int n1 = j >= 4 ? 16 : 4 * j + 2;
#define TILE(g) ((g) < n1 ? (g) : 16 + (g) - n1)
#define DMA_T(src, ldsoff) do { unsigned keep_; asm volatile("s_mov_b32 %0, m0\n\ts_mov_b32 m0, %3\n\ts_nop 2\n\tglobal_load_lds_dwordx4 %1, %2 sc1\n\tglobal_load_lds_dwordx4 %1, %2 offset:1024 sc1\n\ts_mov_b32 m0, %0" \
    : "=&s"(keep_) : "v"(vt2), "s"(src), "s"(dt2 + (ldsoff)) : "memory"); } while (0)
    const int wr = wid >> 2, wc = wid & 3, cls = j & 3;
    const char* kst = ws + WS_KF + ((size_t)(h * 64 + 8 * j + 2 * wr) * 8 + 2 * wc) * 1024;
    const char* vst = ws + WS_VF + ((size_t)(h * 128 + 2 * (8 * j + 2 * wr)) * 4 + wc) * 1024;
#define KVST(i) gstore_sc1_c<(((i) & 1) ? (((i) >> 3) * 8 + (((i) >> 2) & 1) * 2 + (((i) >> 1) & 1)) * 4096 : (((i) >> 3) * 4 + (((i) >> 2) & 1)) * 8192 + (((i) >> 1) & 1) * 1024)>(vl16, kvp[i], ((i) & 1) ? vst : kst)
    unsigned voffW;
    { const int row = 8 * (wid >> 1) + ((lane >> 2) & 7); const int chq = 2 * (wid & 1) + (lane >> 5);
      const int ch = 4 * chq + ((lane & 3) ^ ((row >> 2) & 3)); voffW = (unsigned)(row * 8192 + ch * 16); }
    const char* wqb = (const char*)wq + h * 256;
    const unsigned dstw = lds0 + L_Q + wid * 1024;
#define STAGE_Q1(c, i) glds16_c<(c) * 64 * 8192 + (i) * 32 * 8192>(voffW, wqb, dstw + (((c) + 2) & 3) * 16384 + (i) * 8192)
    const char* xb = ws + WS_XNF + (size_t)(8 * j + wid) * 40 * 1024;
    int boff[2];
    { const int blk = (lane >> 4) & 1, q = (lane & 15) >> 2, p = lane & 3;
#pragma unroll
      for (int t = 0; t < 2; ++t) boff[t] = L_Q + 2048 * hh + 64 * (4 * t + q) + (((2 * blk + (p >> 1)) ^ ((2 * hh + t) & 3)) << 4) + 8 * (p & 1); }
    u32x4 xf[3][4];
    f32x16 acc[4] = {};
    bf16x8 aq[6];
    unsigned fv = 0;
    ldscp bp0 = lds + boff[0], bp1 = lds + boff[1];
#define ARD(u) aq[(u) % 6] = cat8(vtr(bp0 + ((((u) >> 4) + 2) & 3) * 16384 + (((u) >> 2) & 3) * 4096 + ((u) & 3) * 512), vtr(bp1 + ((((u) >> 4) + 2) & 3) * 16384 + (((u) >> 2) & 3) * 4096 + ((u) & 3) * 512))
    GLOAD4(xf[0], vl16, xb);
    GLOAD4(xf[1], vl16, xb + 4096); STAGE_Q1(2, 0); STAGE_Q1(2, 1);
#pragma unroll
    for (int u = 0; u < 5; ++u) ARD(u);
    static_for<10>([&](auto cc_) {
      constexpr int c = decltype(cc_)::value;
#define QW(K) WAITVN(qproj_allowed(c, K))
      if (cls == 0) QW(0); else if (cls == 1) QW(1); else if (cls == 2) QW(2); else QW(3);
      asm volatile("" : "+v"(xf[c % 3][0]), "+v"(xf[c % 3][1]), "+v"(xf[c % 3][2]), "+v"(xf[c % 3][3]) :: "memory");
#undef QW
      if (c == 9 && wid == 0) {
        asm volatile("s_waitcnt vmcnt(0)" : "+v"(fv) :: "memory");
        kvmask = (unsigned)__builtin_amdgcn_ballot_w64(fv != 0u) & 0xffu;
        if (!(kvmask & 1u)) { wait_word(flags + FL_KV + 16 * (8 * h), 1u, flags + FL_TMO); kvmask |= 1u; }
        if (j == 0 && !(kvmask & 16u)) { wait_word(flags + FL_KV + 16 * (8 * h + 4), 1u, flags + FL_TMO); kvmask |= 16u; }
      }
      SBAR(); BAR();
      if (tid == 0 && ((cls == 0 && c == 8) || (cls == 1 && c == 9)))
        __hip_atomic_store(flags + FL_KV + 16 * (8 * h + j), 1u, __ATOMIC_RELAXED, __HIP_MEMORY_SCOPE_AGENT);
      SBAR();
      const char* xn_ = xb + (c + 2) * 4096;
      static_for<16>([&](auto ii_) {
        constexpr int i = decltype(ii_)::value;
        constexpr int u = 16 * c + i;
        acc[i & 3] = MFMA32(aq[u % 6], __builtin_bit_cast(bf16x8, xf[c % 3][i >> 2]), acc[i & 3]);
        if constexpr (u + 5 < 160) ARD(u + 5);
        if (c + 2 < 10) {
          if (i == 1) GLOAD1(xf[(c + 2) % 3][0], vl16, xn_, 0);
          if (i == 3) GLOAD1(xf[(c + 2) % 3][1], vl16, xn_, 1024);
          if (i == 5) GLOAD1(xf[(c + 2) % 3][2], vl16, xn_, 2048);
          if (i == 7) GLOAD1(xf[(c + 2) % 3][3], vl16, xn_, 3072);
        }
        if constexpr (c + 3 < 10) { if constexpr (i == 8) STAGE_Q1(c + 3, 0); if constexpr (i == 9) STAGE_Q1(c + 3, 1); }
        if constexpr (i >= 10) {
          static_for<4>([&](auto kk_) {
            constexpr int k = decltype(kk_)::value;
            constexpr int cnt = qproj_ns(c, k), base = qproj_sbase(c, k);
            if constexpr (((i - 10) & 1) == 0 && (i - 10) / 2 < cnt) { if (cls == k) KVST(base + (i - 10) / 2); }
          });
        }
        if (c == 8 && i == 8 && wid == 0)
          asm volatile("s_nop 4\n\tglobal_load_dword %0, %1, %2 sc1" : "+v"(fv) : "v"((unsigned)(FL_KV + 16 * (8 * h + (lane & 7))) * 4u), "s"(flags) : "memory");
        if (c == 9) {
          if (i == 0) DMA_T(Kh + (size_t)TILE(0) * 16384, L_K);
          if (i == 2) DMA_T(Vh + (size_t)TILE(0) * 16384, L_V);
          if (i == 4) DMA_T(Kh + (size_t)TILE(1) * 16384, L_K + 16384);
          if (i == 6) DMA_T(Vh + (size_t)TILE(1) * 16384, L_V + 16384);
          if (i == 8) DMA_T(Kh + (size_t)TILE(2) * 16384, L_K + 32768);
          if (i == 9) DMA_T(Kh + (size_t)TILE(3) * 16384, L_K + 49152);
        }
        SBAR();
      });
    });
#pragma unroll
    for (int dt = 0; dt < 4; ++dt) { acc[dt] = acc[dt] * QSCALE; qr[2 * dt] = pack8(acc[dt], 0); qr[2 * dt + 1] = pack8(acc[dt], 1); }
    if (cls == 3) WAIT_V(13); else WAIT_V(10);
    BAR();
    { const char* qst = ws + WS_QF + (size_t)(h * 64 + 8 * j + wid) * 8192;
      static_for<8>([&](auto ss_) { constexpr int s = decltype(ss_)::value; gstore_sc1_c<s * 1024>(vl16, qr[s], qst); }); }
#undef KVST
#undef STAGE_Q1
#undef ARD
#undef TILE
#undef DMA_T
  }
  const PfW pfw = {(const char*)wo, (const char*)w1, (const char*)w2};
  if (wid < 4) attn_main<0>(lds, lds0, ws, flags, h, j, wid, lane, tid, qr, kvmask, pfw);
  else attn_main<1>(lds, lds0, ws, flags, h, j, wid, lane, tid, qr, kvmask, pfw);
  WAIT_V(0); WAIT_L(0); BAR();
  if (tid == 0) __hip_atomic_fetch_add(flags + FL_C + 16 * ((7 - j) * 4 + (h >> 3)), 1u, __ATOMIC_RELAXED, __HIP_MEMORY_SCOPE_AGENT);
  tail_phase(smem, x, wo, w1, b1, w2, b2, ws, out, wid);
}

extern "C" void kernel_launch(void* const* d_in, const int* in_sizes, int n_in, void* d_out, int out_size, void* d_ws, size_t ws_size,
                              hipStream_t stream) {
  const u16* x = (const u16*)d_in[0];
  const u16* ckv = (const u16*)d_in[1];
  const u16* wuk = (const u16*)d_in[3];
  const u16* wuv = (const u16*)d_in[4];
  char* ws = (char*)d_ws;
  const u16* wq = (const u16*)d_in[2];
  const u16* wo = (const u16*)d_in[5];
  const u16* w1 = (const u16*)d_in[7];
  const u16* b1 = (const u16*)d_in[8];
  const u16* w2 = (const u16*)d_in[9];
  const u16* b2 = (const u16*)d_in[10];
  (void)hipFuncSetAttribute((const void*)kF1, hipFuncAttributeMaxDynamicSharedMemorySize, F1_LDS);
  hipLaunchKernelGGL(k0_ln, dim3(256), dim3(512), 0, stream, x, (u16*)(ws + WS_XNF), (unsigned*)(ws + WS_FLAGS));
  hipLaunchKernelGGL(kF1, dim3(256), dim3(512), F1_LDS, stream, ckv, wuk, wuv, wq, wo, w1, w2, x, b1, b2, ws, (u16*)d_out);
}
```

```cpp
#include <hip/hip_runtime.h>
#include <stdint.h>

#define LAS __attribute__((address_space(3)))
#define DI __device__ __forceinline__
typedef unsigned short u16;
typedef short bf16x8 __attribute__((ext_vector_type(8)));
typedef short s16x4 __attribute__((ext_vector_type(4)));
typedef float f32x2 __attribute__((ext_vector_type(2)));
typedef float f32x4 __attribute__((ext_vector_type(4)));
typedef float f32x16 __attribute__((ext_vector_type(16)));
typedef unsigned u32x2 __attribute__((ext_vector_type(2)));
typedef unsigned u32x4 __attribute__((ext_vector_type(4)));
typedef __bf16 bf16x2_t __attribute__((ext_vector_type(2)));
typedef LAS unsigned char* ldsp;
typedef LAS const unsigned char* ldscp;

constexpr int SEQ = 2048, DM = 640, DC = 512, NH = 32, HD = 128, FF = 1728;
constexpr float LN_EPS = 1e-5f;

constexpr size_t WS_FLAGS = 0;
constexpr size_t WS_XNF = 131072;
constexpr size_t WS_KF = WS_XNF + (size_t)SEQ * DM * 2;
constexpr size_t WS_VF = WS_KF + (size_t)NH * SEQ * HD * 2;
constexpr size_t WS_CTX = WS_VF + (size_t)NH * SEQ * HD * 2;
constexpr size_t WS_SLAB = WS_CTX + (size_t)NH * SEQ * HD * 2;
constexpr size_t WS_H = WS_SLAB + (size_t)4 * SEQ * DM * 4;
constexpr size_t WS_HNF = WS_H + (size_t)SEQ * DM * 2;
constexpr size_t WS_UF = WS_HNF + (size_t)SEQ * DM * 2;
constexpr size_t WS_QF = WS_UF + (size_t)DM * FF * 2;
constexpr size_t WS_PF = WS_QF + (size_t)NH * SEQ * HD * 2;
constexpr size_t WS_PS = WS_PF + (size_t)NH * SEQ * HD * 2;
constexpr size_t WS_END = WS_PS + (size_t)NH * 8 * 8 * 64 * 8;
constexpr int FL_TMO = 0;
constexpr int FL_KV = 24576;
constexpr int FL_Q = 4096;
constexpr int FL_P = 4096 + 8192;
constexpr int FL_WORDS = 32768;

#define SBAR() __builtin_amdgcn_sched_barrier(0)
#define BAR() asm volatile("s_barrier" ::: "memory")
#define WAIT_V(n) asm volatile("s_waitcnt vmcnt(" #n ")" ::: "memory")
#define WAIT_L(n) asm volatile("s_waitcnt lgkmcnt(" #n ")" ::: "memory")
#define MFMA32(a, b, c) __builtin_amdgcn_mfma_f32_32x32x16_bf16((a), (b), (c), 0, 0, 0)
#define MFMA16(a, b, c) __builtin_amdgcn_mfma_f32_16x16x32_bf16((a), (b), (c), 0, 0, 0)

DI unsigned cvtpk(float lo, float hi) { f32x2 v = {lo, hi}; bf16x2_t b = __builtin_convertvector(v, bf16x2_t); return __builtin_bit_cast(unsigned, b); }
DI float bflo(unsigned u) { return __uint_as_float(u << 16); }
DI float bfhi(unsigned u) { return __uint_as_float(u & 0xffff0000u); }
DI unsigned rfl(unsigned x) { return __builtin_amdgcn_readfirstlane(x); }
DI void glds16(unsigned voff, const void* sbase, unsigned lds_dst) {
  unsigned keep;
  asm volatile("s_mov_b32 %0, m0\n\ts_mov_b32 m0, %3\n\ts_nop 2\n\tglobal_load_lds_dwordx4 %1, %2\n\ts_mov_b32 m0, %0"
               : "=&s"(keep) : "v"(voff), "s"(sbase), "s"(lds_dst) : "memory");
}
DI void glds16x4(unsigned voff, const void* sbase, unsigned lds_dst) {
  unsigned keep;
  asm volatile("s_mov_b32 %0, m0\n\ts_mov_b32 m0, %3\n\ts_nop 2\n\tglobal_load_lds_dwordx4 %1, %2\n\tglobal_load_lds_dwordx4 %1, %2 offset:1024\n\t"
               "global_load_lds_dwordx4 %1, %2 offset:2048\n\tglobal_load_lds_dwordx4 %1, %2 offset:3072\n\ts_mov_b32 m0, %0"
               : "=&s"(keep) : "v"(voff), "s"(sbase), "s"(lds_dst) : "memory");
}
typedef short v4i16_t __attribute__((ext_vector_type(4)));
DI s16x4 vtr(ldscp p) { return __builtin_bit_cast(s16x4, __builtin_amdgcn_ds_read_tr16_b64_v4i16((LAS v4i16_t*)p)); }
DI bf16x8 cat8(s16x4 lo, s16x4 hi) { return __builtin_shufflevector(lo, hi, 0, 1, 2, 3, 4, 5, 6, 7); }
DI bf16x8 ldsr128(ldscp p) { return *(const LAS bf16x8*)p; }
DI bf16x8 pack8(const f32x16& x, int s) {
  u32x4 w = {cvtpk(x[8 * s], x[8 * s + 1]), cvtpk(x[8 * s + 2], x[8 * s + 3]), cvtpk(x[8 * s + 4], x[8 * s + 5]), cvtpk(x[8 * s + 6], x[8 * s + 7])};
  return __builtin_bit_cast(bf16x8, w);
}
DI float wave_sum(float v) {
#pragma unroll
  for (int o = 32; o >= 1; o >>= 1) v += __shfl_xor(v, o);
  return v;
}

__global__ __launch_bounds__(512) void k0_ln(const u16* __restrict__ x, u16* __restrict__ xnf, unsigned* __restrict__ flags) {
  const int w = threadIdx.x >> 6, lane = threadIdx.x & 63;
  const int q = blockIdx.x * 8 + w;
  if (threadIdx.x < 128) flags[blockIdx.x * 128 + threadIdx.x] = 0u;
  const u32x4* row = (const u32x4*)(x + (size_t)q * DM);
  u32x4 c0 = row[lane];
  u32x4 c1 = {0u, 0u, 0u, 0u};
  if (lane < 16) c1 = row[64 + lane];
  float v[16];
#pragma unroll
  for (int i = 0; i < 4; ++i) { v[2 * i] = bflo(c0[i]); v[2 * i + 1] = bfhi(c0[i]); v[8 + 2 * i] = bflo(c1[i]); v[8 + 2 * i + 1] = bfhi(c1[i]); }
  float s = 0.f;
#pragma unroll
  for (int i = 0; i < 16; ++i) s += v[i];
  const float mean = wave_sum(s) * (1.0f / DM);
  float ss = 0.f;
#pragma unroll
  for (int i = 0; i < 8; ++i) { const float d = v[i] - mean; ss += d * d; }
  if (lane < 16) {
#pragma unroll
    for (int i = 8; i < 16; ++i) { const float d = v[i] - mean; ss += d * d; }
  }
  const float rstd = 1.0f / sqrtf(wave_sum(ss) * (1.0f / DM) + LN_EPS);
  u32x4 o0, o1;
#pragma unroll
  for (int i = 0; i < 4; ++i) { o0[i] = cvtpk((v[2 * i] - mean) * rstd, (v[2 * i + 1] - mean) * rstd); o1[i] = cvtpk((v[8 + 2 * i] - mean) * rstd, (v[8 + 2 * i + 1] - mean) * rstd); }
  u32x4* dst = (u32x4*)xnf + (size_t)(q >> 5) * 40 * 64 + (q & 31);
  dst[(lane >> 1) * 64 + (lane & 1) * 32] = o0;
  if (lane < 16) dst[((64 + lane) >> 1) * 64 + (lane & 1) * 32] = o1;
}

constexpr int F1_LDS = 163840;
constexpr int L_K = 0, L_V = 65536, L_Q = 98304;
constexpr float QSCALE = 0.08838834764831845f * 1.4426950408889634f;
constexpr float THR = 16.f;
#define PIN(x) asm volatile("" : "+v"(x))
#define EX2(v) __builtin_amdgcn_exp2f(v)
#define MEMFENCE() asm volatile("" ::: "memory")
DI float max3f(float a, float b, float c) { return __builtin_fmaxf(__builtin_fmaxf(a, b), c); }
DI float xhalf_max(float m) { auto rr = __builtin_amdgcn_permlane32_swap(__float_as_uint(m), __float_as_uint(m), false, false); return __builtin_fmaxf(__uint_as_float(rr[0]), __uint_as_float(rr[1])); }
DI float xhalf_sum(float m) { auto rr = __builtin_amdgcn_permlane32_swap(__float_as_uint(m), __float_as_uint(m), false, false); return __uint_as_float(rr[0]) + __uint_as_float(rr[1]); }
DI float max16(const f32x16& p) {
  float a = max3f(p[0], p[1], p[2]), b = max3f(p[3], p[4], p[5]);
  a = max3f(a, p[6], p[7]); b = max3f(b, p[8], p[9]); a = max3f(a, p[10], p[11]); b = max3f(b, p[12], p[13]); a = max3f(a, p[14], p[15]);
  return __builtin_fmaxf(a, b);
}
DI void cmask(f32x16& p0, f32x16& p1, int dq) {
#pragma unroll
  for (int r = 0; r < 16; ++r) { const int c = (r & 3) + 8 * (r >> 2); if (c > dq) p0[r] = -__builtin_inff(); if (c + 32 > dq) p1[r] = -__builtin_inff(); }
}
#define GLOAD4(d, voff, sbase) asm volatile("s_nop 4\n\tglobal_load_dwordx4 %0, %4, %5\n\tglobal_load_dwordx4 %1, %4, %5 offset:1024\n\tglobal_load_dwordx4 %2, %4, %5 offset:2048\n\tglobal_load_dwordx4 %3, %4, %5 offset:3072" \
    : "=&v"(d[0]), "=&v"(d[1]), "=&v"(d[2]), "=&v"(d[3]) : "v"(voff), "s"(sbase) : "memory")
#define GLOAD1(d, voff, sbase, OFF) asm volatile("s_nop 4\n\tglobal_load_dwordx4 %0, %1, %2 offset:" #OFF : "=&v"(d) : "v"(voff), "s"(sbase) : "memory")
#define GLOAD1_SC1(d, voff, sbase, OFF) asm volatile("s_nop 4\n\tglobal_load_dwordx4 %0, %1, %2 offset:" #OFF " sc1" : "=&v"(d) : "v"(voff), "s"(sbase) : "memory")
#define WAITV_TIE4(n, d) asm volatile("s_waitcnt vmcnt(" #n ")" : "+v"(d[0]), "+v"(d[1]), "+v"(d[2]), "+v"(d[3]) :: "memory")
DI void gstore_sc1(unsigned voff, const bf16x8& v, const void* sbase) {
  asm volatile("s_nop 4\n\tglobal_store_dwordx4 %0, %1, %2 sc1\n\ts_nop 1" :: "v"(voff), "v"(v), "s"(sbase) : "memory");
}
template <int OFFS> DI void gstore_sc1_c(unsigned voff, const bf16x8& v, const void* sbase) {
  unsigned t;
  asm volatile("v_add_u32 %0, %4, %1\n\ts_nop 3\n\tglobal_store_dwordx4 %0, %2, %3 sc1\n\ts_nop 1" : "=&v"(t) : "v"(voff), "v"(v), "s"(sbase), "i"(OFFS) : "memory");
}
template <int OFFS> DI void glds16_c(unsigned voff, const void* sbase, unsigned lds_dst) {
  unsigned keep, t;
  asm volatile("s_mov_b32 %0, m0\n\ts_mov_b32 m0, %4\n\tv_add_u32 %1, %5, %2\n\ts_nop 1\n\tglobal_load_lds_dwordx4 %1, %3\n\ts_mov_b32 m0, %0"
               : "=&s"(keep), "=&v"(t) : "v"(voff), "s"(sbase), "s"(lds_dst), "i"(OFFS) : "memory");
}
#define GLDS_OFF(voff, sbase, ldst, OFF) do { unsigned keep_; asm volatile("s_mov_b32 %0, m0\n\ts_mov_b32 m0, %3\n\ts_nop 2\n\tglobal_load_lds_dwordx4 %1, %2 offset:" #OFF "\n\ts_mov_b32 m0, %0" \
    : "=&s"(keep_) : "v"(voff), "s"(sbase), "s"(ldst) : "memory"); } while (0)
#define WAITVN(n) asm volatile("s_waitcnt vmcnt(%0)" :: "i"(n) : "memory")
DI const char* upt(const char* p) {
  const unsigned long long v = (unsigned long long)p;
  return (const char*)(((unsigned long long)rfl((unsigned)(v >> 32)) << 32) | rfl((unsigned)v));
}
DI void wait_word(unsigned* word, unsigned want, unsigned* tmo) {
  for (unsigned spins = 0;; ++spins) {
    if ((unsigned)rfl(__hip_atomic_load(word, __ATOMIC_RELAXED, __HIP_MEMORY_SCOPE_AGENT)) >= want) break;
    if (spins > (1u << 16)) { __hip_atomic_store(tmo, 1u, __ATOMIC_RELAXED, __HIP_MEMORY_SCOPE_AGENT); break; }
    __builtin_amdgcn_s_sleep(1);
  }
}
#define ACQUIRE() __builtin_amdgcn_fence(__ATOMIC_ACQUIRE, "agent")

template <int V> struct IC { static constexpr int value = V; };
template <int N, int I = 0, class F> DI void static_for(F&& f) { if constexpr (I < N) { f(IC<I>{}); static_for<N, I + 1>(f); } }
DI int lane_id() { unsigned l; asm volatile("v_mbcnt_lo_u32_b32 %0, -1, 0\n\tv_mbcnt_hi_u32_b32 %0, -1, %0" : "=v"(l)); return (int)l; }
struct PfW { const char* wo; const char* w1; const char* w2; };
template <int GRP>
DI void attn_main(ldsp lds, unsigned lds0, char* __restrict__ ws, unsigned* flags, int h, int j, int wid, int lane, int tid, bf16x8 (&qr)[8], unsigned kvmask, const PfW& pfw) {
  const int l31 = lane & 31, hh = lane >> 5;
  const unsigned vl16 = lane * 16;
  const char* Kh = ws + WS_KF + (size_t)h * 64 * 8192;
  const char* Vh = ws + WS_VF + (size_t)h * 128 * 4096;
  const unsigned vt2 = (unsigned)(wid * 2048 + lane * 16);
  const unsigned dt2 = lds0 + wid * 2048;
  const int n1 = j >= 4 ? 16 : 4 * j + 2;
  const int t2 = j >= 4 ? 4 * (8 - j) - 2 : 16;
  const int jp = 7 - j;
#define TILE(g) ((g) < n1 ? (g) : ((g) < 18 ? t2 + (g) - n1 : 0))
#define KSRC(g) (Kh + (size_t)TILE(g) * 16384)
#define VSRC(g) (Vh + (size_t)TILE(g) * 16384)
#define GLDS_T(src, dst, OFF) do { unsigned keep_; asm volatile("s_mov_b32 %0, m0\n\ts_mov_b32 m0, %3\n\ts_nop 2\n\tglobal_load_lds_dwordx4 %1, %2 offset:" #OFF " sc1\n\ts_mov_b32 m0, %0" \
    : "=&s"(keep_) : "v"(vt2), "s"(src), "s"(dst) : "memory"); } while (0)
  const unsigned stoff = lane < 8 ? (unsigned)(FL_KV + 16 * (8 * h + lane)) * 4u : (lane == 8 ? (unsigned)(FL_Q + 32 * (8 * h + jp)) * 4u : (unsigned)(FL_P + 32 * (8 * h + jp)) * 4u);
  unsigned fv = 0;
#define ST_ISSUE() do { if (wid == 0 && kvmask != 0x3ffu) asm volatile("s_nop 4\n\tglobal_load_dword %0, %1, %2 sc1" : "+v"(fv) : "v"(stoff), "s"(flags) : "memory"); } while (0)
#define ST_READ(N) do { if (wid == 0 && kvmask != 0x3ffu) { asm volatile("s_waitcnt vmcnt(" #N ")" : "+v"(fv) :: "memory"); kvmask |= (unsigned)__builtin_amdgcn_ballot_w64(fv != 0u) & 0x3ffu; } } while (0)
#define ENSURE_KV(g) do { if (wid == 0) { const int bk_ = TILE(g) >> 2; if (!((kvmask >> bk_) & 1u)) { wait_word(flags + FL_KV + 16 * (8 * h + bk_), 1u, flags + FL_TMO); kvmask |= 1u << bk_; } } } while (0)
  const int rdoff = lane * 16;
  bf16x8 kq[4], vq[4], pw[4];
  f32x16 C0, C1, o[4], negm;
  float mhat, lsum;
  const f32x16 z = {};
  unsigned pfd = 0;
#pragma unroll
  for (int i = 0; i < 4; ++i) kq[i] = ldsr128(lds + L_K + rdoff + i * 1024);
  {
    ST_ISSUE();
    ldscp kb = lds + L_K + rdoff;
#pragma unroll
    for (int u = 0; u < 16; ++u) {
      if (u < 8) C0 = MFMA32(kq[u & 3], qr[u & 7], u == 0 ? z : C0); else C1 = MFMA32(kq[u & 3], qr[u & 7], u == 8 ? z : C1);
      if (u + 4 < 16) kq[u & 3] = ldsr128(kb + (u + 4) * 1024);
      if (GRP == 1 && u == 11) {
        SBAR(); WAIT_V(0); WAIT_L(0); BAR();
        GLDS_T(VSRC(2), dt2 + L_V + 32768, 0); GLDS_T(VSRC(2), dt2 + L_V + 32768, 1024);
        GLDS_T(KSRC(4), dt2 + L_K, 0);
      }
      SBAR();
    }
    if (j == 0) cmask(C0, C1, 32 * wid + l31 - 4 * hh);
    float rm = xhalf_max(__builtin_fmaxf(max16(C0), max16(C1)));
    mhat = __builtin_fmaxf(rm, -1e30f);
#pragma unroll
    for (int r = 0; r < 16; ++r) { C0[r] -= mhat; C1[r] -= mhat; negm[r] = -mhat; }
    PIN(negm);
    if (GRP == 0) {
      SBAR(); WAIT_V(0); ST_READ(0); ENSURE_KV(2); ENSURE_KV(4);
      WAIT_L(0); BAR();
      if (tid == 0) {
        __hip_atomic_store(flags + FL_Q + 32 * (8 * h + j), 1u, __ATOMIC_RELAXED, __HIP_MEMORY_SCOPE_AGENT);
        if ((j & 3) >= 2) __hip_atomic_store(flags + FL_KV + 16 * (8 * h + j), 1u, __ATOMIC_RELAXED, __HIP_MEMORY_SCOPE_AGENT);
      }
      GLDS_T(VSRC(2), dt2 + L_V + 32768, 0);
    } else {
      GLDS_T(KSRC(4), dt2 + L_K, 1024);
    }
#pragma unroll
    for (int i = 0; i < 4; ++i) kq[i] = ldsr128(lds + L_K + 16384 + rdoff + i * 1024);
    float s0 = 0.f;
#pragma unroll
    for (int r = 0; r < 16; ++r) { C0[r] = EX2(C0[r]); s0 += C0[r]; }
#pragma unroll
    for (int r = 0; r < 8; ++r) C1[r] = EX2(C1[r]);
    lsum = s0;
    pw[0] = pack8(C0, 0); pw[1] = pack8(C0, 1);
#pragma unroll
    for (int d = 0; d < 4; ++d) o[d] = z;
  }
  for (int g = 1; g < 18; ++g) {
    const bool seam = (g == n1);
    const int X = g < n1 ? j : jp;
    const int dd = TILE(g) - 4 * X;
    ldscp kb = lds + L_K + (g & 3) * 16384 + rdoff;
    ldscp vb = lds + L_V + ((g - 1) & 3) * 16384 + rdoff;
    ldscp kn = lds + L_K + ((g + 1) & 3) * 16384 + rdoff;
    const char* vs_n = VSRC(g + 2); const char* ks_n = KSRC(g + 4);
    const char* vs_p = VSRC(g + 1); const char* ks_p = KSRC(g + 3);
    const unsigned vd_n = dt2 + L_V + ((g + 2) & 3) * 16384, kd_n = dt2 + L_K + (g & 3) * 16384;
    const unsigned vd_p = dt2 + L_V + ((g + 1) & 3) * 16384, kd_p = dt2 + L_K + ((g - 1) & 3) * 16384;
    const bool gq = (GRP == 0) || g > 1;
    float lsum1 = 0.f, mhat1 = 0.f;
    if (seam) {
      mhat1 = mhat; mhat = 0.f;
#pragma unroll
      for (int r = 0; r < 16; ++r) negm[r] = 0.f;
      PIN(negm);
    }
    if (g >= 2 && g < 12 && wid == ((g - 2) & 7)) {
      const unsigned lc = (unsigned)(g - 2) * 64u + lane, gid = blockIdx.x * 592u + lc;
      const char* p = gid < 40960u ? pfw.wo + (size_t)gid * 128 : (gid < 96256u ? pfw.w1 + (size_t)(gid - 40960u) * 128 : pfw.w2 + (size_t)(gid - 96256u) * 128);
      if (lc < 592u && gid < 151552u) asm volatile("global_load_dword %0, %1, off" : "+v"(pfd) : "v"(p) : "memory");
    }
    ST_ISSUE();
    float sa = 0.f, sb = 0.f;
    SBAR();
#define KRD(u) kq[(u) & 3] = ldsr128(kb + ((u) + 4) * 1024)
#define VRD(v) vq[(v) & 3] = ldsr128(vb + (v) * 1024)
#define KPRE(i) kq[i] = ldsr128(kn + (i) * 1024)
#define BARRIER_G() do { if (GRP == 0) { ST_READ(3); ENSURE_KV(g + 4); if (wid == 0 && g == n1 - 2 && j != 0 && !(kvmask & 256u)) wait_word(flags + FL_Q + 32 * (8 * h + jp), 1u, flags + FL_TMO); \
        if (g == 17 && wid == 0 && lane == 0) *(LAS unsigned*)(lds + 131072) = kvmask; } \
      if (g < 17) WAIT_V(6); else WAIT_V(0); WAIT_L(0); BAR(); SBAR(); \
      if (GRP == 0 && (g == n1 + 2 || (g == 17 && n1 + 2 > 17)) && tid == 0) \
        __hip_atomic_store(flags + FL_P + 32 * (8 * h + j), 1u, __ATOMIC_RELAXED, __HIP_MEMORY_SCOPE_AGENT); } while (0)
    C0 = MFMA32(kq[0], qr[0], negm); KRD(0); C1[8] = EX2(C1[8]); sa += C1[0]; sb += C1[1]; sa += C1[2]; sb += C1[3]; PIN(C1); PIN(sa); PIN(sb); SBAR();
    C0 = MFMA32(kq[1], qr[1], C0); KRD(1); C1[9] = EX2(C1[9]); sa += C1[4]; sb += C1[5]; sa += C1[6]; sb += C1[7]; PIN(C1); PIN(sa); PIN(sb); SBAR();
    C0 = MFMA32(kq[2], qr[2], C0); KRD(2); if (GRP == 0) GLDS_T(vs_p, vd_p, 1024); else if (gq) GLDS_T(ks_p, kd_p, 1024);
        C1[10] = EX2(C1[10]); pw[2] = pack8(C1, 0); PIN(C1); PIN(pw[2]); SBAR();
    C0 = MFMA32(kq[3], qr[3], C0); KRD(3); C1[11] = EX2(C1[11]); C1[12] = EX2(C1[12]); sa += C1[8]; sb += C1[9]; PIN(C1); PIN(sa); PIN(sb); SBAR();
    C0 = MFMA32(kq[0], qr[4], C0); KRD(4); C1[13] = EX2(C1[13]); C1[14] = EX2(C1[14]); sa += C1[10]; sb += C1[11]; PIN(C1); PIN(sa); PIN(sb); SBAR();
    C0 = MFMA32(kq[1], qr[5], C0); KRD(5); C1[15] = EX2(C1[15]); sa += C1[12]; sb += C1[13]; sa += C1[14]; PIN(C1); PIN(sa); PIN(sb); SBAR();
    C0 = MFMA32(kq[2], qr[6], C0); KRD(6); sb += C1[15]; pw[3] = pack8(C1, 1); PIN(pw[3]); PIN(sb); SBAR();
    C0 = MFMA32(kq[3], qr[7], C0); KRD(7); lsum += sa + sb; PIN(lsum); SBAR();
    C1 = MFMA32(kq[0], qr[0], negm); KRD(8); if (GRP == 0) GLDS_T(ks_p, kd_p, 0); SBAR();
    C1 = MFMA32(kq[1], qr[1], C1); KRD(9); SBAR();
    C1 = MFMA32(kq[2], qr[2], C1); KRD(10); SBAR();
    float mxa, mxb;
    C1 = MFMA32(kq[3], qr[3], C1); KRD(11); mxa = max3f(C0[0], C0[1], C0[2]); mxb = max3f(C0[3], C0[4], C0[5]); PIN(mxa); PIN(mxb); SBAR();
    if (GRP == 1) BARRIER_G();
    C1 = MFMA32(kq[0], qr[4], C1); VRD(0); if (GRP == 1) GLDS_T(vs_n, vd_n, 0); mxa = max3f(mxa, C0[6], C0[7]); mxb = max3f(mxb, C0[8], C0[9]); PIN(mxa); PIN(mxb); SBAR();
    C1 = MFMA32(kq[1], qr[5], C1); VRD(1); mxa = max3f(mxa, C0[10], C0[11]); mxb = max3f(mxb, C0[12], C0[13]); PIN(mxa); PIN(mxb); SBAR();
    C1 = MFMA32(kq[2], qr[6], C1); VRD(2); mxa = max3f(mxa, C0[14], C0[15]); mxa = __builtin_fmaxf(mxa, mxb); PIN(mxa); SBAR();
    C1 = MFMA32(kq[3], qr[7], C1); VRD(3); SBAR();
    if (g == n1 - 1) {
      if (j == 0) wait_word(flags + FL_Q + 32 * (8 * h + jp), 1u, flags + FL_TMO);
      const char* qsrc = ws + WS_QF + (size_t)(h * 64 + 8 * jp + wid) * 8192;
      GLOAD1_SC1(qr[0], vl16, qsrc, 0); GLOAD1_SC1(qr[1], vl16, qsrc, 1024); GLOAD1_SC1(qr[2], vl16, qsrc, 2048); GLOAD1_SC1(qr[3], vl16, qsrc, 3072);
      GLOAD1_SC1(qr[4], vl16, qsrc + 4096, 0); GLOAD1_SC1(qr[5], vl16, qsrc + 4096, 1024); GLOAD1_SC1(qr[6], vl16, qsrc + 4096, 2048); GLOAD1_SC1(qr[7], vl16, qsrc + 4096, 3072);
    }
    SBAR();
    o[0] = MFMA32(vq[0], pw[0], o[0]); VRD(4); if (GRP == 1) GLDS_T(vs_n, vd_n, 1024); SBAR();
    o[1] = MFMA32(vq[1], pw[0], o[1]); VRD(5); SBAR();
    float mxc, mxd;
    o[2] = MFMA32(vq[2], pw[0], o[2]); VRD(6); if (GRP == 0) GLDS_T(ks_p, kd_p, 1024);
        mxc = max3f(C1[0], C1[1], C1[2]); mxd = max3f(C1[3], C1[4], C1[5]); mxc = max3f(mxc, C1[6], C1[7]); mxd = max3f(mxd, C1[8], C1[9]); PIN(mxc); PIN(mxd); SBAR();
    o[3] = MFMA32(vq[3], pw[0], o[3]); VRD(7); mxc = max3f(mxc, C1[10], C1[11]); mxd = max3f(mxd, C1[12], C1[13]); mxc = max3f(mxc, C1[14], C1[15]); mxa = max3f(mxa, mxc, mxd); PIN(mxa); SBAR();
    o[0] = MFMA32(vq[0], pw[1], o[0]); VRD(8);
    float rm = mxa;
    MEMFENCE(); SBAR();
    if (dd >= 0) { cmask(C0, C1, 32 * wid + l31 - 64 * dd - 4 * hh); rm = __builtin_fmaxf(max16(C0), max16(C1)); }
    rm = xhalf_max(rm);
    float fsc = 1.f; bool resc = false;
    if (__builtin_expect(seam || __any(rm > THR), 0)) {
      const float dl = seam ? __builtin_fmaxf(rm, -1e30f) : __builtin_fmaxf(rm, 0.f);
      mhat += dl;
#pragma unroll
      for (int r = 0; r < 16; ++r) { C0[r] -= dl; C1[r] -= dl; negm[r] = -mhat; }
      PIN(negm);
      if (seam) { lsum1 = lsum; lsum = 0.f; }
      else { fsc = EX2(-dl); lsum *= fsc; resc = true; }
    }
    SBAR();
    sa = 0.f; sb = 0.f;
    o[1] = MFMA32(vq[1], pw[1], o[1]); VRD(9); C0[0] = EX2(C0[0]); C0[1] = EX2(C0[1]); C0[2] = EX2(C0[2]); PIN(C0); SBAR();
    o[2] = MFMA32(vq[2], pw[1], o[2]); VRD(10); C0[3] = EX2(C0[3]); C0[4] = EX2(C0[4]); sa += C0[0]; sb += C0[1]; PIN(C0); PIN(sa); PIN(sb); SBAR();
    o[3] = MFMA32(vq[3], pw[1], o[3]); VRD(11); C0[5] = EX2(C0[5]); C0[6] = EX2(C0[6]); sa += C0[2]; sb += C0[3]; PIN(C0); PIN(sa); PIN(sb); SBAR();
    o[0] = MFMA32(vq[0], pw[2], o[0]); VRD(12); if (GRP == 1) GLDS_T(ks_n, kd_n, 0); C0[7] = EX2(C0[7]); C0[8] = EX2(C0[8]); sa += C0[4]; sb += C0[5]; PIN(C0); PIN(sa); PIN(sb); SBAR();
    o[1] = MFMA32(vq[1], pw[2], o[1]); VRD(13); pw[0] = pack8(C0, 0); C0[9] = EX2(C0[9]); PIN(pw[0]); PIN(C0); SBAR();
    o[2] = MFMA32(vq[2], pw[2], o[2]); VRD(14); C0[10] = EX2(C0[10]); C0[11] = EX2(C0[11]); sa += C0[6]; sb += C0[7]; PIN(C0); PIN(sa); PIN(sb); SBAR();
    o[3] = MFMA32(vq[3], pw[2], o[3]); VRD(15); C0[12] = EX2(C0[12]); C0[13] = EX2(C0[13]); sa += C0[8]; sb += C0[9]; PIN(C0); PIN(sa); PIN(sb); SBAR();
    if (GRP == 0) BARRIER_G();
    o[0] = MFMA32(vq[0], pw[3], o[0]); KPRE(0); if (GRP == 0) GLDS_T(vs_n, vd_n, 0); C0[14] = EX2(C0[14]); C0[15] = EX2(C0[15]); sa += C0[10]; sb += C0[11]; PIN(C0); PIN(sa); PIN(sb); SBAR();
    o[1] = MFMA32(vq[1], pw[3], o[1]); KPRE(1); C1[0] = EX2(C1[0]); C1[1] = EX2(C1[1]); sa += C0[12]; sb += C0[13]; PIN(C1); PIN(sa); PIN(sb); SBAR();
    o[2] = MFMA32(vq[2], pw[3], o[2]); KPRE(2); pw[1] = pack8(C0, 1); C1[2] = EX2(C1[2]); C1[3] = EX2(C1[3]); PIN(pw[1]); PIN(C1); SBAR();
    o[3] = MFMA32(vq[3], pw[3], o[3]); KPRE(3); C1[4] = EX2(C1[4]); C1[5] = EX2(C1[5]); sa += C0[14]; sb += C0[15]; PIN(C1); PIN(sa); PIN(sb); SBAR();
    C1[6] = EX2(C1[6]); C1[7] = EX2(C1[7]); lsum += sa + sb; PIN(C1); PIN(lsum);
    if (resc) {
#pragma unroll
      for (int d = 0; d < 4; ++d) o[d] = o[d] * fsc;
    }
    if (seam) {
      const float lt = xhalf_sum(lsum1);
      const char* pst = ws + WS_PF + (size_t)((h * 8 + j) * 8 + wid) * 8192;
      static_for<4>([&](auto dd_) { constexpr int d = decltype(dd_)::value; gstore_sc1_c<(2 * d) * 1024>(vl16, pack8(o[d], 0), pst); gstore_sc1_c<(2 * d + 1) * 1024>(vl16, pack8(o[d], 1), pst); o[d] = z; });
      const unsigned long long sv = ((unsigned long long)__float_as_uint(lt) << 32) | __float_as_uint(mhat1);
      __hip_atomic_store((unsigned long long*)(ws + WS_PS) + (size_t)((h * 8 + j) * 8 + wid) * 64 + lane, sv, __ATOMIC_RELAXED, __HIP_MEMORY_SCOPE_AGENT);
    }
    if (g == n1 - 1) {
      asm volatile("s_waitcnt vmcnt(1)" : "+v"(qr[0]), "+v"(qr[1]), "+v"(qr[2]), "+v"(qr[3]), "+v"(qr[4]), "+v"(qr[5]), "+v"(qr[6]), "+v"(qr[7]) :: "memory");
    }
  }
  asm volatile("" :: "v"(pfd));
  {
    const bool early = (rfl(*(const LAS unsigned*)(lds + 131072)) & 512u) != 0u;
    const char* psrc = upt(ws + WS_PF + (size_t)((h * 8 + jp) * 8 + wid) * 8192);
    const char* psrc2 = upt(psrc + 4096);
    const unsigned long long* svp = (const unsigned long long*)(ws + WS_PS) + (size_t)((h * 8 + jp) * 8 + wid) * 64 + lane;
    u32x4 qa[4], qb[4];
    u32x2 svv;
#define PLOADS() do { GLOAD1_SC1(qa[0], vl16, psrc, 0); GLOAD1_SC1(qa[1], vl16, psrc, 1024); GLOAD1_SC1(qa[2], vl16, psrc, 2048); GLOAD1_SC1(qa[3], vl16, psrc, 3072); \
      GLOAD1_SC1(qb[0], vl16, psrc2, 0); GLOAD1_SC1(qb[1], vl16, psrc2, 1024); GLOAD1_SC1(qb[2], vl16, psrc2, 2048); GLOAD1_SC1(qb[3], vl16, psrc2, 3072); \
      asm volatile("global_load_dwordx2 %0, %1, off sc1" : "=&v"(svv) : "v"(svp) : "memory"); } while (0)
    if (early) PLOADS();
    ldscp vb = lds + L_V + (17 & 3) * 16384 + rdoff;
#pragma unroll
    for (int i = 0; i < 4; ++i) vq[i] = ldsr128(vb + i * 1024);
    float sa = 0.f;
#pragma unroll
    for (int r = 8; r < 16; ++r) C1[r] = EX2(C1[r]);
#pragma unroll
    for (int r = 0; r < 16; ++r) sa += C1[r];
    lsum += sa;
    pw[2] = pack8(C1, 0); pw[3] = pack8(C1, 1);
    SBAR();
#pragma unroll
    for (int v = 0; v < 16; ++v) {
      o[v & 3] = MFMA32(vq[v & 3], pw[v >> 2], o[v & 3]);
      if (v + 4 < 16) vq[v & 3] = ldsr128(vb + (v + 4) * 1024);
      SBAR();
    }
    lsum = xhalf_sum(lsum);
    if (!early) { wait_word(flags + FL_P + 32 * (8 * h + jp), 1u, flags + FL_TMO); PLOADS(); }
    WAITV_TIE4(0, qa); WAITV_TIE4(0, qb);
    asm volatile("" : "+v"(svv));
    const unsigned long long sv = ((unsigned long long)svv[1] << 32) | svv[0];
#undef PLOADS
    const float om = __uint_as_float((unsigned)sv), ol = __uint_as_float((unsigned)(sv >> 32));
    const float m = __builtin_fmaxf(mhat, om);
    const float f0 = EX2(mhat - m), f1 = EX2(om - m);
    const float inv = __builtin_amdgcn_rcpf(lsum * f0 + ol * f1);
    const float g0 = f0 * inv, g1 = f1 * inv;
    const char* cdst = upt(ws + WS_CTX + (size_t)((8 * jp + wid) * 32 + h) * 8192);
    static_for<8>([&](auto ii_) {
      constexpr int i = decltype(ii_)::value; constexpr int d = i >> 1, sp = i & 1;
      const u32x4 v4 = i < 4 ? qa[i & 3] : qb[i & 3];
      u32x4 w;
#pragma unroll
      for (int e = 0; e < 4; ++e) w[e] = cvtpk(o[d][8 * sp + 2 * e] * g0 + bflo(v4[e]) * g1, o[d][8 * sp + 2 * e + 1] * g0 + bfhi(v4[e]) * g1);
      gstore_sc1_c<i * 1024>(vl16, __builtin_bit_cast(bf16x8, w), cdst);
    });
  }
#undef KRD
#undef VRD
#undef KPRE
#undef BARRIER_G
#undef ENSURE_KV
#undef GLDS_T
#undef TILE
#undef KSRC
#undef VSRC
}

constexpr int T_LDS = 163840;
constexpr int FL_C = 28672;
constexpr int FL_P3 = 20480;
constexpr int FL_H = 22528;
constexpr int FL_U = 23552;
#define GLOAD4_SC1(d, voff, sbase) asm volatile("s_nop 4\n\tglobal_load_dwordx4 %0, %4, %5 sc1\n\tglobal_load_dwordx4 %1, %4, %5 offset:1024 sc1\n\tglobal_load_dwordx4 %2, %4, %5 offset:2048 sc1\n\tglobal_load_dwordx4 %3, %4, %5 offset:3072 sc1" \
    : "=&v"(d[0]), "=&v"(d[1]), "=&v"(d[2]), "=&v"(d[3]) : "v"(voff), "s"(sbase) : "memory")
#define GLDS_SC1(voff, sbase, ldst) do { unsigned keep_; asm volatile("s_mov_b32 %0, m0\n\ts_mov_b32 m0, %3\n\ts_nop 2\n\tglobal_load_lds_dwordx4 %1, %2 sc1\n\ts_mov_b32 m0, %0" \
    : "=&s"(keep_) : "v"(voff), "s"(sbase), "s"(ldst) : "memory"); } while (0)
template <int OFFS> DI void gstore_sc1_uc(unsigned voff, const u32x4& v, const void* sbase) {
  unsigned t;
  asm volatile("v_add_u32 %0, %4, %1\n\ts_nop 3\n\tglobal_store_dwordx4 %0, %2, %3 sc1\n\ts_nop 1" : "=&v"(t) : "v"(voff), "v"(v), "s"(sbase), "i"(OFFS) : "memory");
}
DI void gstore_sc1_u(unsigned voff, const u32x4& v, const void* sbase) {
  asm volatile("s_nop 4\n\tglobal_store_dwordx4 %0, %1, %2 sc1\n\ts_nop 1" :: "v"(voff), "v"(v), "s"(sbase) : "memory");
}
template <bool FIRST> struct MlpCfg {
  static constexpr int PITCH = FIRST ? FF * 2 : SEQ * 2;
  static constexpr int NKT = FIRST ? SEQ / 64 : FF / 64;
  static constexpr int NST = (NKT + 1) / 2;
};
template <bool FIRST> DI unsigned mlp_voffw(int wid, int lane) {
  const int row = 8 * wid + (lane >> 3); const int f = ((row >> 1) & 1) | (((row >> 3) & 1) << 1);
  return (unsigned)(row * MlpCfg<FIRST>::PITCH + (((lane & 7) ^ (f << 1)) << 4));
}
template <bool FIRST, int KT0, int NK> DI void mlp_wstage(unsigned lds0, const char* wb, unsigned voffW, int wid, int reg) {
  static_for<NK>([&](auto kk_) {
    constexpr int k = decltype(kk_)::value; constexpr int kt = (KT0 + k) < MlpCfg<FIRST>::NKT ? (KT0 + k) : MlpCfg<FIRST>::NKT - 1;
    glds16_c<kt * 64 * MlpCfg<FIRST>::PITCH>(voffW, wb, lds0 + reg + k * 8192 + wid * 1024);
  });
}
struct MlpW { ldscp p[4]; };
DI MlpW mlp_wptr(ldsp lds, int wid, int lane) {
  const int g = lane >> 4, q = (lane & 15) >> 2, p = lane & 3; const int f = (q >> 1) | ((g & 1) << 1);
  MlpW w;
#pragma unroll
  for (int ct = 0; ct < 4; ++ct) w.p[ct] = lds + (wid >> 1) * 8192 + 128 * (32 * (wid & 1) + 8 * g + q) + (((2 * ct + (p >> 1)) ^ (f << 1)) << 4) + 8 * (p & 1);
  return w;
}
DI void mlp_wrd(const MlpW& w, int off, bf16x8* dst) {
#pragma unroll
  for (int ct = 0; ct < 4; ++ct) dst[ct] = cat8(vtr(w.p[ct] + off), vtr(w.p[ct] + off + 512));
}
#define MLP_ALOAD(dst, base) asm volatile("s_nop 4\n\tglobal_load_dwordx4 %0, %5, %7 sc1\n\tglobal_load_dwordx4 %1, %5, %7 offset:1024 sc1\n\tglobal_load_dwordx4 %2, %5, %7 offset:2048 sc1\n\t" \
    "global_load_dwordx4 %3, %5, %7 offset:3072 sc1\n\tglobal_load_dwordx4 %4, %6, %7 sc1" \
    : "=&v"(dst[0]), "=&v"(dst[1]), "=&v"(dst[2]), "=&v"(dst[3]), "=&v"(dst[4]) : "v"(vl16), "v"(vl16b), "s"(base) : "memory")
template <bool FIRST, int R0, int R1, int LK0, int NW>
DI void mlp_main(ldsp lds, const char* __restrict__ ab, int wid, int lane, const MlpW& wp, const bf16x8 (&wr)[NW], f32x4 (&acc)[4][5]) {
  constexpr int NT = 2 * MlpCfg<FIRST>::NKT, NS = (NT + 7) / 8;
  const unsigned vl16 = lane * 16, vl16b = lane * 16 + 4096;
  u32x4 af[3][5];
  bf16x8 wl[2][4];
#pragma unroll
  for (int c = 0; c < 4; ++c)
#pragma unroll
    for (int d = 0; d < 5; ++d) acc[c][d] = (f32x4){0.f, 0.f, 0.f, 0.f};
  auto abase = [&](int s) { const int T = 8 * s + wid; return upt(ab + (size_t)(T < NT ? T : NT - 1) * 40960); };
  { const char* b0 = abase(0); MLP_ALOAD(af[0], b0); const char* b1 = abase(1); MLP_ALOAD(af[1], b1); }
  if constexpr (!(0 >= R0 && 0 < R1)) mlp_wrd(wp, (0 - LK0) * 8192, wl[0]);
  static_for<NS>([&](auto ss_) {
    constexpr int s = decltype(ss_)::value;
    if constexpr (s + 2 < NS) { const char* bn = abase(s + 2); MLP_ALOAD(af[(s + 2) % 3], bn); }
    if constexpr (s + 1 < NS && !(s + 1 >= R0 && s + 1 < R1)) mlp_wrd(wp, (4 * (s + 1) - LK0) * 8192, wl[(s + 1) & 1]);
    constexpr int ahead = (NS - 1 - s) < 2 ? (NS - 1 - s) : 2;
    asm volatile("s_waitcnt vmcnt(%5)" : "+v"(af[s % 3][0]), "+v"(af[s % 3][1]), "+v"(af[s % 3][2]), "+v"(af[s % 3][3]), "+v"(af[s % 3][4]) : "i"(5 * ahead) : "memory");
    SBAR();
    if (8 * s + 7 < NT || 8 * s + wid < NT) {
#pragma unroll
      for (int c = 0; c < 4; ++c) {
        const bf16x8 wfr = (s >= R0 && s < R1) ? wr[(s >= R0 && s < R1) ? (s - R0) * 4 + c : 0] : wl[s & 1][c];
#pragma unroll
        for (int d = 0; d < 5; ++d) {
          const bf16x8 a8 = __builtin_bit_cast(bf16x8, af[s % 3][d]);
          acc[c][d] = FIRST ? MFMA16(wfr, a8, acc[c][d]) : MFMA16(a8, wfr, acc[c][d]);
        }
      }
    }
    SBAR();
  });
}
DI void mlp_park(ldsp lds, int wid, int lane, f32x4 (&acc)[4][5]) {
  ldsp pw_ = lds + wid * 10240 + lane * 16;
  ldsp pr_ = lds + (wid ^ 4) * 10240 + lane * 16;
  if (wid < 4) {
#pragma unroll
    for (int c = 0; c < 2; ++c)
#pragma unroll
      for (int d = 0; d < 5; ++d) *(LAS f32x4*)(pw_ + (c * 5 + d) * 1024) = acc[2 + c][d];
  } else {
#pragma unroll
    for (int c = 0; c < 2; ++c)
#pragma unroll
      for (int d = 0; d < 5; ++d) *(LAS f32x4*)(pw_ + (c * 5 + d) * 1024) = acc[c][d];
  }
  WAIT_L(0); BAR();
  if (wid < 4) {
#pragma unroll
    for (int c = 0; c < 2; ++c)
#pragma unroll
      for (int d = 0; d < 5; ++d) { const f32x4 o = *(const LAS f32x4*)(pr_ + (c * 5 + d) * 1024); *(LAS f32x4*)(pr_ + (c * 5 + d) * 1024) = acc[c][d] + o; }
  } else {
#pragma unroll
    for (int c = 0; c < 2; ++c)
#pragma unroll
      for (int d = 0; d < 5; ++d) { const f32x4 o = *(const LAS f32x4*)(pr_ + (c * 5 + d) * 1024); *(LAS f32x4*)(pr_ + (c * 5 + d) * 1024) = acc[2 + c][d] + o; }
  }
  WAIT_L(0); BAR();
}
DI void mlp_gather(ldscp lds, int c2, int dt, int gD, int c15, float (&v)[8]) {
  ldscp p = lds + ((c2 < 2 ? 4 : 0) * 10 + (c2 & 1) * 5 + dt) * 1024 + (gD * 16 + c15) * 16;
#pragma unroll
  for (int hf = 0; hf < 2; ++hf) {
    f32x4 a = {0.f, 0.f, 0.f, 0.f};
#pragma unroll
    for (int q = 0; q < 4; ++q) a += *(const LAS f32x4*)(p + q * 10240 + hf * 256);
#pragma unroll
    for (int e = 0; e < 4; ++e) v[4 * hf + e] = a[e];
  }
}

DI void tail_phase(unsigned char* smem, const u16* __restrict__ x, const u16* __restrict__ wo, const u16* __restrict__ w1, const u16* __restrict__ b1,
                   const u16* __restrict__ w2, const u16* __restrict__ b2, char* __restrict__ ws, u16* __restrict__ out, int wid) {
  const int b = blockIdx.x, xcd = b & 7, r = b >> 3;
  ldsp lds = (ldsp)smem;
  const unsigned lds0 = (unsigned)(uintptr_t)smem;
  unsigned* flags = (unsigned*)(ws + WS_FLAGS);
  const int ksp = xcd >> 1, panel = (xcd & 1) * 8 + (r >> 2), ctile = r & 3;
  const int idx5 = xcd * 27 + r;
  const int wt5 = idx5 >> 3, dg5 = idx5 & 7;
  const int wt6 = xcd * 4 + (r >> 3), dg6 = r & 7;
  const bool do5 = r < 27;
  constexpr int P4S = 131072;
  bf16x8 wf[16];
  {
    const int lane = lane_id();
    const unsigned vl16 = lane * 16;
    const int kh = wid >> 2, st = wid & 3, hh = lane >> 5;
    unsigned voff[5];
#pragma unroll
    for (int i = 0; i < 5; ++i) {
      const int id = 2 * (5 * wid + i) + (lane >> 5), half = id / 40, rem = id % 40, rg = rem / 5, c = rem % 5;
      const int row = 64 * half + 8 * rg + ((lane >> 2) & 7);
      voff[i] = (unsigned)(row * 1280 + (32 * c + 8 * ((lane & 3) ^ ((row >> 2) & 3))) * 2);
    }
    const char* wb = (const char*)wo + (size_t)(ksp * 1024) * 1280 + ctile * 320;
    const unsigned dstw = lds0 + wid * 5120;
#define OPIECE(hd, i) glds16_c<(hd) * 128 * 1280>(voff[i], wb, dstw + ((hd) & 3) * 40960 + (i) * 1024)
#define STAGE_O(hd) do { OPIECE(hd, 0); OPIECE(hd, 1); OPIECE(hd, 2); OPIECE(hd, 3); OPIECE(hd, 4); } while (0)
    const char* cb = ws + WS_CTX + ((size_t)((4 * panel + st) * 32 + 8 * ksp) * 8 + 4 * kh) * 1024;
    int boff[2];
    { const int blk = (lane >> 4) & 1, q = (lane & 15) >> 2, p = lane & 3;
#pragma unroll
      for (int t = 0; t < 2; ++t) boff[t] = kh * 20480 + 2560 * t + 64 * (4 * hh + q) + (((2 * blk + (p >> 1)) ^ ((2 * t + hh) & 3)) << 4) + 8 * (p & 1); }
    ldscp bp0 = lds + boff[0], bp1 = lds + boff[1];
    u32x4 xf[3][4];
    f32x16 acc[5] = {};
    bf16x8 aq[6];
#define ARD(U) aq[(U) % 6] = cat8(vtr(bp0 + (((U) / 20) & 3) * 40960 + (((U) % 20) / 5) * 5120 + ((U) % 5) * 512), vtr(bp1 + (((U) / 20) & 3) * 40960 + (((U) % 20) / 5) * 5120 + ((U) % 5) * 512))
    STAGE_O(0); STAGE_O(1); STAGE_O(2);
    if (wid == 0) wait_word(flags + FL_C + 16 * ((panel >> 1) * 4 + ksp), 8u, flags + FL_TMO);
    BAR();
    GLOAD4_SC1(xf[0], vl16, cb); GLOAD4_SC1(xf[1], vl16, cb + 8192);
    WAIT_V(8); BAR();
#pragma unroll
    for (int U = 0; U < 5; ++U) ARD(U);
    static_for<8>([&](auto hh_) {
      constexpr int hd = decltype(hh_)::value;
      if constexpr (hd == 0) { WAITV_TIE4(4, xf[0]); }
      else if constexpr (hd <= 5) { WAITV_TIE4(9, xf[hd % 3]); }
      else if constexpr (hd == 6) { WAITV_TIE4(4, xf[hd % 3]); }
      else { WAITV_TIE4(0, xf[hd % 3]); }
      SBAR(); BAR(); SBAR();
      const char* xn_ = cb + (size_t)(hd + 2) * 8192;
      static_for<20>([&](auto uu_) {
        constexpr int u = decltype(uu_)::value;
        constexpr int U = 20 * hd + u;
        acc[u % 5] = MFMA32(aq[U % 6], __builtin_bit_cast(bf16x8, xf[hd % 3][u / 5]), acc[u % 5]);
        if constexpr (U + 5 < 160) ARD(U + 5);
        if constexpr (hd + 2 < 8) {
          if constexpr (u == 1) GLOAD1_SC1(xf[(hd + 2) % 3][0], vl16, xn_, 0);
          if constexpr (u == 3) GLOAD1_SC1(xf[(hd + 2) % 3][1], vl16, xn_, 1024);
          if constexpr (u == 5) GLOAD1_SC1(xf[(hd + 2) % 3][2], vl16, xn_, 2048);
          if constexpr (u == 7) GLOAD1_SC1(xf[(hd + 2) % 3][3], vl16, xn_, 3072);
        }
        if constexpr (hd + 3 < 8) {
          if constexpr (u == 9) OPIECE(hd + 3, 0);
          if constexpr (u == 11) OPIECE(hd + 3, 1);
          if constexpr (u == 13) OPIECE(hd + 3, 2);
          if constexpr (u == 15) OPIECE(hd + 3, 3);
          if constexpr (u == 17) OPIECE(hd + 3, 4);
        }
        SBAR();
      });
    });
#undef OPIECE
#undef STAGE_O
#undef ARD
    BAR();
    ldsp xw = lds + (kh == 0 ? 49152 : 0) + st * 12288 + lane * 16;
    ldscp xr = lds + (kh == 0 ? 0 : 49152) + st * 12288 + lane * 16;
    if (kh == 0) {
#pragma unroll
      for (int i = 0; i < 2; ++i)
#pragma unroll
        for (int g = 0; g < 4; ++g) *(LAS f32x4*)(xw + (i * 4 + g) * 1024) = (f32x4){acc[3 + i][4 * g], acc[3 + i][4 * g + 1], acc[3 + i][4 * g + 2], acc[3 + i][4 * g + 3]};
    } else {
#pragma unroll
      for (int i = 0; i < 3; ++i)
#pragma unroll
        for (int g = 0; g < 4; ++g) *(LAS f32x4*)(xw + (i * 4 + g) * 1024) = (f32x4){acc[i][4 * g], acc[i][4 * g + 1], acc[i][4 * g + 2], acc[i][4 * g + 3]};
    }
    WAIT_L(0); BAR();
    const char* sb = ws + WS_SLAB + ((size_t)((ksp * 64 + 4 * panel + st) * 20 + 5 * ctile) * 2) * 1024;
#define SLABST(I, T) static_for<2>([&](auto pp_) { \
          constexpr int sp = decltype(pp_)::value; \
          const f32x4 o0 = *(const LAS f32x4*)(xr + ((I) * 4 + 2 * sp) * 1024), o1 = *(const LAS f32x4*)(xr + ((I) * 4 + 2 * sp + 1) * 1024); \
          const u32x4 w = {cvtpk(acc[T][8 * sp] + o0[0], acc[T][8 * sp + 1] + o0[1]), cvtpk(acc[T][8 * sp + 2] + o0[2], acc[T][8 * sp + 3] + o0[3]), \
                           cvtpk(acc[T][8 * sp + 4] + o1[0], acc[T][8 * sp + 5] + o1[1]), cvtpk(acc[T][8 * sp + 6] + o1[2], acc[T][8 * sp + 7] + o1[3])}; \
          gstore_sc1_uc<((T) * 2 + sp) * 1024>(vl16, w, sb); })
    if (kh == 0) { SLABST(0, 0); SLABST(1, 1); SLABST(2, 2); }
    else { SLABST(0, 3); SLABST(1, 4); }
#undef SLABST
    WAIT_V(0); WAIT_L(0); BAR();
    if (wid == 0 && lane == 0) __hip_atomic_fetch_add(flags + FL_P3 + 64 * panel, 1u, __ATOMIC_RELAXED, __HIP_MEMORY_SCOPE_AGENT);
  }
  {
    const int lane = lane_id(), tid = wid * 64 + lane;
    const MlpW wp = mlp_wptr(lds, wid, lane);
    const char* w1b = (const char*)w1 + wt5 * 128;
    const unsigned voffW = mlp_voffw<true>(wid, lane);
    ldsp tr = lds + P4S;
    LAS float* red = (LAS float*)(lds + P4S + 10240);
    const int rr = tid & 7, pc = tid >> 3;
    const int r8 = ksp * 4 + ctile;
    const int s = 128 * panel + 8 * r8 + rr, st = s >> 5;
    int n0[2]; unsigned so[2];
    u32x2 xv[2][2];
#pragma unroll
    for (int i = 0; i < 2; ++i) {
      const int pi = pc + 64 * i; const int pcl = pi < 80 ? pi : 0;
      const int nt = pcl >> 2, sp = (pcl >> 1) & 1, hh = pcl & 1;
      n0[i] = 32 * nt + 16 * sp + 4 * hh;
      so[i] = (unsigned)((((st * 20 + nt) * 2 + sp) * 64 + (s & 31) + 32 * hh) * 16);
      xv[i][0] = *(const u32x2*)(x + (size_t)s * DM + n0[i]); xv[i][1] = *(const u32x2*)(x + (size_t)s * DM + n0[i] + 8);
    }
    asm volatile("" : "+v"(xv[0][0]), "+v"(xv[0][1]), "+v"(xv[1][0]), "+v"(xv[1][1]));
    if (do5) mlp_wstage<true, 0, 16>(lds0, w1b, voffW, wid, 0);
    if (wid == 0) wait_word(flags + FL_P3 + 64 * panel, 16u, flags + FL_TMO);
    BAR();
    u32x4 sv[2][4];
    const char* sl = ws + WS_SLAB;
#pragma unroll
    for (int i = 0; i < 2; ++i)
#pragma unroll
      for (int k = 0; k < 4; ++k) GLOAD1_SC1(sv[i][k], so[i], sl + (size_t)k * (64 * 20 * 2 * 1024), 0);
    WAITV_TIE4(0, sv[0]); WAITV_TIE4(0, sv[1]);
    float hv[2][8];
    unsigned pk[2][4];
    float sum = 0.f, ssq = 0.f;
#pragma unroll
    for (int i = 0; i < 2; ++i) {
      const bool ok = pc + 64 * i < 80;
#pragma unroll
      for (int e = 0; e < 4; ++e) {
        const float lo = (bflo(sv[i][0][e]) + bflo(sv[i][1][e])) + (bflo(sv[i][2][e]) + bflo(sv[i][3][e]));
        const float hi = (bfhi(sv[i][0][e]) + bfhi(sv[i][1][e])) + (bfhi(sv[i][2][e]) + bfhi(sv[i][3][e]));
        const unsigned xw_ = xv[i][e >> 1][e & 1];
        pk[i][e] = cvtpk(lo + bflo(xw_), hi + bfhi(xw_));
        hv[i][2 * e] = ok ? bflo(pk[i][e]) : 0.f; hv[i][2 * e + 1] = ok ? bfhi(pk[i][e]) : 0.f;
        sum += hv[i][2 * e] + hv[i][2 * e + 1];
      }
    }
    sum += __shfl_xor(sum, 8); sum += __shfl_xor(sum, 16); sum += __shfl_xor(sum, 32);
    const float cntw = (wid < 2) ? 128.f : 64.f;
    const float mw = sum / cntw;
#pragma unroll
    for (int i = 0; i < 2; ++i) {
      if (pc + 64 * i < 80) {
#pragma unroll
        for (int e = 0; e < 8; ++e) { const float d = hv[i][e] - mw; ssq += d * d; }
      }
    }
    ssq += __shfl_xor(ssq, 8); ssq += __shfl_xor(ssq, 16); ssq += __shfl_xor(ssq, 32);
    if (lane < 8) { red[(wid * 8 + lane) * 2] = mw; red[(wid * 8 + lane) * 2 + 1] = ssq; }
    WAIT_L(0); BAR();
    float mean = 0.f;
#pragma unroll
    for (int w = 0; w < 8; ++w) mean += red[(w * 8 + rr) * 2] * (w < 2 ? 128.f : 64.f);
    mean *= (1.0f / DM);
    float m2 = 0.f;
#pragma unroll
    for (int w = 0; w < 8; ++w) { const float dm = red[(w * 8 + rr) * 2] - mean; m2 += red[(w * 8 + rr) * 2 + 1] + dm * dm * (w < 2 ? 128.f : 64.f); }
    const float rstd = 1.0f / sqrtf(m2 * (1.0f / DM) + LN_EPS);
#pragma unroll
    for (int i = 0; i < 2; ++i) {
      if (pc + 64 * i < 80) {
        LAS u16* t16 = (LAS u16*)tr;
#pragma unroll
        for (int e = 0; e < 4; ++e) {
          const unsigned p = cvtpk((hv[i][2 * e] - mean) * rstd, (hv[i][2 * e + 1] - mean) * rstd);
          const int n = n0[i] + 8 * (e >> 1) + 2 * (e & 1);
          t16[n * 8 + rr] = (u16)(p & 0xffff); t16[(n + 1) * 8 + rr] = (u16)(p >> 16);
        }
      }
    }
    WAIT_L(0); BAR();
    const char* hdst = ws + WS_HNF + ((size_t)st * 40 * 64 + ((s >> 3) & 3) * 16) * 16;
    { const int d = tid; const u32x4 tv4 = *(const LAS u32x4*)(tr + d * 16); gstore_sc1_u((unsigned)(((d >> 4) * 64 + (d & 15)) * 16), tv4, hdst); }
    if (tid < 128) { const int d = 512 + tid; const u32x4 tv4 = *(const LAS u32x4*)(tr + d * 16); gstore_sc1_u((unsigned)(((d >> 4) * 64 + (d & 15)) * 16), tv4, hdst); }
#pragma unroll
    for (int i = 0; i < 2; ++i) {
      if (pc + 64 * i < 80) {
        unsigned long long* hp = (unsigned long long*)(ws + WS_H + ((size_t)s * DM + n0[i]) * 2);
        __hip_atomic_store(hp, ((unsigned long long)pk[i][1] << 32) | pk[i][0], __ATOMIC_RELAXED, __HIP_MEMORY_SCOPE_AGENT);
        __hip_atomic_store(hp + 2, ((unsigned long long)pk[i][3] << 32) | pk[i][2], __ATOMIC_RELAXED, __HIP_MEMORY_SCOPE_AGENT);
      }
    }
    if (do5) {
#pragma unroll
      for (int s4 = 0; s4 < 4; ++s4) mlp_wrd(wp, s4 * 32768, wf + 4 * s4);
    }
    WAIT_V(0); WAIT_L(0); BAR();
    if (tid == 0) __hip_atomic_fetch_add(flags + FL_H + 64 * (b & 7), 1u, __ATOMIC_RELAXED, __HIP_MEMORY_SCOPE_AGENT);
    if (do5) mlp_wstage<true, 16, 16>(lds0, w1b, voffW, wid, 0);
  }
  constexpr int RHI = 81920;
  {
    const int lane = lane_id(), tid = wid * 64 + lane;
    const char* w2b = (const char*)w2 + wt6 * 128;
    const unsigned voffW2 = mlp_voffw<false>(wid, lane);
    if (do5) {
      if (wid == 0) {
        for (unsigned spins = 0;; ++spins) {
          const unsigned v = __hip_atomic_load(flags + FL_H + 64 * (lane & 7), __ATOMIC_RELAXED, __HIP_MEMORY_SCOPE_AGENT);
          if (__all(v >= 32u)) break;
          if (spins > (1u << 16)) { __hip_atomic_store(flags + FL_TMO, 1u, __ATOMIC_RELAXED, __HIP_MEMORY_SCOPE_AGENT); break; }
          __builtin_amdgcn_s_sleep(1);
        }
      }
      WAIT_V(0); WAIT_L(0); BAR();
      f32x4 acc[4][5];
      { const MlpW wp = mlp_wptr(lds, wid, lane); mlp_main<true, 0, 4, 16, 16>(lds, ws + WS_HNF + (size_t)(5 * dg5) * 1024, wid, lane, wp, wf, acc); }
      BAR();
      mlp_park(lds, wid, lane, acc);
      {
        float v[2][8]; u32x4 bv[2];
#pragma unroll
        for (int k2 = 0; k2 < 2; ++k2) {
          const int it = k2 == 0 ? tid : (tid < 128 ? 512 + tid : tid);
          const int d15 = it & 15, gp = (it >> 4) & 3, fr = it >> 6, dt = fr % 5, Tp = fr / 5;
          bv[k2] = *(const u32x4*)(b1 + 64 * wt5 + 32 * Tp + 8 * gp);
          mlp_gather(lds, 2 * Tp + (gp >> 1), dt, 2 * (gp & 1), d15, v[k2]);
        }
#pragma unroll
        for (int k2 = 0; k2 < 2; ++k2) {
          if (k2 == 0 || tid < 128) {
            const int it = k2 == 0 ? tid : 512 + tid;
            const int d15 = it & 15, gp = (it >> 4) & 3, fr = it >> 6, dt = fr % 5, Tp = fr / 5;
            u32x4 o;
#pragma unroll
            for (int e = 0; e < 4; ++e) {
              float z[2] = {v[k2][2 * e] + bflo(bv[k2][e]), v[k2][2 * e + 1] + bfhi(bv[k2][e])};
#pragma unroll
              for (int k = 0; k < 2; ++k) {
                const float xx = z[k];
                const float a = xx * (1.0f + 0.044715f * xx * xx) * (-2.0f * 0.7978845608028654f * 1.4426950408889634f);
                z[k] = xx * __builtin_amdgcn_rcpf(1.0f + __builtin_amdgcn_exp2f(a));
              }
              o[e] = cvtpk(z[0], z[1]);
            }
            gstore_sc1_u((unsigned)((((2 * wt5 + Tp) * 40 + 5 * dg5 + dt) * 64 + gp * 16 + d15) * 16), o, ws + WS_UF);
          }
        }
      }
      mlp_wstage<false, 20, 8>(lds0, w2b, voffW2, wid, RHI);
      WAIT_V(8); WAIT_L(0); BAR();
      if (tid == 0) __hip_atomic_fetch_add(flags + FL_U + 64 * dg5, 1u, __ATOMIC_RELAXED, __HIP_MEMORY_SCOPE_AGENT);
    } else {
      WAIT_L(0); BAR();
      mlp_wstage<false, 20, 8>(lds0, w2b, voffW2, wid, RHI);
    }
  }
  {
    const int lane = lane_id(), tid = wid * 64 + lane;
    bf16x8 wg[8];
    const MlpW wp = mlp_wptr(lds, wid, lane);
    const char* w2b = (const char*)w2 + wt6 * 128;
    const unsigned voffW2 = mlp_voffw<false>(wid, lane);
    mlp_wstage<false, 0, 10>(lds0, w2b, voffW2, wid, 0);
    WAIT_V(10); BAR();
    mlp_wrd(wp, RHI, wg); mlp_wrd(wp, RHI + 32768, wg + 4);
    WAIT_L(0); BAR();
    mlp_wstage<false, 10, 10>(lds0, w2b, voffW2, wid, RHI);
    if (wid == 0) wait_word(flags + FL_U + 64 * dg6, 27u, flags + FL_TMO);
    WAIT_V(0); BAR();
    const char* ab = ws + WS_UF + (size_t)(5 * dg6) * 1024;
    f32x4 acc[4][5];
    mlp_main<false, 5, 7, 0, 8>(lds, ab, wid, lane, wp, wg, acc);
    u32x4 hv[2]; float bb[2]; size_t oo[2];
#pragma unroll
    for (int k2 = 0; k2 < 2; ++k2) {
      const int it = k2 == 0 ? tid : (tid < 128 ? 512 + tid : tid);
      const int sl = it / 10, c = it % 10, srow = 64 * wt6 + sl;
      oo[k2] = (size_t)srow * DM + 80 * dg6 + 8 * c;
      GLOAD1_SC1(hv[k2], (unsigned)(oo[k2] * 2), ws + WS_H, 0);
      bb[k2] = bflo((unsigned)b2[srow]);
    }
    BAR();
    mlp_park(lds, wid, lane, acc);
    float v[2][8];
#pragma unroll
    for (int k2 = 0; k2 < 2; ++k2) {
      const int it = k2 == 0 ? tid : (tid < 128 ? 512 + tid : tid);
      const int sl = it / 10, c = it % 10;
      mlp_gather(lds, sl >> 4, c >> 1, 2 * (c & 1), sl & 15, v[k2]);
    }
    asm volatile("s_waitcnt vmcnt(0)" : "+v"(hv[0]), "+v"(hv[1]) :: "memory");
#pragma unroll
    for (int k2 = 0; k2 < 2; ++k2) {
      if (k2 == 0 || tid < 128) {
        u32x4 ov;
#pragma unroll
        for (int e = 0; e < 4; ++e) ov[e] = cvtpk(v[k2][2 * e] + bb[k2] + bflo(hv[k2][e]), v[k2][2 * e + 1] + bb[k2] + bfhi(hv[k2][e]));
        *(u32x4*)(out + oo[k2]) = ov;
      }
    }
  }
}

constexpr int qproj_ns(int c, int cls) {
  constexpr int tab[4][10] = {{3, 3, 3, 3, 2, 2, 0, 0, 0, 0}, {1, 2, 2, 2, 3, 3, 3, 0, 0, 0}, {0, 1, 1, 2, 2, 2, 2, 3, 3, 0}, {0, 0, 1, 1, 1, 2, 2, 3, 3, 3}};
  return (c < 0 || c > 9) ? 0 : tab[cls][c];
}
constexpr int qproj_sbase(int c, int cls) { int n = 0; for (int i = 0; i < c; ++i) n += qproj_ns(i, cls); return n; }
constexpr int qproj_nl(int c) { return (c + 2 < 10 ? 4 : 0) + (c + 3 < 10 ? 2 : 0); }
constexpr int qproj_allowed(int c, int cls) {
  return c == 0 ? 6 : (c == 1 ? qproj_nl(0) + qproj_ns(0, cls) : qproj_ns(c - 2, cls) + qproj_nl(c - 1) + qproj_ns(c - 1, cls));
}
__global__ __launch_bounds__(512, 2) void kF1(const u16* __restrict__ ckv, const u16* __restrict__ wuk, const u16* __restrict__ wuv, const u16* __restrict__ wq,
                                              const u16* __restrict__ wo, const u16* __restrict__ w1, const u16* __restrict__ w2,
                                              const u16* __restrict__ x, const u16* __restrict__ b1, const u16* __restrict__ b2, char* __restrict__ ws, u16* __restrict__ out) {
  extern __shared__ __attribute__((aligned(16))) unsigned char smem[];
  const int bidx = blockIdx.x, xcd = bidx & 7, rr_ = bidx >> 3;
  const int h = xcd * 4 + (rr_ >> 3), j = rr_ & 7;
  const int wid = rfl(threadIdx.x >> 6);
  ldsp lds = (ldsp)smem;
  const unsigned lds0 = (unsigned)(uintptr_t)smem;
  unsigned* flags = (unsigned*)(ws + WS_FLAGS);
  bf16x8 kvp[16];
  {
    const int wr = wid >> 2, wc = wid & 3;
    const int lane = threadIdx.x & 63, l31 = lane & 31, hh = lane >> 5;
    unsigned voffC, voffW;
    { const int row = 8 * wid + (lane >> 3); voffC = (unsigned)(row * 1024 + (((lane & 7) ^ ((row >> 1) & 7)) << 4)); }
    { const int row = 8 * (wid >> 1) + ((lane >> 2) & 7); const int chq = 2 * (wid & 1) + (lane >> 5);
      const int ch = 4 * chq + ((lane & 3) ^ ((row >> 2) & 3)); voffW = (unsigned)(row * 8192 + ch * 16); }
    const char* cbase = (const char*)ckv + (size_t)(256 * j) * 1024;
    const char* wkb = (const char*)wuk + h * 256;
    const char* wvb = (const char*)wuv + h * 256;
    const unsigned dstw = lds0 + wid * 1024;
#define CBUF(b, a) (((b) * 2 + (a)) * 16384)
#define WBUF(b, a) (65536 + ((b) * 2 + (a)) * 16384)
#define STAGE_C(b, a, kt) do { const char* s_ = cbase + (a) * 131072 + (kt) * 128; glds16(voffC, s_, dstw + CBUF(b, a)); glds16(voffC, s_ + 65536, dstw + CBUF(b, a) + 8192); } while (0)
#define STAGE_W(b, a, kt) do { const char* s_ = ((a) ? wvb : wkb) + (size_t)(kt) * 64 * 8192; glds16(voffW, s_, dstw + WBUF(b, a)); glds16(voffW, s_ + 32 * 8192, dstw + WBUF(b, a) + 8192); } while (0)
    int aoff[4], boff[2];
#pragma unroll
    for (int s = 0; s < 4; ++s) aoff[s] = 128 * (wr * 64 + l31) + (((2 * s + hh) ^ ((l31 >> 1) & 7)) << 4);
    { const int blk = (lane >> 4) & 1, q = (lane & 15) >> 2, p = lane & 3;
#pragma unroll
      for (int t = 0; t < 2; ++t) boff[t] = 2048 * hh + 512 * wc + 64 * (4 * t + q) + (((2 * blk + (p >> 1)) ^ ((2 * hh + t) & 3)) << 4) + 8 * (p & 1); }
#define LDA(dst, b, a) do { _Pragma("unroll") for (int m = 0; m < 2; ++m) _Pragma("unroll") for (int s = 0; s < 4; ++s) dst[m][s] = ldsr128(lds + CBUF(b, a) + m * 4096 + aoff[s]); } while (0)
#define LDB(dst, b, a) do { _Pragma("unroll") for (int s = 0; s < 4; ++s) dst[s] = cat8(vtr(lds + WBUF(b, a) + s * 4096 + boff[0]), vtr(lds + WBUF(b, a) + s * 4096 + boff[1])); } while (0)
#define MMAK(ai, At, Bt) do { __builtin_amdgcn_s_setprio(1); _Pragma("unroll") for (int s = 0; s < 4; ++s) _Pragma("unroll") for (int m = 0; m < 2; ++m) \
    accK[ai][m] = MFMA32(Bt[s], At[m][s], accK[ai][m]); __builtin_amdgcn_s_setprio(0); } while (0)
#define MMAV(ai, At, Bt) do { __builtin_amdgcn_s_setprio(1); _Pragma("unroll") for (int s = 0; s < 4; ++s) _Pragma("unroll") for (int m = 0; m < 2; ++m) \
    accV[ai][m] = MFMA32(At[m][s], Bt[s], accV[ai][m]); __builtin_amdgcn_s_setprio(0); } while (0)
    f32x16 accK[2][2] = {}, accV[2][2] = {};
    bf16x8 At[2][4], B0[4], B1[4];
    constexpr int nt = DC / 64;
    STAGE_W(0, 0, 0); STAGE_C(0, 0, 0); STAGE_W(0, 1, 0); STAGE_C(0, 1, 0);
    if (wr == 1) BAR();
    WAIT_V(4); BAR();
    STAGE_W(1, 0, 1); STAGE_C(1, 0, 1); STAGE_W(1, 1, 1);
    WAIT_V(6); BAR();
    unsigned pfd = 0;
    if (wid < 3) {
      const int ln = wid * 64 + lane;
      if (ln < 160) asm volatile("global_load_dword %0, %1, off" : "+v"(pfd) : "v"((const char*)wq + (size_t)(80 * j + (ln >> 1)) * 8192 + h * 256 + (ln & 1) * 128) : "memory");
    }
    for (int t = 0; t < nt - 2; t += 2) {
      LDB(B0, 0, 0); SBAR(); LDA(At, 0, 0); STAGE_C(1, 1, t + 1);
      WAIT_L(8); BAR(); WAIT_L(0); MMAK(0, At, B0); BAR(); SBAR();
      LDB(B1, 0, 1); STAGE_W(0, 0, t + 2);
      BAR(); WAIT_L(0); MMAV(0, At, B1); BAR();
      LDA(At, 0, 1); STAGE_C(0, 0, t + 2);
      BAR(); WAIT_L(0); MMAK(1, At, B0); BAR(); SBAR();
      STAGE_W(0, 1, t + 2);
      WAIT_V(6); BAR(); MMAV(1, At, B1); BAR();
      LDB(B0, 1, 0); SBAR(); LDA(At, 1, 0); STAGE_C(0, 1, t + 2);
      WAIT_L(8); BAR(); WAIT_L(0); MMAK(0, At, B0); BAR(); SBAR();
      LDB(B1, 1, 1); STAGE_W(1, 0, t + 3);
      BAR(); WAIT_L(0); MMAV(0, At, B1); BAR();
      LDA(At, 1, 1); STAGE_C(1, 0, t + 3);
      BAR(); WAIT_L(0); MMAK(1, At, B0); BAR(); SBAR();
      STAGE_W(1, 1, t + 3);
      WAIT_V(6); BAR(); MMAV(1, At, B1); BAR();
    }
    {
      const char* wqb_ = (const char*)wq + h * 256;
      glds16_c<0>(voffW, wqb_, lds0 + 131072 + wid * 1024); glds16_c<32 * 8192>(voffW, wqb_, lds0 + 131072 + 8192 + wid * 1024);
      glds16_c<64 * 8192>(voffW, wqb_, lds0 + 147456 + wid * 1024); glds16_c<96 * 8192>(voffW, wqb_, lds0 + 147456 + 8192 + wid * 1024); }
    { LDB(B0, 0, 0); WAIT_V(0); LDA(At, 0, 0); STAGE_C(1, 1, nt - 1);
      BAR(); WAIT_L(0); MMAK(0, At, B0); BAR();
      LDB(B1, 0, 1); BAR(); WAIT_L(0); MMAV(0, At, B1); BAR();
      LDA(At, 0, 1); WAIT_V(4); BAR(); WAIT_L(0); MMAK(1, At, B0); MMAV(1, At, B1); BAR(); }
    { LDB(B0, 1, 0); LDA(At, 1, 0); WAIT_V(2); BAR(); WAIT_L(0); MMAK(0, At, B0); BAR();
      LDB(B1, 1, 1); WAIT_V(0); BAR(); WAIT_L(0); MMAV(0, At, B1); BAR();
      LDA(At, 1, 1); BAR(); WAIT_L(0); MMAK(1, At, B0); MMAV(1, At, B1); BAR(); }
    if (wr == 0) BAR();
    asm volatile("" :: "v"(pfd));
#pragma unroll
    for (int ai = 0; ai < 2; ++ai)
#pragma unroll
      for (int m = 0; m < 2; ++m)
#pragma unroll
        for (int sp = 0; sp < 2; ++sp) {
          kvp[((ai * 2 + m) * 2 + sp) * 2] = pack8(accK[ai][m], sp);
          kvp[((ai * 2 + m) * 2 + sp) * 2 + 1] = pack8(accV[ai][m], sp);
          PIN(kvp[((ai * 2 + m) * 2 + sp) * 2]); PIN(kvp[((ai * 2 + m) * 2 + sp) * 2 + 1]);
        }
#undef CBUF
#undef WBUF
#undef STAGE_C
#undef STAGE_W
#undef LDA
#undef LDB
#undef MMAK
#undef MMAV
  }
  SBAR();
  bf16x8 qr[8];
  unsigned kvmask = 0;
  const int lane = lane_id(), tid = wid * 64 + lane;
  const unsigned vl16 = lane * 16;
  {
    const int hh = lane >> 5;
    const char* Kh = ws + WS_KF + (size_t)h * 64 * 8192;
    const char* Vh = ws + WS_VF + (size_t)h * 128 * 4096;
    const unsigned vt2 = (unsigned)(wid * 2048 + lane * 16);
    const unsigned dt2 = lds0 + wid * 2048;
    const int n1 = j >= 4 ? 16 : 4 * j + 2;
#define TILE(g) ((g) < n1 ? (g) : 16 + (g) - n1)
#define DMA_T(src, ldsoff) do { unsigned keep_; asm volatile("s_mov_b32 %0, m0\n\ts_mov_b32 m0, %3\n\ts_nop 2\n\tglobal_load_lds_dwordx4 %1, %2 sc1\n\tglobal_load_lds_dwordx4 %1, %2 offset:1024 sc1\n\ts_mov_b32 m0, %0" \
    : "=&s"(keep_) : "v"(vt2), "s"(src), "s"(dt2 + (ldsoff)) : "memory"); } while (0)
    const int wr = wid >> 2, wc = wid & 3, cls = j & 3;
    const char* kst = ws + WS_KF + ((size_t)(h * 64 + 8 * j + 2 * wr) * 8 + 2 * wc) * 1024;
    const char* vst = ws + WS_VF + ((size_t)(h * 128 + 2 * (8 * j + 2 * wr)) * 4 + wc) * 1024;
#define KVST(i) gstore_sc1_c<(((i) & 1) ? (((i) >> 3) * 8 + (((i) >> 2) & 1) * 2 + (((i) >> 1) & 1)) * 4096 : (((i) >> 3) * 4 + (((i) >> 2) & 1)) * 8192 + (((i) >> 1) & 1) * 1024)>(vl16, kvp[i], ((i) & 1) ? vst : kst)
    unsigned voffW;
    { const int row = 8 * (wid >> 1) + ((lane >> 2) & 7); const int chq = 2 * (wid & 1) + (lane >> 5);
      const int ch = 4 * chq + ((lane & 3) ^ ((row >> 2) & 3)); voffW = (unsigned)(row * 8192 + ch * 16); }
    const char* wqb = (const char*)wq + h * 256;
    const unsigned dstw = lds0 + L_Q + wid * 1024;
#define STAGE_Q1(c, i) glds16_c<(c) * 64 * 8192 + (i) * 32 * 8192>(voffW, wqb, dstw + (((c) + 2) & 3) * 16384 + (i) * 8192)
    const char* xb = ws + WS_XNF + (size_t)(8 * j + wid) * 40 * 1024;
    int boff[2];
    { const int blk = (lane >> 4) & 1, q = (lane & 15) >> 2, p = lane & 3;
#pragma unroll
      for (int t = 0; t < 2; ++t) boff[t] = L_Q + 2048 * hh + 64 * (4 * t + q) + (((2 * blk + (p >> 1)) ^ ((2 * hh + t) & 3)) << 4) + 8 * (p & 1); }
    u32x4 xf[3][4];
    f32x16 acc[4] = {};
    bf16x8 aq[6];
    unsigned fv = 0;
    ldscp bp0 = lds + boff[0], bp1 = lds + boff[1];
#define ARD(u) aq[(u) % 6] = cat8(vtr(bp0 + ((((u) >> 4) + 2) & 3) * 16384 + (((u) >> 2) & 3) * 4096 + ((u) & 3) * 512), vtr(bp1 + ((((u) >> 4) + 2) & 3) * 16384 + (((u) >> 2) & 3) * 4096 + ((u) & 3) * 512))
    GLOAD4(xf[0], vl16, xb);
    GLOAD4(xf[1], vl16, xb + 4096); STAGE_Q1(2, 0); STAGE_Q1(2, 1);
#pragma unroll
    for (int u = 0; u < 5; ++u) ARD(u);
    static_for<10>([&](auto cc_) {
      constexpr int c = decltype(cc_)::value;
#define QW(K) WAITVN(qproj_allowed(c, K))
      if (cls == 0) QW(0); else if (cls == 1) QW(1); else if (cls == 2) QW(2); else QW(3);
      asm volatile("" : "+v"(xf[c % 3][0]), "+v"(xf[c % 3][1]), "+v"(xf[c % 3][2]), "+v"(xf[c % 3][3]) :: "memory");
#undef QW
      if (c == 9 && wid == 0) {
        asm volatile("s_waitcnt vmcnt(0)" : "+v"(fv) :: "memory");
        kvmask = (unsigned)__builtin_amdgcn_ballot_w64(fv != 0u) & 0xffu;
        if (!(kvmask & 1u)) { wait_word(flags + FL_KV + 16 * (8 * h), 1u, flags + FL_TMO); kvmask |= 1u; }
        if (j == 0 && !(kvmask & 16u)) { wait_word(flags + FL_KV + 16 * (8 * h + 4), 1u, flags + FL_TMO); kvmask |= 16u; }
      }
      SBAR(); BAR();
      if (tid == 0 && ((cls == 0 && c == 8) || (cls == 1 && c == 9)))
        __hip_atomic_store(flags + FL_KV + 16 * (8 * h + j), 1u, __ATOMIC_RELAXED, __HIP_MEMORY_SCOPE_AGENT);
      SBAR();
      const char* xn_ = xb + (c + 2) * 4096;
      static_for<16>([&](auto ii_) {
        constexpr int i = decltype(ii_)::value;
        constexpr int u = 16 * c + i;
        acc[i & 3] = MFMA32(aq[u % 6], __builtin_bit_cast(bf16x8, xf[c % 3][i >> 2]), acc[i & 3]);
        if constexpr (u + 5 < 160) ARD(u + 5);
        if (c + 2 < 10) {
          if (i == 1) GLOAD1(xf[(c + 2) % 3][0], vl16, xn_, 0);
          if (i == 3) GLOAD1(xf[(c + 2) % 3][1], vl16, xn_, 1024);
          if (i == 5) GLOAD1(xf[(c + 2) % 3][2], vl16, xn_, 2048);
          if (i == 7) GLOAD1(xf[(c + 2) % 3][3], vl16, xn_, 3072);
        }
        if constexpr (c + 3 < 10) { if constexpr (i == 8) STAGE_Q1(c + 3, 0); if constexpr (i == 9) STAGE_Q1(c + 3, 1); }
        if constexpr (i >= 10) {
          static_for<4>([&](auto kk_) {
            constexpr int k = decltype(kk_)::value;
            constexpr int cnt = qproj_ns(c, k), base = qproj_sbase(c, k);
            if constexpr (((i - 10) & 1) == 0 && (i - 10) / 2 < cnt) { if (cls == k) KVST(base + (i - 10) / 2); }
          });
        }
        if (c == 8 && i == 8 && wid == 0)
          asm volatile("s_nop 4\n\tglobal_load_dword %0, %1, %2 sc1" : "+v"(fv) : "v"((unsigned)(FL_KV + 16 * (8 * h + (lane & 7))) * 4u), "s"(flags) : "memory");
        if (c == 9) {
          if (i == 0) DMA_T(Kh + (size_t)TILE(0) * 16384, L_K);
          if (i == 2) DMA_T(Vh + (size_t)TILE(0) * 16384, L_V);
          if (i == 4) DMA_T(Kh + (size_t)TILE(1) * 16384, L_K + 16384);
          if (i == 6) DMA_T(Vh + (size_t)TILE(1) * 16384, L_V + 16384);
          if (i == 8) DMA_T(Kh + (size_t)TILE(2) * 16384, L_K + 32768);
          if (i == 9) DMA_T(Kh + (size_t)TILE(3) * 16384, L_K + 49152);
        }
        SBAR();
      });
    });
#pragma unroll
    for (int dt = 0; dt < 4; ++dt) { acc[dt] = acc[dt] * QSCALE; qr[2 * dt] = pack8(acc[dt], 0); qr[2 * dt + 1] = pack8(acc[dt], 1); }
    if (cls == 3) WAIT_V(13); else WAIT_V(10);
    BAR();
    { const char* qst = ws + WS_QF + (size_t)(h * 64 + 8 * j + wid) * 8192;
      static_for<8>([&](auto ss_) { constexpr int s = decltype(ss_)::value; gstore_sc1_c<s * 1024>(vl16, qr[s], qst); }); }
#undef KVST
#undef STAGE_Q1
#undef ARD
#undef TILE
#undef DMA_T
  }
  const PfW pfw = {(const char*)wo, (const char*)w1, (const char*)w2};
  if (wid < 4) attn_main<0>(lds, lds0, ws, flags, h, j, wid, lane, tid, qr, kvmask, pfw);
  else attn_main<1>(lds, lds0, ws, flags, h, j, wid, lane, tid, qr, kvmask, pfw);
  WAIT_V(0); WAIT_L(0); BAR();
  if (tid == 0) __hip_atomic_fetch_add(flags + FL_C + 16 * ((7 - j) * 4 + (h >> 3)), 1u, __ATOMIC_RELAXED, __HIP_MEMORY_SCOPE_AGENT);
  tail_phase(smem, x, wo, w1, b1, w2, b2, ws, out, wid);
}

extern "C" void kernel_launch(void* const* d_in, const int* in_sizes, int n_in, void* d_out, int out_size, void* d_ws, size_t ws_size,
                              hipStream_t stream) {
  const u16* x = (const u16*)d_in[0];
  const u16* ckv = (const u16*)d_in[1];
  const u16* wuk = (const u16*)d_in[3];
  const u16* wuv = (const u16*)d_in[4];
  char* ws = (char*)d_ws;
  const u16* wq = (const u16*)d_in[2];
  const u16* wo = (const u16*)d_in[5];
  const u16* w1 = (const u16*)d_in[7];
  const u16* b1 = (const u16*)d_in[8];
  const u16* w2 = (const u16*)d_in[9];
  const u16* b2 = (const u16*)d_in[10];
  (void)hipFuncSetAttribute((const void*)kF1, hipFuncAttributeMaxDynamicSharedMemorySize, F1_LDS);
  hipLaunchKernelGGL(k0_ln, dim3(256), dim3(512), 0, stream, x, (u16*)(ws + WS_XNF), (unsigned*)(ws + WS_FLAGS));
  hipLaunchKernelGGL(kF1, dim3(256), dim3(512), F1_LDS, stream, ckv, wuk, wuv, wq, wo, w1, w2, x, b1, b2, ws, (u16*)d_out);
}
```

```cpp
#include <hip/hip_runtime.h>
#include <stdint.h>

#define LAS __attribute__((address_space(3)))
#define DI __device__ __forceinline__
typedef unsigned short u16;
typedef short bf16x8 __attribute__((ext_vector_type(8)));
typedef short s16x4 __attribute__((ext_vector_type(4)));
typedef float f32x2 __attribute__((ext_vector_type(2)));
typedef float f32x4 __attribute__((ext_vector_type(4)));
typedef float f32x16 __attribute__((ext_vector_type(16)));
typedef unsigned u32x2 __attribute__((ext_vector_type(2)));
typedef unsigned u32x4 __attribute__((ext_vector_type(4)));
typedef __bf16 bf16x2_t __attribute__((ext_vector_type(2)));
typedef LAS unsigned char* ldsp;
typedef LAS const unsigned char* ldscp;

constexpr int SEQ = 2048, DM = 640, DC = 512, NH = 32, HD = 128, FF = 1728;
constexpr float LN_EPS = 1e-5f;

constexpr size_t WS_FLAGS = 0;
constexpr size_t WS_XNF = 131072;
constexpr size_t WS_KF = WS_XNF + (size_t)SEQ * DM * 2;
constexpr size_t WS_VF = WS_KF + (size_t)NH * SEQ * HD * 2;
constexpr size_t WS_CTX = WS_VF + (size_t)NH * SEQ * HD * 2;
constexpr size_t WS_SLAB = WS_CTX + (size_t)NH * SEQ * HD * 2;
constexpr size_t WS_H = WS_SLAB + (size_t)4 * SEQ * DM * 4;
constexpr size_t WS_HNF = WS_H + (size_t)SEQ * DM * 2;
constexpr size_t WS_UF = WS_HNF + (size_t)SEQ * DM * 2;
constexpr size_t WS_QF = WS_UF + (size_t)DM * FF * 2;
constexpr size_t WS_PF = WS_QF + (size_t)NH * SEQ * HD * 2;
constexpr size_t WS_PS = WS_PF + (size_t)NH * SEQ * HD * 2;
constexpr size_t WS_END = WS_PS + (size_t)NH * 8 * 8 * 64 * 8;
constexpr int FL_TMO = 0;
constexpr int FL_KV = 24576;
constexpr int FL_Q = 4096;
constexpr int FL_P = 4096 + 8192;
constexpr int FL_WORDS = 32768;

#define SBAR() __builtin_amdgcn_sched_barrier(0)
#define BAR() asm volatile("s_barrier" ::: "memory")
#define WAIT_V(n) asm volatile("s_waitcnt vmcnt(" #n ")" ::: "memory")
#define WAIT_L(n) asm volatile("s_waitcnt lgkmcnt(" #n ")" ::: "memory")
#define MFMA32(a, b, c) __builtin_amdgcn_mfma_f32_32x32x16_bf16((a), (b), (c), 0, 0, 0)
#define MFMA16(a, b, c) __builtin_amdgcn_mfma_f32_16x16x32_bf16((a), (b), (c), 0, 0, 0)

DI unsigned cvtpk(float lo, float hi) { f32x2 v = {lo, hi}; bf16x2_t b = __builtin_convertvector(v, bf16x2_t); return __builtin_bit_cast(unsigned, b); }
DI float bflo(unsigned u) { return __uint_as_float(u << 16); }
DI float bfhi(unsigned u) { return __uint_as_float(u & 0xffff0000u); }
DI unsigned rfl(unsigned x) { return __builtin_amdgcn_readfirstlane(x); }
DI void glds16(unsigned voff, const void* sbase, unsigned lds_dst) {
  unsigned keep;
  asm volatile("s_mov_b32 %0, m0\n\ts_mov_b32 m0, %3\n\ts_nop 2\n\tglobal_load_lds_dwordx4 %1, %2\n\ts_mov_b32 m0, %0"
               : "=&s"(keep) : "v"(voff), "s"(sbase), "s"(lds_dst) : "memory");
}
DI void glds16x4(unsigned voff, const void* sbase, unsigned lds_dst) {
  unsigned keep;
  asm volatile("s_mov_b32 %0, m0\n\ts_mov_b32 m0, %3\n\ts_nop 2\n\tglobal_load_lds_dwordx4 %1, %2\n\tglobal_load_lds_dwordx4 %1, %2 offset:1024\n\t"
               "global_load_lds_dwordx4 %1, %2 offset:2048\n\tglobal_load_lds_dwordx4 %1, %2 offset:3072\n\ts_mov_b32 m0, %0"
               : "=&s"(keep) : "v"(voff), "s"(sbase), "s"(lds_dst) : "memory");
}
typedef short v4i16_t __attribute__((ext_vector_type(4)));
DI s16x4 vtr(ldscp p) { return __builtin_bit_cast(s16x4, __builtin_amdgcn_ds_read_tr16_b64_v4i16((LAS v4i16_t*)p)); }
DI bf16x8 cat8(s16x4 lo, s16x4 hi) { return __builtin_shufflevector(lo, hi, 0, 1, 2, 3, 4, 5, 6, 7); }
DI bf16x8 ldsr128(ldscp p) { return *(const LAS bf16x8*)p; }
DI bf16x8 pack8(const f32x16& x, int s) {
  u32x4 w = {cvtpk(x[8 * s], x[8 * s + 1]), cvtpk(x[8 * s + 2], x[8 * s + 3]), cvtpk(x[8 * s + 4], x[8 * s + 5]), cvtpk(x[8 * s + 6], x[8 * s + 7])};
  return __builtin_bit_cast(bf16x8, w);
}
DI float wave_sum(float v) {
#pragma unroll
  for (int o = 32; o >= 1; o >>= 1) v += __shfl_xor(v, o);
  return v;
}

__global__ __launch_bounds__(512) void k0_ln(const u16* __restrict__ x, u16* __restrict__ xnf, unsigned* __restrict__ flags) {
  const int w = threadIdx.x >> 6, lane = threadIdx.x & 63;
  const int q = blockIdx.x * 8 + w;
  if (threadIdx.x < 128) flags[blockIdx.x * 128 + threadIdx.x] = 0u;
  const u32x4* row = (const u32x4*)(x + (size_t)q * DM);
  u32x4 c0 = row[lane];
  u32x4 c1 = {0u, 0u, 0u, 0u};
  if (lane < 16) c1 = row[64 + lane];
  float v[16];
#pragma unroll
  for (int i = 0; i < 4; ++i) { v[2 * i] = bflo(c0[i]); v[2 * i + 1] = bfhi(c0[i]); v[8 + 2 * i] = bflo(c1[i]); v[8 + 2 * i + 1] = bfhi(c1[i]); }
  float s = 0.f;
#pragma unroll
  for (int i = 0; i < 16; ++i) s += v[i];
  const float mean = wave_sum(s) * (1.0f / DM);
  float ss = 0.f;
#pragma unroll
  for (int i = 0; i < 8; ++i) { const float d = v[i] - mean; ss += d * d; }
  if (lane < 16) {
#pragma unroll
    for (int i = 8; i < 16; ++i) { const float d = v[i] - mean; ss += d * d; }
  }
  const float rstd = 1.0f / sqrtf(wave_sum(ss) * (1.0f / DM) + LN_EPS);
  u32x4 o0, o1;
#pragma unroll
  for (int i = 0; i < 4; ++i) { o0[i] = cvtpk((v[2 * i] - mean) * rstd, (v[2 * i + 1] - mean) * rstd); o1[i] = cvtpk((v[8 + 2 * i] - mean) * rstd, (v[8 + 2 * i + 1] - mean) * rstd); }
  u32x4* dst = (u32x4*)xnf + (size_t)(q >> 5) * 40 * 64 + (q & 31);
  dst[(lane >> 1) * 64 + (lane & 1) * 32] = o0;
  if (lane < 16) dst[((64 + lane) >> 1) * 64 + (lane & 1) * 32] = o1;
}

constexpr int F1_LDS = 163840;
constexpr int L_K = 0, L_V = 65536, L_Q = 98304;
constexpr float QSCALE = 0.08838834764831845f * 1.4426950408889634f;
constexpr float THR = 16.f;
#define PIN(x) asm volatile("" : "+v"(x))
#define EX2(v) __builtin_amdgcn_exp2f(v)
#define MEMFENCE() asm volatile("" ::: "memory")
DI float max3f(float a, float b, float c) { return __builtin_fmaxf(__builtin_fmaxf(a, b), c); }
DI float xhalf_max(float m) { auto rr = __builtin_amdgcn_permlane32_swap(__float_as_uint(m), __float_as_uint(m), false, false); return __builtin_fmaxf(__uint_as_float(rr[0]), __uint_as_float(rr[1])); }
DI float xhalf_sum(float m) { auto rr = __builtin_amdgcn_permlane32_swap(__float_as_uint(m), __float_as_uint(m), false, false); return __uint_as_float(rr[0]) + __uint_as_float(rr[1]); }
DI float max16(const f32x16& p) {
  float a = max3f(p[0], p[1], p[2]), b = max3f(p[3], p[4], p[5]);
  a = max3f(a, p[6], p[7]); b = max3f(b, p[8], p[9]); a = max3f(a, p[10], p[11]); b = max3f(b, p[12], p[13]); a = max3f(a, p[14], p[15]);
  return __builtin_fmaxf(a, b);
}
DI void cmask(f32x16& p0, f32x16& p1, int dq) {
#pragma unroll
  for (int r = 0; r < 16; ++r) { const int c = (r & 3) + 8 * (r >> 2); if (c > dq) p0[r] = -__builtin_inff(); if (c + 32 > dq) p1[r] = -__builtin_inff(); }
}
#define GLOAD4(d, voff, sbase) asm volatile("s_nop 4\n\tglobal_load_dwordx4 %0, %4, %5\n\tglobal_load_dwordx4 %1, %4, %5 offset:1024\n\tglobal_load_dwordx4 %2, %4, %5 offset:2048\n\tglobal_load_dwordx4 %3, %4, %5 offset:3072" \
    : "=&v"(d[0]), "=&v"(d[1]), "=&v"(d[2]), "=&v"(d[3]) : "v"(voff), "s"(sbase) : "memory")
#define GLOAD1(d, voff, sbase, OFF) asm volatile("s_nop 4\n\tglobal_load_dwordx4 %0, %1, %2 offset:" #OFF : "=&v"(d) : "v"(voff), "s"(sbase) : "memory")
#define GLOAD1_SC1(d, voff, sbase, OFF) asm volatile("s_nop 4\n\tglobal_load_dwordx4 %0, %1, %2 offset:" #OFF " sc1" : "=&v"(d) : "v"(voff), "s"(sbase) : "memory")
#define WAITV_TIE4(n, d) asm volatile("s_waitcnt vmcnt(" #n ")" : "+v"(d[0]), "+v"(d[1]), "+v"(d[2]), "+v"(d[3]) :: "memory")
DI void gstore_sc1(unsigned voff, const bf16x8& v, const void* sbase) {
  asm volatile("s_nop 4\n\tglobal_store_dwordx4 %0, %1, %2 sc1\n\ts_nop 1" :: "v"(voff), "v"(v), "s"(sbase) : "memory");
}
template <int OFFS> DI void gstore_sc1_c(unsigned voff, const bf16x8& v, const void* sbase) {
  unsigned t;
  asm volatile("v_add_u32 %0, %4, %1\n\ts_nop 3\n\tglobal_store_dwordx4 %0, %2, %3 sc1\n\ts_nop 1" : "=&v"(t) : "v"(voff), "v"(v), "s"(sbase), "i"(OFFS) : "memory");
}
template <int OFFS> DI void glds16_c(unsigned voff, const void* sbase, unsigned lds_dst) {
  unsigned keep, t;
  asm volatile("s_mov_b32 %0, m0\n\ts_mov_b32 m0, %4\n\tv_add_u32 %1, %5, %2\n\ts_nop 1\n\tglobal_load_lds_dwordx4 %1, %3\n\ts_mov_b32 m0, %0"
               : "=&s"(keep), "=&v"(t) : "v"(voff), "s"(sbase), "s"(lds_dst), "i"(OFFS) : "memory");
}
#define GLDS_OFF(voff, sbase, ldst, OFF) do { unsigned keep_; asm volatile("s_mov_b32 %0, m0\n\ts_mov_b32 m0, %3\n\ts_nop 2\n\tglobal_load_lds_dwordx4 %1, %2 offset:" #OFF "\n\ts_mov_b32 m0, %0" \
    : "=&s"(keep_) : "v"(voff), "s"(sbase), "s"(ldst) : "memory"); } while (0)
#define WAITVN(n) asm volatile("s_waitcnt vmcnt(%0)" :: "i"(n) : "memory")
DI const char* upt(const char* p) {
  const unsigned long long v = (unsigned long long)p;
  return (const char*)(((unsigned long long)rfl((unsigned)(v >> 32)) << 32) | rfl((unsigned)v));
}
DI void wait_word(unsigned* word, unsigned want, unsigned* tmo) {
  for (unsigned spins = 0;; ++spins) {
    if ((unsigned)rfl(__hip_atomic_load(word, __ATOMIC_RELAXED, __HIP_MEMORY_SCOPE_AGENT)) >= want) break;
    if (spins > (1u << 16)) { __hip_atomic_store(tmo, 1u, __ATOMIC_RELAXED, __HIP_MEMORY_SCOPE_AGENT); break; }
    __builtin_amdgcn_s_sleep(1);
  }
}
#define ACQUIRE() __builtin_amdgcn_fence(__ATOMIC_ACQUIRE, "agent")

template <int V> struct IC { static constexpr int value = V; };
template <int N, int I = 0, class F> DI void static_for(F&& f) { if constexpr (I < N) { f(IC<I>{}); static_for<N, I + 1>(f); } }
DI int lane_id() { unsigned l; asm volatile("v_mbcnt_lo_u32_b32 %0, -1, 0\n\tv_mbcnt_hi_u32_b32 %0, -1, %0" : "=v"(l)); return (int)l; }
struct PfW { const char* wo; const char* w1; const char* w2; };
template <int GRP>
DI void attn_main(ldsp lds, unsigned lds0, char* __restrict__ ws, unsigned* flags, int h, int j, int wid, int lane, int tid, bf16x8 (&qr)[8], unsigned kvmask, const PfW& pfw) {
  const int l31 = lane & 31, hh = lane >> 5;
  const unsigned vl16 = lane * 16;
  const char* Kh = ws + WS_KF + (size_t)h * 64 * 8192;
  const char* Vh = ws + WS_VF + (size_t)h * 128 * 4096;
  const unsigned vt2 = (unsigned)(wid * 2048 + lane * 16);
  const unsigned dt2 = lds0 + wid * 2048;
  const int n1 = j >= 4 ? 16 : 4 * j + 2;
  const int t2 = j >= 4 ? 4 * (8 - j) - 2 : 16;
  const int jp = 7 - j;
#define TILE(g) ((g) < n1 ? (g) : ((g) < 18 ? t2 + (g) - n1 : 0))
#define KSRC(g) (Kh + (size_t)TILE(g) * 16384)
#define VSRC(g) (Vh + (size_t)TILE(g) * 16384)
#define GLDS_T(src, dst, OFF) do { unsigned keep_; asm volatile("s_mov_b32 %0, m0\n\ts_mov_b32 m0, %3\n\ts_nop 2\n\tglobal_load_lds_dwordx4 %1, %2 offset:" #OFF " sc1\n\ts_mov_b32 m0, %0" \
    : "=&s"(keep_) : "v"(vt2), "s"(src), "s"(dst) : "memory"); } while (0)
  const unsigned stoff = lane < 8 ? (unsigned)(FL_KV + 16 * (8 * h + lane)) * 4u : (lane == 8 ? (unsigned)(FL_Q + 32 * (8 * h + jp)) * 4u : (unsigned)(FL_P + 32 * (8 * h + jp)) * 4u);
  unsigned fv = 0;
#define ST_ISSUE() do { if (wid == 0 && kvmask != 0x3ffu) asm volatile("s_nop 4\n\tglobal_load_dword %0, %1, %2 sc1" : "+v"(fv) : "v"(stoff), "s"(flags) : "memory"); } while (0)
#define ST_READ(N) do { if (wid == 0 && kvmask != 0x3ffu) { asm volatile("s_waitcnt vmcnt(" #N ")" : "+v"(fv) :: "memory"); kvmask |= (unsigned)__builtin_amdgcn_ballot_w64(fv != 0u) & 0x3ffu; } } while (0)
#define ENSURE_KV(g) do { if (wid == 0) { const int bk_ = TILE(g) >> 2; if (!((kvmask >> bk_) & 1u)) { wait_word(flags + FL_KV + 16 * (8 * h + bk_), 1u, flags + FL_TMO); kvmask |= 1u << bk_; } } } while (0)
  const int rdoff = lane * 16;
  bf16x8 kq[4], vq[4], pw[4];
  f32x16 C0, C1, o[4], negm;
  float mhat, lsum;
  const f32x16 z = {};
  unsigned pfd = 0;
#pragma unroll
  for (int i = 0; i < 4; ++i) kq[i] = ldsr128(lds + L_K + rdoff + i * 1024);
  {
    ST_ISSUE();
    ldscp kb = lds + L_K + rdoff;
#pragma unroll
    for (int u = 0; u < 16; ++u) {
      if (u < 8) C0 = MFMA32(kq[u & 3], qr[u & 7], u == 0 ? z : C0); else C1 = MFMA32(kq[u & 3], qr[u & 7], u == 8 ? z : C1);
      if (u + 4 < 16) kq[u & 3] = ldsr128(kb + (u + 4) * 1024);
      if (GRP == 1 && u == 11) {
        SBAR(); WAIT_V(0); WAIT_L(0); BAR();
        GLDS_T(VSRC(2), dt2 + L_V + 32768, 0); GLDS_T(VSRC(2), dt2 + L_V + 32768, 1024);
        GLDS_T(KSRC(4), dt2 + L_K, 0);
      }
      SBAR();
    }
    if (j == 0) cmask(C0, C1, 32 * wid + l31 - 4 * hh);
    float rm = xhalf_max(__builtin_fmaxf(max16(C0), max16(C1)));
    mhat = __builtin_fmaxf(rm, -1e30f);
#pragma unroll
    for (int r = 0; r < 16; ++r) { C0[r] -= mhat; C1[r] -= mhat; negm[r] = -mhat; }
    PIN(negm);
    if (GRP == 0) {
      SBAR(); WAIT_V(0); ST_READ(0); ENSURE_KV(2); ENSURE_KV(4);
      WAIT_L(0); BAR();
      if (tid == 0) {
        __hip_atomic_store(flags + FL_Q + 32 * (8 * h + j), 1u, __ATOMIC_RELAXED, __HIP_MEMORY_SCOPE_AGENT);
        if ((j & 3) >= 2) __hip_atomic_store(flags + FL_KV + 16 * (8 * h + j), 1u, __ATOMIC_RELAXED, __HIP_MEMORY_SCOPE_AGENT);
      }
      GLDS_T(VSRC(2), dt2 + L_V + 32768, 0);
    } else {
      GLDS_T(KSRC(4), dt2 + L_K, 1024);
    }
#pragma unroll
    for (int i = 0; i < 4; ++i) kq[i] = ldsr128(lds + L_K + 16384 + rdoff + i * 1024);
    float s0 = 0.f;
#pragma unroll
    for (int r = 0; r < 16; ++r) { C0[r] = EX2(C0[r]); s0 += C0[r]; }
#pragma unroll
    for (int r = 0; r < 8; ++r) C1[r] = EX2(C1[r]);
    lsum = s0;
    pw[0] = pack8(C0, 0); pw[1] = pack8(C0, 1);
#pragma unroll
    for (int d = 0; d < 4; ++d) o[d] = z;
  }
  const int ddm = (wid >> 1) + 1;
  const int gA = ddm > 3 ? 18 : (4 * jp + ddm - t2 > 0 ? n1 + 4 * jp + ddm - t2 : n1);
  auto stepg = [&](auto aq_, auto ap_, const int g) {
    constexpr bool AQ = decltype(aq_)::value != 0, AP = decltype(ap_)::value != 0;
    const bool seam = (g == n1);
    const int X = g < n1 ? j : jp;
    const int dd = TILE(g) - 4 * X;
    ldscp kb = lds + L_K + (g & 3) * 16384 + rdoff;
    ldscp vb = lds + L_V + ((g - 1) & 3) * 16384 + rdoff;
    ldscp kn = lds + L_K + ((g + 1) & 3) * 16384 + rdoff;
    const char* vs_n = VSRC(g + 2); const char* ks_n = KSRC(g + 4);
    const char* vs_p = VSRC(g + 1); const char* ks_p = KSRC(g + 3);
    const unsigned vd_n = dt2 + L_V + ((g + 2) & 3) * 16384, kd_n = dt2 + L_K + (g & 3) * 16384;
    const unsigned vd_p = dt2 + L_V + ((g + 1) & 3) * 16384, kd_p = dt2 + L_K + ((g - 1) & 3) * 16384;
    const bool gq = (GRP == 0) || g > 1;
    float lsum1 = 0.f, mhat1 = 0.f;
    if (seam) {
      mhat1 = mhat; mhat = 0.f;
#pragma unroll
      for (int r = 0; r < 16; ++r) negm[r] = 0.f;
      PIN(negm);
    }
    if (g >= 2 && g < 12 && wid == ((g - 2) & 7)) {
      const unsigned lc = (unsigned)(g - 2) * 64u + lane, gid = blockIdx.x * 592u + lc;
      const char* p = gid < 40960u ? pfw.wo + (size_t)gid * 128 : (gid < 96256u ? pfw.w1 + (size_t)(gid - 40960u) * 128 : pfw.w2 + (size_t)(gid - 96256u) * 128);
      if (lc < 592u && gid < 151552u) asm volatile("global_load_dword %0, %1, off" : "+v"(pfd) : "v"(p) : "memory");
    }
    ST_ISSUE();
    float sa = 0.f, sb = 0.f;
    SBAR();
#define KRD(u) kq[(u) & 3] = ldsr128(kb + ((u) + 4) * 1024)
#define VRD(v) vq[(v) & 3] = ldsr128(vb + (v) * 1024)
#define KPRE(i) kq[i] = ldsr128(kn + (i) * 1024)
#define BARRIER_G() do { if (GRP == 0) { ST_READ(3); ENSURE_KV(g + 4); if (wid == 0 && g == n1 - 2 && j != 0 && !(kvmask & 256u)) wait_word(flags + FL_Q + 32 * (8 * h + jp), 1u, flags + FL_TMO); \
        if (g == 17 && wid == 0 && lane == 0) *(LAS unsigned*)(lds + 131072) = kvmask; } \
      if (g < 17) WAIT_V(6); else WAIT_V(0); WAIT_L(0); BAR(); SBAR(); \
      if (GRP == 0 && (g == n1 + 2 || (g == 17 && n1 + 2 > 17)) && tid == 0) \
        __hip_atomic_store(flags + FL_P + 32 * (8 * h + j), 1u, __ATOMIC_RELAXED, __HIP_MEMORY_SCOPE_AGENT); } while (0)
    float fsc = 1.f; bool resc = false;
    {
#define Q_(x) do { if constexpr (AQ) { x; } } while (0)
#define P_(x) do { if constexpr (AP) { x; } } while (0)
      Q_(C0 = MFMA32(kq[0], qr[0], negm); KRD(0)); P_(C1[8] = EX2(C1[8]); sa += C1[0]; sb += C1[1]; sa += C1[2]; sb += C1[3]; PIN(C1); PIN(sa); PIN(sb)); SBAR();
      Q_(C0 = MFMA32(kq[1], qr[1], C0); KRD(1)); P_(C1[9] = EX2(C1[9]); sa += C1[4]; sb += C1[5]; sa += C1[6]; sb += C1[7]; PIN(C1); PIN(sa); PIN(sb)); SBAR();
      Q_(C0 = MFMA32(kq[2], qr[2], C0); KRD(2)); if (GRP == 0) GLDS_T(vs_p, vd_p, 1024); else if (gq) GLDS_T(ks_p, kd_p, 1024);
          P_(C1[10] = EX2(C1[10]); pw[2] = pack8(C1, 0); PIN(C1); PIN(pw[2])); SBAR();
      Q_(C0 = MFMA32(kq[3], qr[3], C0); KRD(3)); P_(C1[11] = EX2(C1[11]); C1[12] = EX2(C1[12]); sa += C1[8]; sb += C1[9]; PIN(C1); PIN(sa); PIN(sb)); SBAR();
      Q_(C0 = MFMA32(kq[0], qr[4], C0); KRD(4)); P_(C1[13] = EX2(C1[13]); C1[14] = EX2(C1[14]); sa += C1[10]; sb += C1[11]; PIN(C1); PIN(sa); PIN(sb)); SBAR();
      Q_(C0 = MFMA32(kq[1], qr[5], C0); KRD(5)); P_(C1[15] = EX2(C1[15]); sa += C1[12]; sb += C1[13]; sa += C1[14]; PIN(C1); PIN(sa); PIN(sb)); SBAR();
      Q_(C0 = MFMA32(kq[2], qr[6], C0); KRD(6)); P_(sb += C1[15]; pw[3] = pack8(C1, 1); PIN(pw[3]); PIN(sb)); SBAR();
      Q_(C0 = MFMA32(kq[3], qr[7], C0); KRD(7)); P_(lsum += sa + sb; PIN(lsum)); SBAR();
      Q_(C1 = MFMA32(kq[0], qr[0], negm); KRD(8)); if (GRP == 0) GLDS_T(ks_p, kd_p, 0); SBAR();
      Q_(C1 = MFMA32(kq[1], qr[1], C1); KRD(9)); SBAR();
      Q_(C1 = MFMA32(kq[2], qr[2], C1); KRD(10)); SBAR();
      float mxa = 0.f, mxb = 0.f;
      Q_(C1 = MFMA32(kq[3], qr[3], C1); KRD(11); mxa = max3f(C0[0], C0[1], C0[2]); mxb = max3f(C0[3], C0[4], C0[5]); PIN(mxa); PIN(mxb)); SBAR();
      if (GRP == 1) BARRIER_G();
      Q_(C1 = MFMA32(kq[0], qr[4], C1)); P_(VRD(0)); if (GRP == 1) GLDS_T(vs_n, vd_n, 0); Q_(mxa = max3f(mxa, C0[6], C0[7]); mxb = max3f(mxb, C0[8], C0[9]); PIN(mxa); PIN(mxb)); SBAR();
      Q_(C1 = MFMA32(kq[1], qr[5], C1)); P_(VRD(1)); Q_(mxa = max3f(mxa, C0[10], C0[11]); mxb = max3f(mxb, C0[12], C0[13]); PIN(mxa); PIN(mxb)); SBAR();
      Q_(C1 = MFMA32(kq[2], qr[6], C1)); P_(VRD(2)); Q_(mxa = max3f(mxa, C0[14], C0[15]); mxa = __builtin_fmaxf(mxa, mxb); PIN(mxa)); SBAR();
      Q_(C1 = MFMA32(kq[3], qr[7], C1)); P_(VRD(3)); SBAR();
      if (g == n1 - 1) {
        if (j == 0) wait_word(flags + FL_Q + 32 * (8 * h + jp), 1u, flags + FL_TMO);
        const char* qsrc = ws + WS_QF + (size_t)(h * 64 + 8 * jp + wid) * 8192;
        GLOAD1_SC1(qr[0], vl16, qsrc, 0); GLOAD1_SC1(qr[1], vl16, qsrc, 1024); GLOAD1_SC1(qr[2], vl16, qsrc, 2048); GLOAD1_SC1(qr[3], vl16, qsrc, 3072);
        GLOAD1_SC1(qr[4], vl16, qsrc + 4096, 0); GLOAD1_SC1(qr[5], vl16, qsrc + 4096, 1024); GLOAD1_SC1(qr[6], vl16, qsrc + 4096, 2048); GLOAD1_SC1(qr[7], vl16, qsrc + 4096, 3072);
      }
      SBAR();
      P_(o[0] = MFMA32(vq[0], pw[0], o[0]); VRD(4)); if (GRP == 1) GLDS_T(vs_n, vd_n, 1024); SBAR();
      P_(o[1] = MFMA32(vq[1], pw[0], o[1]); VRD(5)); SBAR();
      float mxc = 0.f, mxd = 0.f;
      P_(o[2] = MFMA32(vq[2], pw[0], o[2]); VRD(6)); if (GRP == 0) GLDS_T(ks_p, kd_p, 1024);
          Q_(mxc = max3f(C1[0], C1[1], C1[2]); mxd = max3f(C1[3], C1[4], C1[5]); mxc = max3f(mxc, C1[6], C1[7]); mxd = max3f(mxd, C1[8], C1[9]); PIN(mxc); PIN(mxd)); SBAR();
      P_(o[3] = MFMA32(vq[3], pw[0], o[3]); VRD(7)); Q_(mxc = max3f(mxc, C1[10], C1[11]); mxd = max3f(mxd, C1[12], C1[13]); mxc = max3f(mxc, C1[14], C1[15]); mxa = max3f(mxa, mxc, mxd); PIN(mxa)); SBAR();
      P_(o[0] = MFMA32(vq[0], pw[1], o[0]); VRD(8));
      MEMFENCE(); SBAR();
      if constexpr (AQ) {
        float rm = mxa;
        if (dd >= 0 && wid <= 2 * dd + 1) { cmask(C0, C1, 32 * wid + l31 - 64 * dd - 4 * hh); rm = __builtin_fmaxf(max16(C0), max16(C1)); }
        rm = xhalf_max(rm);
        if (__builtin_expect(seam || __any(rm > THR), 0)) {
          const float dl = seam ? __builtin_fmaxf(rm, -1e30f) : __builtin_fmaxf(rm, 0.f);
          mhat += dl;
#pragma unroll
          for (int r = 0; r < 16; ++r) { C0[r] -= dl; C1[r] -= dl; negm[r] = -mhat; }
          PIN(negm);
          if (seam) { lsum1 = lsum; lsum = 0.f; }
          else { fsc = EX2(-dl); lsum *= fsc; resc = true; }
        }
      } else {
        if (seam) { lsum1 = lsum; lsum = 0.f; mhat = -1e30f; }
      }
      SBAR();
      sa = 0.f; sb = 0.f;
      P_(o[1] = MFMA32(vq[1], pw[1], o[1]); VRD(9)); Q_(C0[0] = EX2(C0[0]); C0[1] = EX2(C0[1]); C0[2] = EX2(C0[2]); PIN(C0)); SBAR();
      P_(o[2] = MFMA32(vq[2], pw[1], o[2]); VRD(10)); Q_(C0[3] = EX2(C0[3]); C0[4] = EX2(C0[4]); sa += C0[0]; sb += C0[1]; PIN(C0); PIN(sa); PIN(sb)); SBAR();
      P_(o[3] = MFMA32(vq[3], pw[1], o[3]); VRD(11)); Q_(C0[5] = EX2(C0[5]); C0[6] = EX2(C0[6]); sa += C0[2]; sb += C0[3]; PIN(C0); PIN(sa); PIN(sb)); SBAR();
      P_(o[0] = MFMA32(vq[0], pw[2], o[0]); VRD(12)); if (GRP == 1) GLDS_T(ks_n, kd_n, 0); Q_(C0[7] = EX2(C0[7]); C0[8] = EX2(C0[8]); sa += C0[4]; sb += C0[5]; PIN(C0); PIN(sa); PIN(sb)); SBAR();
      P_(o[1] = MFMA32(vq[1], pw[2], o[1]); VRD(13)); Q_(pw[0] = pack8(C0, 0); C0[9] = EX2(C0[9]); PIN(pw[0]); PIN(C0)); SBAR();
      P_(o[2] = MFMA32(vq[2], pw[2], o[2]); VRD(14)); Q_(C0[10] = EX2(C0[10]); C0[11] = EX2(C0[11]); sa += C0[6]; sb += C0[7]; PIN(C0); PIN(sa); PIN(sb)); SBAR();
      P_(o[3] = MFMA32(vq[3], pw[2], o[3]); VRD(15)); Q_(C0[12] = EX2(C0[12]); C0[13] = EX2(C0[13]); sa += C0[8]; sb += C0[9]; PIN(C0); PIN(sa); PIN(sb)); SBAR();
      if (GRP == 0) BARRIER_G();
      P_(o[0] = MFMA32(vq[0], pw[3], o[0])); KPRE(0); if (GRP == 0) GLDS_T(vs_n, vd_n, 0); Q_(C0[14] = EX2(C0[14]); C0[15] = EX2(C0[15]); sa += C0[10]; sb += C0[11]; PIN(C0); PIN(sa); PIN(sb)); SBAR();
      P_(o[1] = MFMA32(vq[1], pw[3], o[1])); KPRE(1); Q_(C1[0] = EX2(C1[0]); C1[1] = EX2(C1[1]); sa += C0[12]; sb += C0[13]; PIN(C1); PIN(sa); PIN(sb)); SBAR();
      P_(o[2] = MFMA32(vq[2], pw[3], o[2])); KPRE(2); Q_(pw[1] = pack8(C0, 1); C1[2] = EX2(C1[2]); C1[3] = EX2(C1[3]); PIN(pw[1]); PIN(C1)); SBAR();
      P_(o[3] = MFMA32(vq[3], pw[3], o[3])); KPRE(3); Q_(C1[4] = EX2(C1[4]); C1[5] = EX2(C1[5]); sa += C0[14]; sb += C0[15]; PIN(C1); PIN(sa); PIN(sb)); SBAR();
      Q_(C1[6] = EX2(C1[6]); C1[7] = EX2(C1[7]); lsum += sa + sb; PIN(C1); PIN(lsum));
#undef Q_
#undef P_
    }
    if (resc) {
#pragma unroll
      for (int d = 0; d < 4; ++d) o[d] = o[d] * fsc;
    }
    if (seam) {
      const float lt = xhalf_sum(lsum1);
      const char* pst = ws + WS_PF + (size_t)((h * 8 + j) * 8 + wid) * 8192;
      static_for<4>([&](auto dd_) { constexpr int d = decltype(dd_)::value; gstore_sc1_c<(2 * d) * 1024>(vl16, pack8(o[d], 0), pst); gstore_sc1_c<(2 * d + 1) * 1024>(vl16, pack8(o[d], 1), pst); o[d] = z; });
      const unsigned long long sv = ((unsigned long long)__float_as_uint(lt) << 32) | __float_as_uint(mhat1);
      __hip_atomic_store((unsigned long long*)(ws + WS_PS) + (size_t)((h * 8 + j) * 8 + wid) * 64 + lane, sv, __ATOMIC_RELAXED, __HIP_MEMORY_SCOPE_AGENT);
    }
    if (g == n1 - 1) {
      asm volatile("s_waitcnt vmcnt(1)" : "+v"(qr[0]), "+v"(qr[1]), "+v"(qr[2]), "+v"(qr[3]), "+v"(qr[4]), "+v"(qr[5]), "+v"(qr[6]), "+v"(qr[7]) :: "memory");
    }
  };
  for (int g = 1; g < gA; ++g) stepg(IC<1>{}, IC<1>{}, g);
  if (gA < 18) stepg(IC<0>{}, IC<1>{}, gA);
  for (int g = gA + 1; g < 18; ++g) stepg(IC<0>{}, IC<0>{}, g);
  const bool actP = gA >= 18;
  asm volatile("" :: "v"(pfd));
  {
    const bool early = (rfl(*(const LAS unsigned*)(lds + 131072)) & 512u) != 0u;
    const char* psrc = upt(ws + WS_PF + (size_t)((h * 8 + jp) * 8 + wid) * 8192);
    const char* psrc2 = upt(psrc + 4096);
    const unsigned long long* svp = (const unsigned long long*)(ws + WS_PS) + (size_t)((h * 8 + jp) * 8 + wid) * 64 + lane;
    u32x4 qa[4], qb[4];
    u32x2 svv;
#define PLOADS() do { GLOAD1_SC1(qa[0], vl16, psrc, 0); GLOAD1_SC1(qa[1], vl16, psrc, 1024); GLOAD1_SC1(qa[2], vl16, psrc, 2048); GLOAD1_SC1(qa[3], vl16, psrc, 3072); \
      GLOAD1_SC1(qb[0], vl16, psrc2, 0); GLOAD1_SC1(qb[1], vl16, psrc2, 1024); GLOAD1_SC1(qb[2], vl16, psrc2, 2048); GLOAD1_SC1(qb[3], vl16, psrc2, 3072); \
      asm volatile("global_load_dwordx2 %0, %1, off sc1" : "=&v"(svv) : "v"(svp) : "memory"); } while (0)
    if (early) PLOADS();
    ldscp vb = lds + L_V + (17 & 3) * 16384 + rdoff;
    if (actP) {
#pragma unroll
    for (int i = 0; i < 4; ++i) vq[i] = ldsr128(vb + i * 1024);
    float sa = 0.f;
#pragma unroll
    for (int r = 8; r < 16; ++r) C1[r] = EX2(C1[r]);
#pragma unroll
    for (int r = 0; r < 16; ++r) sa += C1[r];
    lsum += sa;
    pw[2] = pack8(C1, 0); pw[3] = pack8(C1, 1);
    SBAR();
#pragma unroll
    for (int v = 0; v < 16; ++v) {
      o[v & 3] = MFMA32(vq[v & 3], pw[v >> 2], o[v & 3]);
      if (v + 4 < 16) vq[v & 3] = ldsr128(vb + (v + 4) * 1024);
      SBAR();
    }
    }
    lsum = xhalf_sum(lsum);
    if (!early) { wait_word(flags + FL_P + 32 * (8 * h + jp), 1u, flags + FL_TMO); PLOADS(); }
    WAITV_TIE4(0, qa); WAITV_TIE4(0, qb);
    asm volatile("" : "+v"(svv));
    const unsigned long long sv = ((unsigned long long)svv[1] << 32) | svv[0];
#undef PLOADS
    const float om = __uint_as_float((unsigned)sv), ol = __uint_as_float((unsigned)(sv >> 32));
    const float m = __builtin_fmaxf(mhat, om);
    const float f0 = EX2(mhat - m), f1 = EX2(om - m);
    const float inv = __builtin_amdgcn_rcpf(lsum * f0 + ol * f1);
    const float g0 = f0 * inv, g1 = f1 * inv;
    const char* cdst = upt(ws + WS_CTX + (size_t)((8 * jp + wid) * 32 + h) * 8192);
    static_for<8>([&](auto ii_) {
      constexpr int i = decltype(ii_)::value; constexpr int d = i >> 1, sp = i & 1;
      const u32x4 v4 = i < 4 ? qa[i & 3] : qb[i & 3];
      u32x4 w;
#pragma unroll
      for (int e = 0; e < 4; ++e) w[e] = cvtpk(o[d][8 * sp + 2 * e] * g0 + bflo(v4[e]) * g1, o[d][8 * sp + 2 * e + 1] * g0 + bfhi(v4[e]) * g1);
      gstore_sc1_c<i * 1024>(vl16, __builtin_bit_cast(bf16x8, w), cdst);
    });
  }
#undef KRD
#undef VRD
#undef KPRE
#undef BARRIER_G
#undef ENSURE_KV
#undef GLDS_T
#undef TILE
#undef KSRC
#undef VSRC
}

constexpr int T_LDS = 163840;
constexpr int FL_C = 28672;
constexpr int FL_P3 = 20480;
constexpr int FL_H = 22528;
constexpr int FL_U = 23552;
#define GLOAD4_SC1(d, voff, sbase) asm volatile("s_nop 4\n\tglobal_load_dwordx4 %0, %4, %5 sc1\n\tglobal_load_dwordx4 %1, %4, %5 offset:1024 sc1\n\tglobal_load_dwordx4 %2, %4, %5 offset:2048 sc1\n\tglobal_load_dwordx4 %3, %4, %5 offset:3072 sc1" \
    : "=&v"(d[0]), "=&v"(d[1]), "=&v"(d[2]), "=&v"(d[3]) : "v"(voff), "s"(sbase) : "memory")
#define GLDS_SC1(voff, sbase, ldst) do { unsigned keep_; asm volatile("s_mov_b32 %0, m0\n\ts_mov_b32 m0, %3\n\ts_nop 2\n\tglobal_load_lds_dwordx4 %1, %2 sc1\n\ts_mov_b32 m0, %0" \
    : "=&s"(keep_) : "v"(voff), "s"(sbase), "s"(ldst) : "memory"); } while (0)
template <int OFFS> DI void gstore_sc1_uc(unsigned voff, const u32x4& v, const void* sbase) {
  unsigned t;
  asm volatile("v_add_u32 %0, %4, %1\n\ts_nop 3\n\tglobal_store_dwordx4 %0, %2, %3 sc1\n\ts_nop 1" : "=&v"(t) : "v"(voff), "v"(v), "s"(sbase), "i"(OFFS) : "memory");
}
DI void gstore_sc1_u(unsigned voff, const u32x4& v, const void* sbase) {
  asm volatile("s_nop 4\n\tglobal_store_dwordx4 %0, %1, %2 sc1\n\ts_nop 1" :: "v"(voff), "v"(v), "s"(sbase) : "memory");
}
template <bool FIRST> struct MlpCfg {
  static constexpr int PITCH = FIRST ? FF * 2 : SEQ * 2;
  static constexpr int NKT = FIRST ? SEQ / 64 : FF / 64;
  static constexpr int NST = (NKT + 1) / 2;
};
template <bool FIRST> DI unsigned mlp_voffw(int wid, int lane) {
  const int row = 8 * wid + (lane >> 3); const int f = ((row >> 1) & 1) | (((row >> 3) & 1) << 1);
  return (unsigned)(row * MlpCfg<FIRST>::PITCH + (((lane & 7) ^ (f << 1)) << 4));
}
template <bool FIRST, int KT0, int NK> DI void mlp_wstage(unsigned lds0, const char* wb, unsigned voffW, int wid, int reg) {
  static_for<NK>([&](auto kk_) {
    constexpr int k = decltype(kk_)::value; constexpr int kt = (KT0 + k) < MlpCfg<FIRST>::NKT ? (KT0 + k) : MlpCfg<FIRST>::NKT - 1;
    glds16_c<kt * 64 * MlpCfg<FIRST>::PITCH>(voffW, wb, lds0 + reg + k * 8192 + wid * 1024);
  });
}
struct MlpW { ldscp p[4]; };
DI MlpW mlp_wptr(ldsp lds, int wid, int lane) {
  const int g = lane >> 4, q = (lane & 15) >> 2, p = lane & 3; const int f = (q >> 1) | ((g & 1) << 1);
  MlpW w;
#pragma unroll
  for (int ct = 0; ct < 4; ++ct) w.p[ct] = lds + (wid >> 1) * 8192 + 128 * (32 * (wid & 1) + 8 * g + q) + (((2 * ct + (p >> 1)) ^ (f << 1)) << 4) + 8 * (p & 1);
  return w;
}
DI void mlp_wrd(const MlpW& w, int off, bf16x8* dst) {
#pragma unroll
  for (int ct = 0; ct < 4; ++ct) dst[ct] = cat8(vtr(w.p[ct] + off), vtr(w.p[ct] + off + 512));
}
#define MLP_ALOAD(dst, base) asm volatile("s_nop 4\n\tglobal_load_dwordx4 %0, %5, %7 sc1\n\tglobal_load_dwordx4 %1, %5, %7 offset:1024 sc1\n\tglobal_load_dwordx4 %2, %5, %7 offset:2048 sc1\n\t" \
    "global_load_dwordx4 %3, %5, %7 offset:3072 sc1\n\tglobal_load_dwordx4 %4, %6, %7 sc1" \
    : "=&v"(dst[0]), "=&v"(dst[1]), "=&v"(dst[2]), "=&v"(dst[3]), "=&v"(dst[4]) : "v"(vl16), "v"(vl16b), "s"(base) : "memory")
template <bool FIRST, int R0, int R1, int LK0, int NW>
DI void mlp_main(ldsp lds, const char* __restrict__ ab, int wid, int lane, const MlpW& wp, const bf16x8 (&wr)[NW], f32x4 (&acc)[4][5]) {
  constexpr int NT = 2 * MlpCfg<FIRST>::NKT, NS = (NT + 7) / 8;
  const unsigned vl16 = lane * 16, vl16b = lane * 16 + 4096;
  u32x4 af[3][5];
  bf16x8 wl[2][4];
#pragma unroll
  for (int c = 0; c < 4; ++c)
#pragma unroll
    for (int d = 0; d < 5; ++d) acc[c][d] = (f32x4){0.f, 0.f, 0.f, 0.f};
  auto abase = [&](int s) { const int T = 8 * s + wid; return upt(ab + (size_t)(T < NT ? T : NT - 1) * 40960); };
  { const char* b0 = abase(0); MLP_ALOAD(af[0], b0); const char* b1 = abase(1); MLP_ALOAD(af[1], b1); }
  if constexpr (!(0 >= R0 && 0 < R1)) mlp_wrd(wp, (0 - LK0) * 8192, wl[0]);
  static_for<NS>([&](auto ss_) {
    constexpr int s = decltype(ss_)::value;
    if constexpr (s + 2 < NS) { const char* bn = abase(s + 2); MLP_ALOAD(af[(s + 2) % 3], bn); }
    if constexpr (s + 1 < NS && !(s + 1 >= R0 && s + 1 < R1)) mlp_wrd(wp, (4 * (s + 1) - LK0) * 8192, wl[(s + 1) & 1]);
    constexpr int ahead = (NS - 1 - s) < 2 ? (NS - 1 - s) : 2;
    asm volatile("s_waitcnt vmcnt(%5)" : "+v"(af[s % 3][0]), "+v"(af[s % 3][1]), "+v"(af[s % 3][2]), "+v"(af[s % 3][3]), "+v"(af[s % 3][4]) : "i"(5 * ahead) : "memory");
    SBAR();
    if (8 * s + 7 < NT || 8 * s + wid < NT) {
#pragma unroll
      for (int c = 0; c < 4; ++c) {
        const bf16x8 wfr = (s >= R0 && s < R1) ? wr[(s >= R0 && s < R1) ? (s - R0) * 4 + c : 0] : wl[s & 1][c];
#pragma unroll
        for (int d = 0; d < 5; ++d) {
          const bf16x8 a8 = __builtin_bit_cast(bf16x8, af[s % 3][d]);
          acc[c][d] = FIRST ? MFMA16(wfr, a8, acc[c][d]) : MFMA16(a8, wfr, acc[c][d]);
        }
      }
    }
    SBAR();
  });
}
DI void mlp_park(ldsp lds, int wid, int lane, f32x4 (&acc)[4][5]) {
  ldsp pw_ = lds + wid * 10240 + lane * 16;
  ldsp pr_ = lds + (wid ^ 4) * 10240 + lane * 16;
  if (wid < 4) {
#pragma unroll
    for (int c = 0; c < 2; ++c)
#pragma unroll
      for (int d = 0; d < 5; ++d) *(LAS f32x4*)(pw_ + (c * 5 + d) * 1024) = acc[2 + c][d];
  } else {
#pragma unroll
    for (int c = 0; c < 2; ++c)
#pragma unroll
      for (int d = 0; d < 5; ++d) *(LAS f32x4*)(pw_ + (c * 5 + d) * 1024) = acc[c][d];
  }
  WAIT_L(0); BAR();
  if (wid < 4) {
#pragma unroll
    for (int c = 0; c < 2; ++c)
#pragma unroll
      for (int d = 0; d < 5; ++d) { const f32x4 o = *(const LAS f32x4*)(pr_ + (c * 5 + d) * 1024); *(LAS f32x4*)(pr_ + (c * 5 + d) * 1024) = acc[c][d] + o; }
  } else {
#pragma unroll
    for (int c = 0; c < 2; ++c)
#pragma unroll
      for (int d = 0; d < 5; ++d) { const f32x4 o = *(const LAS f32x4*)(pr_ + (c * 5 + d) * 1024); *(LAS f32x4*)(pr_ + (c * 5 + d) * 1024) = acc[2 + c][d] + o; }
  }
  WAIT_L(0); BAR();
}
DI void mlp_gather(ldscp lds, int c2, int dt, int gD, int c15, float (&v)[8]) {
  ldscp p = lds + ((c2 < 2 ? 4 : 0) * 10 + (c2 & 1) * 5 + dt) * 1024 + (gD * 16 + c15) * 16;
#pragma unroll
  for (int hf = 0; hf < 2; ++hf) {
    f32x4 a = {0.f, 0.f, 0.f, 0.f};
#pragma unroll
    for (int q = 0; q < 4; ++q) a += *(const LAS f32x4*)(p + q * 10240 + hf * 256);
#pragma unroll
    for (int e = 0; e < 4; ++e) v[4 * hf + e] = a[e];
  }
}

DI void tail_phase(unsigned char* smem, const u16* __restrict__ x, const u16* __restrict__ wo, const u16* __restrict__ w1, const u16* __restrict__ b1,
                   const u16* __restrict__ w2, const u16* __restrict__ b2, char* __restrict__ ws, u16* __restrict__ out, int wid) {
  const int b = blockIdx.x, xcd = b & 7, r = b >> 3;
  ldsp lds = (ldsp)smem;
  const unsigned lds0 = (unsigned)(uintptr_t)smem;
  unsigned* flags = (unsigned*)(ws + WS_FLAGS);
  const int ksp = xcd >> 1, panel = (xcd & 1) * 8 + (r >> 2), ctile = r & 3;
  const int idx5 = xcd * 27 + r;
  const int wt5 = idx5 >> 3, dg5 = idx5 & 7;
  const int wt6 = xcd * 4 + (r >> 3), dg6 = r & 7;
  const bool do5 = r < 27;
  constexpr int P4S = 131072;
  bf16x8 wf[16];
  {
    const int lane = lane_id();
    const unsigned vl16 = lane * 16;
    const int kh = wid >> 2, st = wid & 3, hh = lane >> 5;
    unsigned voff[5];
#pragma unroll
    for (int i = 0; i < 5; ++i) {
      const int id = 2 * (5 * wid + i) + (lane >> 5), half = id / 40, rem = id % 40, rg = rem / 5, c = rem % 5;
      const int row = 64 * half + 8 * rg + ((lane >> 2) & 7);
      voff[i] = (unsigned)(row * 1280 + (32 * c + 8 * ((lane & 3) ^ ((row >> 2) & 3))) * 2);
    }
    const char* wb = (const char*)wo + (size_t)(ksp * 1024) * 1280 + ctile * 320;
    const unsigned dstw = lds0 + wid * 5120;
#define OPIECE(hd, i) glds16_c<(hd) * 128 * 1280>(voff[i], wb, dstw + ((hd) & 3) * 40960 + (i) * 1024)
#define STAGE_O(hd) do { OPIECE(hd, 0); OPIECE(hd, 1); OPIECE(hd, 2); OPIECE(hd, 3); OPIECE(hd, 4); } while (0)
    const char* cb = ws + WS_CTX + ((size_t)((4 * panel + st) * 32 + 8 * ksp) * 8 + 4 * kh) * 1024;
    int boff[2];
    { const int blk = (lane >> 4) & 1, q = (lane & 15) >> 2, p = lane & 3;
#pragma unroll
      for (int t = 0; t < 2; ++t) boff[t] = kh * 20480 + 2560 * t + 64 * (4 * hh + q) + (((2 * blk + (p >> 1)) ^ ((2 * t + hh) & 3)) << 4) + 8 * (p & 1); }
    ldscp bp0 = lds + boff[0], bp1 = lds + boff[1];
    u32x4 xf[3][4];
    f32x16 acc[5] = {};
    bf16x8 aq[6];
#define ARD(U) aq[(U) % 6] = cat8(vtr(bp0 + (((U) / 20) & 3) * 40960 + (((U) % 20) / 5) * 5120 + ((U) % 5) * 512), vtr(bp1 + (((U) / 20) & 3) * 40960 + (((U) % 20) / 5) * 5120 + ((U) % 5) * 512))
    STAGE_O(0); STAGE_O(1); STAGE_O(2);
    if (wid == 0) wait_word(flags + FL_C + 16 * ((panel >> 1) * 4 + ksp), 8u, flags + FL_TMO);
    BAR();
    GLOAD4_SC1(xf[0], vl16, cb); GLOAD4_SC1(xf[1], vl16, cb + 8192);
    WAIT_V(8); BAR();
#pragma unroll
    for (int U = 0; U < 5; ++U) ARD(U);
    static_for<8>([&](auto hh_) {
      constexpr int hd = decltype(hh_)::value;
      if constexpr (hd == 0) { WAITV_TIE4(4, xf[0]); }
      else if constexpr (hd <= 5) { WAITV_TIE4(9, xf[hd % 3]); }
      else if constexpr (hd == 6) { WAITV_TIE4(4, xf[hd % 3]); }
      else { WAITV_TIE4(0, xf[hd % 3]); }
      SBAR(); BAR(); SBAR();
      const char* xn_ = cb + (size_t)(hd + 2) * 8192;
      static_for<20>([&](auto uu_) {
        constexpr int u = decltype(uu_)::value;
        constexpr int U = 20 * hd + u;
        acc[u % 5] = MFMA32(aq[U % 6], __builtin_bit_cast(bf16x8, xf[hd % 3][u / 5]), acc[u % 5]);
        if constexpr (U + 5 < 160) ARD(U + 5);
        if constexpr (hd + 2 < 8) {
          if constexpr (u == 1) GLOAD1_SC1(xf[(hd + 2) % 3][0], vl16, xn_, 0);
          if constexpr (u == 3) GLOAD1_SC1(xf[(hd + 2) % 3][1], vl16, xn_, 1024);
          if constexpr (u == 5) GLOAD1_SC1(xf[(hd + 2) % 3][2], vl16, xn_, 2048);
          if constexpr (u == 7) GLOAD1_SC1(xf[(hd + 2) % 3][3], vl16, xn_, 3072);
        }
        if constexpr (hd + 3 < 8) {
          if constexpr (u == 9) OPIECE(hd + 3, 0);
          if constexpr (u == 11) OPIECE(hd + 3, 1);
          if constexpr (u == 13) OPIECE(hd + 3, 2);
          if constexpr (u == 15) OPIECE(hd + 3, 3);
          if constexpr (u == 17) OPIECE(hd + 3, 4);
        }
        SBAR();
      });
    });
#undef OPIECE
#undef STAGE_O
#undef ARD
    BAR();
    ldsp xw = lds + (kh == 0 ? 49152 : 0) + st * 12288 + lane * 16;
    ldscp xr = lds + (kh == 0 ? 0 : 49152) + st * 12288 + lane * 16;
    if (kh == 0) {
#pragma unroll
      for (int i = 0; i < 2; ++i)
#pragma unroll
        for (int g = 0; g < 4; ++g) *(LAS f32x4*)(xw + (i * 4 + g) * 1024) = (f32x4){acc[3 + i][4 * g], acc[3 + i][4 * g + 1], acc[3 + i][4 * g + 2], acc[3 + i][4 * g + 3]};
    } else {
#pragma unroll
      for (int i = 0; i < 3; ++i)
#pragma unroll
        for (int g = 0; g < 4; ++g) *(LAS f32x4*)(xw + (i * 4 + g) * 1024) = (f32x4){acc[i][4 * g], acc[i][4 * g + 1], acc[i][4 * g + 2], acc[i][4 * g + 3]};
    }
    WAIT_L(0); BAR();
    const char* sb = ws + WS_SLAB + ((size_t)((ksp * 64 + 4 * panel + st) * 20 + 5 * ctile) * 2) * 1024;
#define SLABST(I, T) static_for<2>([&](auto pp_) { \
          constexpr int sp = decltype(pp_)::value; \
          const f32x4 o0 = *(const LAS f32x4*)(xr + ((I) * 4 + 2 * sp) * 1024), o1 = *(const LAS f32x4*)(xr + ((I) * 4 + 2 * sp + 1) * 1024); \
          const u32x4 w = {cvtpk(acc[T][8 * sp] + o0[0], acc[T][8 * sp + 1] + o0[1]), cvtpk(acc[T][8 * sp + 2] + o0[2], acc[T][8 * sp + 3] + o0[3]), \
                           cvtpk(acc[T][8 * sp + 4] + o1[0], acc[T][8 * sp + 5] + o1[1]), cvtpk(acc[T][8 * sp + 6] + o1[2], acc[T][8 * sp + 7] + o1[3])}; \
          gstore_sc1_uc<((T) * 2 + sp) * 1024>(vl16, w, sb); })
    if (kh == 0) { SLABST(0, 0); SLABST(1, 1); SLABST(2, 2); }
    else { SLABST(0, 3); SLABST(1, 4); }
#undef SLABST
    WAIT_V(0); WAIT_L(0); BAR();
    if (wid == 0 && lane == 0) __hip_atomic_fetch_add(flags + FL_P3 + 64 * panel, 1u, __ATOMIC_RELAXED, __HIP_MEMORY_SCOPE_AGENT);
  }
  {
    const int lane = lane_id(), tid = wid * 64 + lane;
    const MlpW wp = mlp_wptr(lds, wid, lane);
    const char* w1b = (const char*)w1 + wt5 * 128;
    const unsigned voffW = mlp_voffw<true>(wid, lane);
    ldsp tr = lds + P4S;
    LAS float* red = (LAS float*)(lds + P4S + 10240);
    const int rr = tid & 7, pc = tid >> 3;
    const int r8 = ksp * 4 + ctile;
    const int s = 128 * panel + 8 * r8 + rr, st = s >> 5;
    int n0[2]; unsigned so[2];
    u32x2 xv[2][2];
#pragma unroll
    for (int i = 0; i < 2; ++i) {
      const int pi = pc + 64 * i; const int pcl = pi < 80 ? pi : 0;
      const int nt = pcl >> 2, sp = (pcl >> 1) & 1, hh = pcl & 1;
      n0[i] = 32 * nt + 16 * sp + 4 * hh;
      so[i] = (unsigned)((((st * 20 + nt) * 2 + sp) * 64 + (s & 31) + 32 * hh) * 16);
      xv[i][0] = *(const u32x2*)(x + (size_t)s * DM + n0[i]); xv[i][1] = *(const u32x2*)(x + (size_t)s * DM + n0[i] + 8);
    }
    asm volatile("" : "+v"(xv[0][0]), "+v"(xv[0][1]), "+v"(xv[1][0]), "+v"(xv[1][1]));
    if (do5) mlp_wstage<true, 0, 16>(lds0, w1b, voffW, wid, 0);
    if (wid == 0) wait_word(flags + FL_P3 + 64 * panel, 16u, flags + FL_TMO);
    BAR();
    u32x4 sv[2][4];
    const char* sl = ws + WS_SLAB;
#pragma unroll
    for (int i = 0; i < 2; ++i)
#pragma unroll
      for (int k = 0; k < 4; ++k) GLOAD1_SC1(sv[i][k], so[i], sl + (size_t)k * (64 * 20 * 2 * 1024), 0);
    WAITV_TIE4(0, sv[0]); WAITV_TIE4(0, sv[1]);
    float hv[2][8];
    unsigned pk[2][4];
    float sum = 0.f, ssq = 0.f;
#pragma unroll
    for (int i = 0; i < 2; ++i) {
      const bool ok = pc + 64 * i < 80;
#pragma unroll
      for (int e = 0; e < 4; ++e) {
        const float lo = (bflo(sv[i][0][e]) + bflo(sv[i][1][e])) + (bflo(sv[i][2][e]) + bflo(sv[i][3][e]));
        const float hi = (bfhi(sv[i][0][e]) + bfhi(sv[i][1][e])) + (bfhi(sv[i][2][e]) + bfhi(sv[i][3][e]));
        const unsigned xw_ = xv[i][e >> 1][e & 1];
        pk[i][e] = cvtpk(lo + bflo(xw_), hi + bfhi(xw_));
        hv[i][2 * e] = ok ? bflo(pk[i][e]) : 0.f; hv[i][2 * e + 1] = ok ? bfhi(pk[i][e]) : 0.f;
        sum += hv[i][2 * e] + hv[i][2 * e + 1];
      }
    }
    sum += __shfl_xor(sum, 8); sum += __shfl_xor(sum, 16); sum += __shfl_xor(sum, 32);
    const float cntw = (wid < 2) ? 128.f : 64.f;
    const float mw = sum / cntw;
#pragma unroll
    for (int i = 0; i < 2; ++i) {
      if (pc + 64 * i < 80) {
#pragma unroll
        for (int e = 0; e < 8; ++e) { const float d = hv[i][e] - mw; ssq += d * d; }
      }
    }
    ssq += __shfl_xor(ssq, 8); ssq += __shfl_xor(ssq, 16); ssq += __shfl_xor(ssq, 32);
    if (lane < 8) { red[(wid * 8 + lane) * 2] = mw; red[(wid * 8 + lane) * 2 + 1] = ssq; }
    WAIT_L(0); BAR();
    float mean = 0.f;
#pragma unroll
    for (int w = 0; w < 8; ++w) mean += red[(w * 8 + rr) * 2] * (w < 2 ? 128.f : 64.f);
    mean *= (1.0f / DM);
    float m2 = 0.f;
#pragma unroll
    for (int w = 0; w < 8; ++w) { const float dm = red[(w * 8 + rr) * 2] - mean; m2 += red[(w * 8 + rr) * 2 + 1] + dm * dm * (w < 2 ? 128.f : 64.f); }
    const float rstd = 1.0f / sqrtf(m2 * (1.0f / DM) + LN_EPS);
#pragma unroll
    for (int i = 0; i < 2; ++i) {
      if (pc + 64 * i < 80) {
        LAS u16* t16 = (LAS u16*)tr;
#pragma unroll
        for (int e = 0; e < 4; ++e) {
          const unsigned p = cvtpk((hv[i][2 * e] - mean) * rstd, (hv[i][2 * e + 1] - mean) * rstd);
          const int n = n0[i] + 8 * (e >> 1) + 2 * (e & 1);
          t16[n * 8 + rr] = (u16)(p & 0xffff); t16[(n + 1) * 8 + rr] = (u16)(p >> 16);
        }
      }
    }
    WAIT_L(0); BAR();
    const char* hdst = ws + WS_HNF + ((size_t)st * 40 * 64 + ((s >> 3) & 3) * 16) * 16;
    { const int d = tid; const u32x4 tv4 = *(const LAS u32x4*)(tr + d * 16); gstore_sc1_u((unsigned)(((d >> 4) * 64 + (d & 15)) * 16), tv4, hdst); }
    if (tid < 128) { const int d = 512 + tid; const u32x4 tv4 = *(const LAS u32x4*)(tr + d * 16); gstore_sc1_u((unsigned)(((d >> 4) * 64 + (d & 15)) * 16), tv4, hdst); }
#pragma unroll
    for (int i = 0; i < 2; ++i) {
      if (pc + 64 * i < 80) {
        unsigned long long* hp = (unsigned long long*)(ws + WS_H + ((size_t)s * DM + n0[i]) * 2);
        __hip_atomic_store(hp, ((unsigned long long)pk[i][1] << 32) | pk[i][0], __ATOMIC_RELAXED, __HIP_MEMORY_SCOPE_AGENT);
        __hip_atomic_store(hp + 2, ((unsigned long long)pk[i][3] << 32) | pk[i][2], __ATOMIC_RELAXED, __HIP_MEMORY_SCOPE_AGENT);
      }
    }
    if (do5) {
#pragma unroll
      for (int s4 = 0; s4 < 4; ++s4) mlp_wrd(wp, s4 * 32768, wf + 4 * s4);
    }
    WAIT_V(0); WAIT_L(0); BAR();
    if (tid == 0) __hip_atomic_fetch_add(flags + FL_H + 64 * (b & 7), 1u, __ATOMIC_RELAXED, __HIP_MEMORY_SCOPE_AGENT);
    if (do5) mlp_wstage<true, 16, 16>(lds0, w1b, voffW, wid, 0);
  }
  constexpr int RHI = 81920;
  {
    const int lane = lane_id(), tid = wid * 64 + lane;
    const char* w2b = (const char*)w2 + wt6 * 128;
    const unsigned voffW2 = mlp_voffw<false>(wid, lane);
    if (do5) {
      if (wid == 0) {
        for (unsigned spins = 0;; ++spins) {
          const unsigned v = __hip_atomic_load(flags + FL_H + 64 * (lane & 7), __ATOMIC_RELAXED, __HIP_MEMORY_SCOPE_AGENT);
          if (__all(v >= 32u)) break;
          if (spins > (1u << 16)) { __hip_atomic_store(flags + FL_TMO, 1u, __ATOMIC_RELAXED, __HIP_MEMORY_SCOPE_AGENT); break; }
          __builtin_amdgcn_s_sleep(1);
        }
      }
      WAIT_V(0); WAIT_L(0); BAR();
      f32x4 acc[4][5];
      { const MlpW wp = mlp_wptr(lds, wid, lane); mlp_main<true, 0, 4, 16, 16>(lds, ws + WS_HNF + (size_t)(5 * dg5) * 1024, wid, lane, wp, wf, acc); }
      BAR();
      mlp_park(lds, wid, lane, acc);
      {
        float v[2][8]; u32x4 bv[2];
#pragma unroll
        for (int k2 = 0; k2 < 2; ++k2) {
          const int it = k2 == 0 ? tid : (tid < 128 ? 512 + tid : tid);
          const int d15 = it & 15, gp = (it >> 4) & 3, fr = it >> 6, dt = fr % 5, Tp = fr / 5;
          bv[k2] = *(const u32x4*)(b1 + 64 * wt5 + 32 * Tp + 8 * gp);
          mlp_gather(lds, 2 * Tp + (gp >> 1), dt, 2 * (gp & 1), d15, v[k2]);
        }
#pragma unroll
        for (int k2 = 0; k2 < 2; ++k2) {
          if (k2 == 0 || tid < 128) {
            const int it = k2 == 0 ? tid : 512 + tid;
            const int d15 = it & 15, gp = (it >> 4) & 3, fr = it >> 6, dt = fr % 5, Tp = fr / 5;
            u32x4 o;
#pragma unroll
            for (int e = 0; e < 4; ++e) {
              float z[2] = {v[k2][2 * e] + bflo(bv[k2][e]), v[k2][2 * e + 1] + bfhi(bv[k2][e])};
#pragma unroll
              for (int k = 0; k < 2; ++k) {
                const float xx = z[k];
                const float a = xx * (1.0f + 0.044715f * xx * xx) * (-2.0f * 0.7978845608028654f * 1.4426950408889634f);
                z[k] = xx * __builtin_amdgcn_rcpf(1.0f + __builtin_amdgcn_exp2f(a));
              }
              o[e] = cvtpk(z[0], z[1]);
            }
            gstore_sc1_u((unsigned)((((2 * wt5 + Tp) * 40 + 5 * dg5 + dt) * 64 + gp * 16 + d15) * 16), o, ws + WS_UF);
          }
        }
      }
      mlp_wstage<false, 20, 8>(lds0, w2b, voffW2, wid, RHI);
      WAIT_V(8); WAIT_L(0); BAR();
      if (tid == 0) __hip_atomic_fetch_add(flags + FL_U + 64 * dg5, 1u, __ATOMIC_RELAXED, __HIP_MEMORY_SCOPE_AGENT);
    } else {
      WAIT_L(0); BAR();
      mlp_wstage<false, 20, 8>(lds0, w2b, voffW2, wid, RHI);
    }
  }
  {
    const int lane = lane_id(), tid = wid * 64 + lane;
    bf16x8 wg[8];
    const MlpW wp = mlp_wptr(lds, wid, lane);
    const char* w2b = (const char*)w2 + wt6 * 128;
    const unsigned voffW2 = mlp_voffw<false>(wid, lane);
    mlp_wstage<false, 0, 10>(lds0, w2b, voffW2, wid, 0);
    WAIT_V(10); BAR();
    mlp_wrd(wp, RHI, wg); mlp_wrd(wp, RHI + 32768, wg + 4);
    WAIT_L(0); BAR();
    mlp_wstage<false, 10, 10>(lds0, w2b, voffW2, wid, RHI);
    if (wid == 0) wait_word(flags + FL_U + 64 * dg6, 27u, flags + FL_TMO);
    WAIT_V(0); BAR();
    const char* ab = ws + WS_UF + (size_t)(5 * dg6) * 1024;
    f32x4 acc[4][5];
    mlp_main<false, 5, 7, 0, 8>(lds, ab, wid, lane, wp, wg, acc);
    u32x4 hv[2]; float bb[2]; size_t oo[2];
#pragma unroll
    for (int k2 = 0; k2 < 2; ++k2) {
      const int it = k2 == 0 ? tid : (tid < 128 ? 512 + tid : tid);
      const int sl = it / 10, c = it % 10, srow = 64 * wt6 + sl;
      oo[k2] = (size_t)srow * DM + 80 * dg6 + 8 * c;
      GLOAD1_SC1(hv[k2], (unsigned)(oo[k2] * 2), ws + WS_H, 0);
      bb[k2] = bflo((unsigned)b2[srow]);
    }
    BAR();
    mlp_park(lds, wid, lane, acc);
    float v[2][8];
#pragma unroll
    for (int k2 = 0; k2 < 2; ++k2) {
      const int it = k2 == 0 ? tid : (tid < 128 ? 512 + tid : tid);
      const int sl = it / 10, c = it % 10;
      mlp_gather(lds, sl >> 4, c >> 1, 2 * (c & 1), sl & 15, v[k2]);
    }
    asm volatile("s_waitcnt vmcnt(0)" : "+v"(hv[0]), "+v"(hv[1]) :: "memory");
#pragma unroll
    for (int k2 = 0; k2 < 2; ++k2) {
      if (k2 == 0 || tid < 128) {
        u32x4 ov;
#pragma unroll
        for (int e = 0; e < 4; ++e) ov[e] = cvtpk(v[k2][2 * e] + bb[k2] + bflo(hv[k2][e]), v[k2][2 * e + 1] + bb[k2] + bfhi(hv[k2][e]));
        *(u32x4*)(out + oo[k2]) = ov;
      }
    }
  }
}

constexpr int CODE_BYTES = 90112;
constexpr int qproj_ns(int c, int cls) {
  constexpr int tab[4][10] = {{3, 3, 3, 3, 2, 2, 0, 0, 0, 0}, {1, 2, 2, 2, 3, 3, 3, 0, 0, 0}, {0, 1, 1, 2, 2, 2, 2, 3, 3, 0}, {0, 0, 1, 1, 1, 2, 2, 3, 3, 3}};
  return (c < 0 || c > 9) ? 0 : tab[cls][c];
}
constexpr int qproj_sbase(int c, int cls) { int n = 0; for (int i = 0; i < c; ++i) n += qproj_ns(i, cls); return n; }
constexpr int qproj_nl(int c) { return (c + 2 < 10 ? 4 : 0) + (c + 3 < 10 ? 2 : 0); }
constexpr int qproj_allowed(int c, int cls) {
  return c == 0 ? 6 : (c == 1 ? qproj_nl(0) + qproj_ns(0, cls) : qproj_ns(c - 2, cls) + qproj_nl(c - 1) + qproj_ns(c - 1, cls));
}
__global__ __launch_bounds__(512, 2) void kF1(const u16* __restrict__ ckv, const u16* __restrict__ wuk, const u16* __restrict__ wuv, const u16* __restrict__ wq,
                                              const u16* __restrict__ wo, const u16* __restrict__ w1, const u16* __restrict__ w2,
                                              const u16* __restrict__ x, const u16* __restrict__ b1, const u16* __restrict__ b2, char* __restrict__ ws, u16* __restrict__ out) {
  extern __shared__ __attribute__((aligned(16))) unsigned char smem[];
  const int bidx = blockIdx.x, xcd = bidx & 7, rr_ = bidx >> 3;
  const int h = xcd * 4 + (rr_ >> 3), j = rr_ & 7;
  const int wid = rfl(threadIdx.x >> 6);
  ldsp lds = (ldsp)smem;
  const unsigned lds0 = (unsigned)(uintptr_t)smem;
  unsigned* flags = (unsigned*)(ws + WS_FLAGS);
  const char* code0 = (const char*)__builtin_amdgcn_s_getpc();
  unsigned cpf = 0;
#define CODE_TOUCH() do { const unsigned ln_ = (unsigned)rr_ * 32u + (threadIdx.x & 63u); if (wid == 7 && (threadIdx.x & 63u) < 32u && ln_ * 128u < (unsigned)CODE_BYTES) \
    asm volatile("global_load_dword %0, %1, off" : "+v"(cpf) : "v"(code0 + (size_t)ln_ * 128) : "memory"); } while (0)
  CODE_TOUCH();
  bf16x8 kvp[16];
  {
    const int wr = wid >> 2, wc = wid & 3;
    const int lane = threadIdx.x & 63, l31 = lane & 31, hh = lane >> 5;
    unsigned voffC, voffW;
    { const int row = 8 * wid + (lane >> 3); voffC = (unsigned)(row * 1024 + (((lane & 7) ^ ((row >> 1) & 7)) << 4)); }
    { const int row = 8 * (wid >> 1) + ((lane >> 2) & 7); const int chq = 2 * (wid & 1) + (lane >> 5);
      const int ch = 4 * chq + ((lane & 3) ^ ((row >> 2) & 3)); voffW = (unsigned)(row * 8192 + ch * 16); }
    const char* cbase = (const char*)ckv + (size_t)(256 * j) * 1024;
    const char* wkb = (const char*)wuk + h * 256;
    const char* wvb = (const char*)wuv + h * 256;
    const unsigned dstw = lds0 + wid * 1024;
#define CBUF(b, a) (((b) * 2 + (a)) * 16384)
#define WBUF(b, a) (65536 + ((b) * 2 + (a)) * 16384)
#define STAGE_C(b, a, kt) do { const char* s_ = cbase + (a) * 131072 + (kt) * 128; glds16(voffC, s_, dstw + CBUF(b, a)); glds16(voffC, s_ + 65536, dstw + CBUF(b, a) + 8192); } while (0)
#define STAGE_W(b, a, kt) do { const char* s_ = ((a) ? wvb : wkb) + (size_t)(kt) * 64 * 8192; glds16(voffW, s_, dstw + WBUF(b, a)); glds16(voffW, s_ + 32 * 8192, dstw + WBUF(b, a) + 8192); } while (0)
    int aoff[4], boff[2];
#pragma unroll
    for (int s = 0; s < 4; ++s) aoff[s] = 128 * (wr * 64 + l31) + (((2 * s + hh) ^ ((l31 >> 1) & 7)) << 4);
    { const int blk = (lane >> 4) & 1, q = (lane & 15) >> 2, p = lane & 3;
#pragma unroll
      for (int t = 0; t < 2; ++t) boff[t] = 2048 * hh + 512 * wc + 64 * (4 * t + q) + (((2 * blk + (p >> 1)) ^ ((2 * hh + t) & 3)) << 4) + 8 * (p & 1); }
#define LDA(dst, b, a) do { _Pragma("unroll") for (int m = 0; m < 2; ++m) _Pragma("unroll") for (int s = 0; s < 4; ++s) dst[m][s] = ldsr128(lds + CBUF(b, a) + m * 4096 + aoff[s]); } while (0)
#define LDB(dst, b, a) do { _Pragma("unroll") for (int s = 0; s < 4; ++s) dst[s] = cat8(vtr(lds + WBUF(b, a) + s * 4096 + boff[0]), vtr(lds + WBUF(b, a) + s * 4096 + boff[1])); } while (0)
#define MMAK(ai, At, Bt) do { __builtin_amdgcn_s_setprio(1); _Pragma("unroll") for (int s = 0; s < 4; ++s) _Pragma("unroll") for (int m = 0; m < 2; ++m) \
    accK[ai][m] = MFMA32(Bt[s], At[m][s], accK[ai][m]); __builtin_amdgcn_s_setprio(0); } while (0)
#define MMAV(ai, At, Bt) do { __builtin_amdgcn_s_setprio(1); _Pragma("unroll") for (int s = 0; s < 4; ++s) _Pragma("unroll") for (int m = 0; m < 2; ++m) \
    accV[ai][m] = MFMA32(At[m][s], Bt[s], accV[ai][m]); __builtin_amdgcn_s_setprio(0); } while (0)
    f32x16 accK[2][2] = {}, accV[2][2] = {};
    bf16x8 At[2][4], B0[4], B1[4];
    constexpr int nt = DC / 64;
    STAGE_W(0, 0, 0); STAGE_C(0, 0, 0); STAGE_W(0, 1, 0); STAGE_C(0, 1, 0);
    if (wr == 1) BAR();
    WAIT_V(4); BAR();
    STAGE_W(1, 0, 1); STAGE_C(1, 0, 1); STAGE_W(1, 1, 1);
    WAIT_V(6); BAR();
    unsigned pfd = 0;
    if (wid < 3) {
      const int ln = wid * 64 + lane;
      if (ln < 160) asm volatile("global_load_dword %0, %1, off" : "+v"(pfd) : "v"((const char*)wq + (size_t)(80 * j + (ln >> 1)) * 8192 + h * 256 + (ln & 1) * 128) : "memory");
    }
    for (int t = 0; t < nt - 2; t += 2) {
      LDB(B0, 0, 0); SBAR(); LDA(At, 0, 0); STAGE_C(1, 1, t + 1);
      WAIT_L(8); BAR(); WAIT_L(0); MMAK(0, At, B0); BAR(); SBAR();
      LDB(B1, 0, 1); STAGE_W(0, 0, t + 2);
      BAR(); WAIT_L(0); MMAV(0, At, B1); BAR();
      LDA(At, 0, 1); STAGE_C(0, 0, t + 2);
      BAR(); WAIT_L(0); MMAK(1, At, B0); BAR(); SBAR();
      STAGE_W(0, 1, t + 2);
      WAIT_V(6); BAR(); MMAV(1, At, B1); BAR();
      LDB(B0, 1, 0); SBAR(); LDA(At, 1, 0); STAGE_C(0, 1, t + 2);
      WAIT_L(8); BAR(); WAIT_L(0); MMAK(0, At, B0); BAR(); SBAR();
      LDB(B1, 1, 1); STAGE_W(1, 0, t + 3);
      BAR(); WAIT_L(0); MMAV(0, At, B1); BAR();
      LDA(At, 1, 1); STAGE_C(1, 0, t + 3);
      BAR(); WAIT_L(0); MMAK(1, At, B0); BAR(); SBAR();
      STAGE_W(1, 1, t + 3);
      WAIT_V(6); BAR(); MMAV(1, At, B1); BAR();
    }
    {
      const char* wqb_ = (const char*)wq + h * 256;
      glds16_c<0>(voffW, wqb_, lds0 + 131072 + wid * 1024); glds16_c<32 * 8192>(voffW, wqb_, lds0 + 131072 + 8192 + wid * 1024);
      glds16_c<64 * 8192>(voffW, wqb_, lds0 + 147456 + wid * 1024); glds16_c<96 * 8192>(voffW, wqb_, lds0 + 147456 + 8192 + wid * 1024); }
    { LDB(B0, 0, 0); WAIT_V(0); LDA(At, 0, 0); STAGE_C(1, 1, nt - 1);
      BAR(); WAIT_L(0); MMAK(0, At, B0); BAR();
      LDB(B1, 0, 1); BAR(); WAIT_L(0); MMAV(0, At, B1); BAR();
      LDA(At, 0, 1); WAIT_V(4); BAR(); WAIT_L(0); MMAK(1, At, B0); MMAV(1, At, B1); BAR(); }
    { LDB(B0, 1, 0); LDA(At, 1, 0); WAIT_V(2); BAR(); WAIT_L(0); MMAK(0, At, B0); BAR();
      LDB(B1, 1, 1); WAIT_V(0); BAR(); WAIT_L(0); MMAV(0, At, B1); BAR();
      LDA(At, 1, 1); BAR(); WAIT_L(0); MMAK(1, At, B0); MMAV(1, At, B1); BAR(); }
    if (wr == 0) BAR();
    asm volatile("" :: "v"(pfd), "v"(cpf));
#pragma unroll
    for (int ai = 0; ai < 2; ++ai)
#pragma unroll
      for (int m = 0; m < 2; ++m)
#pragma unroll
        for (int sp = 0; sp < 2; ++sp) {
          kvp[((ai * 2 + m) * 2 + sp) * 2] = pack8(accK[ai][m], sp);
          kvp[((ai * 2 + m) * 2 + sp) * 2 + 1] = pack8(accV[ai][m], sp);
          PIN(kvp[((ai * 2 + m) * 2 + sp) * 2]); PIN(kvp[((ai * 2 + m) * 2 + sp) * 2 + 1]);
        }
#undef CBUF
#undef WBUF
#undef STAGE_C
#undef STAGE_W
#undef LDA
#undef LDB
#undef MMAK
#undef MMAV
  }
  SBAR();
  bf16x8 qr[8];
  unsigned kvmask = 0;
  const int lane = lane_id(), tid = wid * 64 + lane;
  const unsigned vl16 = lane * 16;
  {
    const int hh = lane >> 5;
    const char* Kh = ws + WS_KF + (size_t)h * 64 * 8192;
    const char* Vh = ws + WS_VF + (size_t)h * 128 * 4096;
    const unsigned vt2 = (unsigned)(wid * 2048 + lane * 16);
    const unsigned dt2 = lds0 + wid * 2048;
    const int n1 = j >= 4 ? 16 : 4 * j + 2;
#define TILE(g) ((g) < n1 ? (g) : 16 + (g) - n1)
#define DMA_T(src, ldsoff) do { unsigned keep_; asm volatile("s_mov_b32 %0, m0\n\ts_mov_b32 m0, %3\n\ts_nop 2\n\tglobal_load_lds_dwordx4 %1, %2 sc1\n\tglobal_load_lds_dwordx4 %1, %2 offset:1024 sc1\n\ts_mov_b32 m0, %0" \
    : "=&s"(keep_) : "v"(vt2), "s"(src), "s"(dt2 + (ldsoff)) : "memory"); } while (0)
    const int wr = wid >> 2, wc = wid & 3, cls = j & 3;
    const char* kst = ws + WS_KF + ((size_t)(h * 64 + 8 * j + 2 * wr) * 8 + 2 * wc) * 1024;
    const char* vst = ws + WS_VF + ((size_t)(h * 128 + 2 * (8 * j + 2 * wr)) * 4 + wc) * 1024;
#define KVST(i) gstore_sc1_c<(((i) & 1) ? (((i) >> 3) * 8 + (((i) >> 2) & 1) * 2 + (((i) >> 1) & 1)) * 4096 : (((i) >> 3) * 4 + (((i) >> 2) & 1)) * 8192 + (((i) >> 1) & 1) * 1024)>(vl16, kvp[i], ((i) & 1) ? vst : kst)
    unsigned voffW;
    { const int row = 8 * (wid >> 1) + ((lane >> 2) & 7); const int chq = 2 * (wid & 1) + (lane >> 5);
      const int ch = 4 * chq + ((lane & 3) ^ ((row >> 2) & 3)); voffW = (unsigned)(row * 8192 + ch * 16); }
    const char* wqb = (const char*)wq + h * 256;
    const unsigned dstw = lds0 + L_Q + wid * 1024;
#define STAGE_Q1(c, i) glds16_c<(c) * 64 * 8192 + (i) * 32 * 8192>(voffW, wqb, dstw + (((c) + 2) & 3) * 16384 + (i) * 8192)
    const char* xb = ws + WS_XNF + (size_t)(8 * j + wid) * 40 * 1024;
    int boff[2];
    { const int blk = (lane >> 4) & 1, q = (lane & 15) >> 2, p = lane & 3;
#pragma unroll
      for (int t = 0; t < 2; ++t) boff[t] = L_Q + 2048 * hh + 64 * (4 * t + q) + (((2 * blk + (p >> 1)) ^ ((2 * hh + t) & 3)) << 4) + 8 * (p & 1); }
    u32x4 xf[3][4];
    f32x16 acc[4] = {};
    bf16x8 aq[6];
    unsigned fv = 0;
    ldscp bp0 = lds + boff[0], bp1 = lds + boff[1];
#define ARD(u) aq[(u) % 6] = cat8(vtr(bp0 + ((((u) >> 4) + 2) & 3) * 16384 + (((u) >> 2) & 3) * 4096 + ((u) & 3) * 512), vtr(bp1 + ((((u) >> 4) + 2) & 3) * 16384 + (((u) >> 2) & 3) * 4096 + ((u) & 3) * 512))
    GLOAD4(xf[0], vl16, xb);
    GLOAD4(xf[1], vl16, xb + 4096); STAGE_Q1(2, 0); STAGE_Q1(2, 1);
#pragma unroll
    for (int u = 0; u < 5; ++u) ARD(u);
    static_for<10>([&](auto cc_) {
      constexpr int c = decltype(cc_)::value;
#define QW(K) WAITVN(qproj_allowed(c, K))
      if (cls == 0) QW(0); else if (cls == 1) QW(1); else if (cls == 2) QW(2); else QW(3);
      asm volatile("" : "+v"(xf[c % 3][0]), "+v"(xf[c % 3][1]), "+v"(xf[c % 3][2]), "+v"(xf[c % 3][3]) :: "memory");
#undef QW
      if (c == 9 && wid == 0) {
        asm volatile("s_waitcnt vmcnt(0)" : "+v"(fv) :: "memory");
        kvmask = (unsigned)__builtin_amdgcn_ballot_w64(fv != 0u) & 0xffu;
        if (!(kvmask & 1u)) { wait_word(flags + FL_KV + 16 * (8 * h), 1u, flags + FL_TMO); kvmask |= 1u; }
        if (j == 0 && !(kvmask & 16u)) { wait_word(flags + FL_KV + 16 * (8 * h + 4), 1u, flags + FL_TMO); kvmask |= 16u; }
      }
      SBAR(); BAR();
      if (tid == 0 && ((cls == 0 && c == 8) || (cls == 1 && c == 9)))
        __hip_atomic_store(flags + FL_KV + 16 * (8 * h + j), 1u, __ATOMIC_RELAXED, __HIP_MEMORY_SCOPE_AGENT);
      SBAR();
      const char* xn_ = xb + (c + 2) * 4096;
      static_for<16>([&](auto ii_) {
        constexpr int i = decltype(ii_)::value;
        constexpr int u = 16 * c + i;
        acc[i & 3] = MFMA32(aq[u % 6], __builtin_bit_cast(bf16x8, xf[c % 3][i >> 2]), acc[i & 3]);
        if constexpr (u + 5 < 160) ARD(u + 5);
        if (c + 2 < 10) {
          if (i == 1) GLOAD1(xf[(c + 2) % 3][0], vl16, xn_, 0);
          if (i == 3) GLOAD1(xf[(c + 2) % 3][1], vl16, xn_, 1024);
          if (i == 5) GLOAD1(xf[(c + 2) % 3][2], vl16, xn_, 2048);
          if (i == 7) GLOAD1(xf[(c + 2) % 3][3], vl16, xn_, 3072);
        }
        if constexpr (c + 3 < 10) { if constexpr (i == 8) STAGE_Q1(c + 3, 0); if constexpr (i == 9) STAGE_Q1(c + 3, 1); }
        if constexpr (i >= 10) {
          static_for<4>([&](auto kk_) {
            constexpr int k = decltype(kk_)::value;
            constexpr int cnt = qproj_ns(c, k), base = qproj_sbase(c, k);
            if constexpr (((i - 10) & 1) == 0 && (i - 10) / 2 < cnt) { if (cls == k) KVST(base + (i - 10) / 2); }
          });
        }
        if (c == 8 && i == 8 && wid == 0)
          asm volatile("s_nop 4\n\tglobal_load_dword %0, %1, %2 sc1" : "+v"(fv) : "v"((unsigned)(FL_KV + 16 * (8 * h + (lane & 7))) * 4u), "s"(flags) : "memory");
        if (c == 9) {
          if (i == 0) DMA_T(Kh + (size_t)TILE(0) * 16384, L_K);
          if (i == 2) DMA_T(Vh + (size_t)TILE(0) * 16384, L_V);
          if (i == 4) DMA_T(Kh + (size_t)TILE(1) * 16384, L_K + 16384);
          if (i == 6) DMA_T(Vh + (size_t)TILE(1) * 16384, L_V + 16384);
          if (i == 8) DMA_T(Kh + (size_t)TILE(2) * 16384, L_K + 32768);
          if (i == 9) DMA_T(Kh + (size_t)TILE(3) * 16384, L_K + 49152);
        }
        SBAR();
      });
    });
#pragma unroll
    for (int dt = 0; dt < 4; ++dt) { acc[dt] = acc[dt] * QSCALE; qr[2 * dt] = pack8(acc[dt], 0); qr[2 * dt + 1] = pack8(acc[dt], 1); }
    if (cls == 3) WAIT_V(13); else WAIT_V(10);
    BAR();
    { const char* qst = ws + WS_QF + (size_t)(h * 64 + 8 * j + wid) * 8192;
      static_for<8>([&](auto ss_) { constexpr int s = decltype(ss_)::value; gstore_sc1_c<s * 1024>(vl16, qr[s], qst); }); }
#undef KVST
#undef STAGE_Q1
#undef ARD
#undef TILE
#undef DMA_T
  }
  const PfW pfw = {(const char*)wo, (const char*)w1, (const char*)w2};
  if (wid < 4) attn_main<0>(lds, lds0, ws, flags, h, j, wid, lane, tid, qr, kvmask, pfw);
  else attn_main<1>(lds, lds0, ws, flags, h, j, wid, lane, tid, qr, kvmask, pfw);
  WAIT_V(0); WAIT_L(0); BAR();
  if (tid == 0) __hip_atomic_fetch_add(flags + FL_C + 16 * ((7 - j) * 4 + (h >> 3)), 1u, __ATOMIC_RELAXED, __HIP_MEMORY_SCOPE_AGENT);
  tail_phase(smem, x, wo, w1, b1, w2, b2, ws, out, wid);
}

extern "C" void kernel_launch(void* const* d_in, const int* in_sizes, int n_in, void* d_out, int out_size, void* d_ws, size_t ws_size,
                              hipStream_t stream) {
  const u16* x = (const u16*)d_in[0];
  const u16* ckv = (const u16*)d_in[1];
  const u16* wuk = (const u16*)d_in[3];
  const u16* wuv = (const u16*)d_in[4];
  char* ws = (char*)d_ws;
  const u16* wq = (const u16*)d_in[2];
  const u16* wo = (const u16*)d_in[5];
  const u16* w1 = (const u16*)d_in[7];
  const u16* b1 = (const u16*)d_in[8];
  const u16* w2 = (const u16*)d_in[9];
  const u16* b2 = (const u16*)d_in[10];
  (void)hipFuncSetAttribute((const void*)kF1, hipFuncAttributeMaxDynamicSharedMemorySize, F1_LDS);
  hipLaunchKernelGGL(k0_ln, dim3(256), dim3(512), 0, stream, x, (u16*)(ws + WS_XNF), (unsigned*)(ws + WS_FLAGS));
  hipLaunchKernelGGL(kF1, dim3(256), dim3(512), F1_LDS, stream, ckv, wuk, wuv, wq, wo, w1, w2, x, b1, b2, ws, (u16*)d_out);
}
```

```cpp
#include <hip/hip_runtime.h>
#include <stdint.h>

#define LAS __attribute__((address_space(3)))
#define DI __device__ __forceinline__
typedef unsigned short u16;
typedef short bf16x8 __attribute__((ext_vector_type(8)));
typedef short s16x4 __attribute__((ext_vector_type(4)));
typedef float f32x2 __attribute__((ext_vector_type(2)));
typedef float f32x4 __attribute__((ext_vector_type(4)));
typedef float f32x16 __attribute__((ext_vector_type(16)));
typedef unsigned u32x2 __attribute__((ext_vector_type(2)));
typedef unsigned u32x4 __attribute__((ext_vector_type(4)));
typedef __bf16 bf16x2_t __attribute__((ext_vector_type(2)));
typedef LAS unsigned char* ldsp;
typedef LAS const unsigned char* ldscp;

constexpr int SEQ = 2048, DM = 640, DC = 512, NH = 32, HD = 128, FF = 1728;
constexpr float LN_EPS = 1e-5f;

constexpr size_t WS_FLAGS = 0;
constexpr size_t WS_XNF = 131072;
constexpr size_t WS_KF = WS_XNF + (size_t)SEQ * DM * 2;
constexpr size_t WS_VF = WS_KF + (size_t)NH * SEQ * HD * 2;
constexpr size_t WS_CTX = WS_VF + (size_t)NH * SEQ * HD * 2;
constexpr size_t WS_SLAB = WS_CTX + (size_t)NH * SEQ * HD * 2;
constexpr size_t WS_H = WS_SLAB + (size_t)4 * SEQ * DM * 4;
constexpr size_t WS_HNF = WS_H + (size_t)SEQ * DM * 2;
constexpr size_t WS_UF = WS_HNF + (size_t)SEQ * DM * 2;
constexpr size_t WS_QF = WS_UF + (size_t)DM * FF * 2;
constexpr size_t WS_PF = WS_QF + (size_t)NH * SEQ * HD * 2;
constexpr size_t WS_PS = WS_PF + (size_t)NH * SEQ * HD * 2;
constexpr size_t WS_END = WS_PS + (size_t)NH * 8 * 8 * 64 * 8;
constexpr int FL_TMO = 0;
constexpr int FL_KV = 24576;
constexpr int FL_Q = 4096;
constexpr int FL_P = 4096 + 8192;
constexpr int FL_WORDS = 32768;

#define SBAR() __builtin_amdgcn_sched_barrier(0)
#define BAR() asm volatile("s_barrier" ::: "memory")
#define WAIT_V(n) asm volatile("s_waitcnt vmcnt(" #n ")" ::: "memory")
#define WAIT_L(n) asm volatile("s_waitcnt lgkmcnt(" #n ")" ::: "memory")
#define MFMA32(a, b, c) __builtin_amdgcn_mfma_f32_32x32x16_bf16((a), (b), (c), 0, 0, 0)
#define MFMA16(a, b, c) __builtin_amdgcn_mfma_f32_16x16x32_bf16((a), (b), (c), 0, 0, 0)

DI unsigned cvtpk(float lo, float hi) { f32x2 v = {lo, hi}; bf16x2_t b = __builtin_convertvector(v, bf16x2_t); return __builtin_bit_cast(unsigned, b); }
DI float bflo(unsigned u) { return __uint_as_float(u << 16); }
DI float bfhi(unsigned u) { return __uint_as_float(u & 0xffff0000u); }
DI unsigned rfl(unsigned x) { return __builtin_amdgcn_readfirstlane(x); }
DI void glds16(unsigned voff, const void* sbase, unsigned lds_dst) {
  unsigned keep;
  asm volatile("s_mov_b32 %0, m0\n\ts_mov_b32 m0, %3\n\ts_nop 2\n\tglobal_load_lds_dwordx4 %1, %2\n\ts_mov_b32 m0, %0"
               : "=&s"(keep) : "v"(voff), "s"(sbase), "s"(lds_dst) : "memory");
}
DI void glds16x4(unsigned voff, const void* sbase, unsigned lds_dst) {
  unsigned keep;
  asm volatile("s_mov_b32 %0, m0\n\ts_mov_b32 m0, %3\n\ts_nop 2\n\tglobal_load_lds_dwordx4 %1, %2\n\tglobal_load_lds_dwordx4 %1, %2 offset:1024\n\t"
               "global_load_lds_dwordx4 %1, %2 offset:2048\n\tglobal_load_lds_dwordx4 %1, %2 offset:3072\n\ts_mov_b32 m0, %0"
               : "=&s"(keep) : "v"(voff), "s"(sbase), "s"(lds_dst) : "memory");
}
typedef short v4i16_t __attribute__((ext_vector_type(4)));
DI s16x4 vtr(ldscp p) { return __builtin_bit_cast(s16x4, __builtin_amdgcn_ds_read_tr16_b64_v4i16((LAS v4i16_t*)p)); }
DI bf16x8 cat8(s16x4 lo, s16x4 hi) { return __builtin_shufflevector(lo, hi, 0, 1, 2, 3, 4, 5, 6, 7); }
DI bf16x8 ldsr128(ldscp p) { return *(const LAS bf16x8*)p; }
DI bf16x8 pack8(const f32x16& x, int s) {
  u32x4 w = {cvtpk(x[8 * s], x[8 * s + 1]), cvtpk(x[8 * s + 2], x[8 * s + 3]), cvtpk(x[8 * s + 4], x[8 * s + 5]), cvtpk(x[8 * s + 6], x[8 * s + 7])};
  return __builtin_bit_cast(bf16x8, w);
}
DI float wave_sum(float v) {
#pragma unroll
  for (int o = 32; o >= 1; o >>= 1) v += __shfl_xor(v, o);
  return v;
}

constexpr int K0_TOUCH_BYTES = 106496;
__global__ __launch_bounds__(512) void k0_ln(const u16* __restrict__ x, u16* __restrict__ xnf, unsigned* __restrict__ flags) {
  const int w = threadIdx.x >> 6, lane = threadIdx.x & 63;
  const int q = blockIdx.x * 8 + w;
  unsigned cpf = 0;
  { const unsigned ln = (blockIdx.x >> 3) * 32u + (unsigned)lane;
    if (w == 0 && lane < 32 && ln * 128u < (unsigned)K0_TOUCH_BYTES)
      asm volatile("global_load_dword %0, %1, off" : "+v"(cpf) : "v"((const char*)__builtin_amdgcn_s_getpc() + (size_t)ln * 128) : "memory"); }
  if (threadIdx.x < 128) flags[blockIdx.x * 128 + threadIdx.x] = 0u;
  const u32x4* row = (const u32x4*)(x + (size_t)q * DM);
  u32x4 c0 = row[lane];
  u32x4 c1 = {0u, 0u, 0u, 0u};
  if (lane < 16) c1 = row[64 + lane];
  float v[16];
#pragma unroll
  for (int i = 0; i < 4; ++i) { v[2 * i] = bflo(c0[i]); v[2 * i + 1] = bfhi(c0[i]); v[8 + 2 * i] = bflo(c1[i]); v[8 + 2 * i + 1] = bfhi(c1[i]); }
  float s = 0.f;
#pragma unroll
  for (int i = 0; i < 16; ++i) s += v[i];
  const float mean = wave_sum(s) * (1.0f / DM);
  float ss = 0.f;
#pragma unroll
  for (int i = 0; i < 8; ++i) { const float d = v[i] - mean; ss += d * d; }
  if (lane < 16) {
#pragma unroll
    for (int i = 8; i < 16; ++i) { const float d = v[i] - mean; ss += d * d; }
  }
  const float rstd = 1.0f / sqrtf(wave_sum(ss) * (1.0f / DM) + LN_EPS);
  u32x4 o0, o1;
#pragma unroll
  for (int i = 0; i < 4; ++i) { o0[i] = cvtpk((v[2 * i] - mean) * rstd, (v[2 * i + 1] - mean) * rstd); o1[i] = cvtpk((v[8 + 2 * i] - mean) * rstd, (v[8 + 2 * i + 1] - mean) * rstd); }
  u32x4* dst = (u32x4*)xnf + (size_t)(q >> 5) * 40 * 64 + (q & 31);
  dst[(lane >> 1) * 64 + (lane & 1) * 32] = o0;
  if (lane < 16) dst[((64 + lane) >> 1) * 64 + (lane & 1) * 32] = o1;
  asm volatile("s_waitcnt vmcnt(0)" : "+v"(cpf) :: "memory");
}

constexpr int F1_LDS = 163840;
constexpr int L_K = 0, L_V = 65536, L_Q = 98304;
constexpr float QSCALE = 0.08838834764831845f * 1.4426950408889634f;
constexpr float THR = 16.f;
#define PIN(x) asm volatile("" : "+v"(x))
#define EX2(v) __builtin_amdgcn_exp2f(v)
#define MEMFENCE() asm volatile("" ::: "memory")
DI float max3f(float a, float b, float c) { return __builtin_fmaxf(__builtin_fmaxf(a, b), c); }
DI float xhalf_max(float m) { auto rr = __builtin_amdgcn_permlane32_swap(__float_as_uint(m), __float_as_uint(m), false, false); return __builtin_fmaxf(__uint_as_float(rr[0]), __uint_as_float(rr[1])); }
DI float xhalf_sum(float m) { auto rr = __builtin_amdgcn_permlane32_swap(__float_as_uint(m), __float_as_uint(m), false, false); return __uint_as_float(rr[0]) + __uint_as_float(rr[1]); }
DI float max16(const f32x16& p) {
  float a = max3f(p[0], p[1], p[2]), b = max3f(p[3], p[4], p[5]);
  a = max3f(a, p[6], p[7]); b = max3f(b, p[8], p[9]); a = max3f(a, p[10], p[11]); b = max3f(b, p[12], p[13]); a = max3f(a, p[14], p[15]);
  return __builtin_fmaxf(a, b);
}
DI void cmask(f32x16& p0, f32x16& p1, int dq) {
#pragma unroll
  for (int r = 0; r < 16; ++r) { const int c = (r & 3) + 8 * (r >> 2); if (c > dq) p0[r] = -__builtin_inff(); if (c + 32 > dq) p1[r] = -__builtin_inff(); }
}
#define GLOAD4(d, voff, sbase) asm volatile("s_nop 4\n\tglobal_load_dwordx4 %0, %4, %5\n\tglobal_load_dwordx4 %1, %4, %5 offset:1024\n\tglobal_load_dwordx4 %2, %4, %5 offset:2048\n\tglobal_load_dwordx4 %3, %4, %5 offset:3072" \
    : "=&v"(d[0]), "=&v"(d[1]), "=&v"(d[2]), "=&v"(d[3]) : "v"(voff), "s"(sbase) : "memory")
#define GLOAD1(d, voff, sbase, OFF) asm volatile("s_nop 4\n\tglobal_load_dwordx4 %0, %1, %2 offset:" #OFF : "=&v"(d) : "v"(voff), "s"(sbase) : "memory")
#define GLOAD1_SC1(d, voff, sbase, OFF) asm volatile("s_nop 4\n\tglobal_load_dwordx4 %0, %1, %2 offset:" #OFF " sc1" : "=&v"(d) : "v"(voff), "s"(sbase) : "memory")
#define WAITV_TIE4(n, d) asm volatile("s_waitcnt vmcnt(" #n ")" : "+v"(d[0]), "+v"(d[1]), "+v"(d[2]), "+v"(d[3]) :: "memory")
DI void gstore_sc1(unsigned voff, const bf16x8& v, const void* sbase) {
  asm volatile("s_nop 4\n\tglobal_store_dwordx4 %0, %1, %2 sc1\n\ts_nop 1" :: "v"(voff), "v"(v), "s"(sbase) : "memory");
}
template <int OFFS> DI void gstore_sc1_c(unsigned voff, const bf16x8& v, const void* sbase) {
  unsigned t;
  asm volatile("v_add_u32 %0, %4, %1\n\ts_nop 3\n\tglobal_store_dwordx4 %0, %2, %3 sc1\n\ts_nop 1" : "=&v"(t) : "v"(voff), "v"(v), "s"(sbase), "i"(OFFS) : "memory");
}
template <int OFFS> DI void glds16_c(unsigned voff, const void* sbase, unsigned lds_dst) {
  unsigned keep, t;
  asm volatile("s_mov_b32 %0, m0\n\ts_mov_b32 m0, %4\n\tv_add_u32 %1, %5, %2\n\ts_nop 1\n\tglobal_load_lds_dwordx4 %1, %3\n\ts_mov_b32 m0, %0"
               : "=&s"(keep), "=&v"(t) : "v"(voff), "s"(sbase), "s"(lds_dst), "i"(OFFS) : "memory");
}
#define GLDS_OFF(voff, sbase, ldst, OFF) do { unsigned keep_; asm volatile("s_mov_b32 %0, m0\n\ts_mov_b32 m0, %3\n\ts_nop 2\n\tglobal_load_lds_dwordx4 %1, %2 offset:" #OFF "\n\ts_mov_b32 m0, %0" \
    : "=&s"(keep_) : "v"(voff), "s"(sbase), "s"(ldst) : "memory"); } while (0)
#define WAITVN(n) asm volatile("s_waitcnt vmcnt(%0)" :: "i"(n) : "memory")
DI const char* upt(const char* p) {
  const unsigned long long v = (unsigned long long)p;
  return (const char*)(((unsigned long long)rfl((unsigned)(v >> 32)) << 32) | rfl((unsigned)v));
}
DI void wait_word(unsigned* word, unsigned want, unsigned* tmo) {
  for (unsigned spins = 0;; ++spins) {
    if ((unsigned)rfl(__hip_atomic_load(word, __ATOMIC_RELAXED, __HIP_MEMORY_SCOPE_AGENT)) >= want) break;
    if (spins > (1u << 16)) { __hip_atomic_store(tmo, 1u, __ATOMIC_RELAXED, __HIP_MEMORY_SCOPE_AGENT); break; }
    __builtin_amdgcn_s_sleep(1);
  }
}
#define ACQUIRE() __builtin_amdgcn_fence(__ATOMIC_ACQUIRE, "agent")

template <int V> struct IC { static constexpr int value = V; };
template <int N, int I = 0, class F> DI void static_for(F&& f) { if constexpr (I < N) { f(IC<I>{}); static_for<N, I + 1>(f); } }
DI int lane_id() { unsigned l; asm volatile("v_mbcnt_lo_u32_b32 %0, -1, 0\n\tv_mbcnt_hi_u32_b32 %0, -1, %0" : "=v"(l)); return (int)l; }
struct PfW { const char* wo; const char* w1; const char* w2; };
template <int GRP>
DI void attn_main(ldsp lds, unsigned lds0, char* __restrict__ ws, unsigned* flags, int h, int j, int wid, int lane, int tid, bf16x8 (&qr)[8], unsigned kvmask, const PfW& pfw) {
  const int l31 = lane & 31, hh = lane >> 5;
  const unsigned vl16 = lane * 16;
  const char* Kh = ws + WS_KF + (size_t)h * 64 * 8192;
  const char* Vh = ws + WS_VF + (size_t)h * 128 * 4096;
  const unsigned vt2 = (unsigned)(wid * 2048 + lane * 16);
  const unsigned dt2 = lds0 + wid * 2048;
  const int n1 = j >= 4 ? 16 : 4 * j + 2;
  const int t2 = j >= 4 ? 4 * (8 - j) - 2 : 16;
  const int jp = 7 - j;
#define TILE(g) ((g) < n1 ? (g) : ((g) < 18 ? t2 + (g) - n1 : 0))
#define KSRC(g) (Kh + (size_t)TILE(g) * 16384)
#define VSRC(g) (Vh + (size_t)TILE(g) * 16384)
#define GLDS_T(src, dst, OFF) do { unsigned keep_; asm volatile("s_mov_b32 %0, m0\n\ts_mov_b32 m0, %3\n\ts_nop 2\n\tglobal_load_lds_dwordx4 %1, %2 offset:" #OFF " sc1\n\ts_mov_b32 m0, %0" \
    : "=&s"(keep_) : "v"(vt2), "s"(src), "s"(dst) : "memory"); } while (0)
  const unsigned stoff = lane < 8 ? (unsigned)(FL_KV + 16 * (8 * h + lane)) * 4u : (lane == 8 ? (unsigned)(FL_Q + 32 * (8 * h + jp)) * 4u : (unsigned)(FL_P + 32 * (8 * h + jp)) * 4u);
  unsigned fv = 0;
#define ST_ISSUE() do { if (wid == 0 && kvmask != 0x3ffu) asm volatile("s_nop 4\n\tglobal_load_dword %0, %1, %2 sc1" : "+v"(fv) : "v"(stoff), "s"(flags) : "memory"); } while (0)
#define ST_READ(N) do { if (wid == 0 && kvmask != 0x3ffu) { asm volatile("s_waitcnt vmcnt(" #N ")" : "+v"(fv) :: "memory"); kvmask |= (unsigned)__builtin_amdgcn_ballot_w64(fv != 0u) & 0x3ffu; } } while (0)
#define ENSURE_KV(g) do { if (wid == 0) { const int bk_ = TILE(g) >> 2; if (!((kvmask >> bk_) & 1u)) { wait_word(flags + FL_KV + 16 * (8 * h + bk_), 1u, flags + FL_TMO); kvmask |= 1u << bk_; } } } while (0)
  const int rdoff = lane * 16;
  bf16x8 kq[4], vq[4], pw[4];
  f32x16 C0, C1, o[4], negm;
  float mhat, lsum;
  const f32x16 z = {};
  unsigned pfd = 0;
#pragma unroll
  for (int i = 0; i < 4; ++i) kq[i] = ldsr128(lds + L_K + rdoff + i * 1024);
  {
    ST_ISSUE();
    ldscp kb = lds + L_K + rdoff;
#pragma unroll
    for (int u = 0; u < 16; ++u) {
      if (u < 8) C0 = MFMA32(kq[u & 3], qr[u & 7], u == 0 ? z : C0); else C1 = MFMA32(kq[u & 3], qr[u & 7], u == 8 ? z : C1);
      if (u + 4 < 16) kq[u & 3] = ldsr128(kb + (u + 4) * 1024);
      if (GRP == 1 && u == 11) {
        SBAR(); WAIT_V(0); WAIT_L(0); BAR();
        GLDS_T(VSRC(2), dt2 + L_V + 32768, 0); GLDS_T(VSRC(2), dt2 + L_V + 32768, 1024);
        GLDS_T(KSRC(4), dt2 + L_K, 0);
      }
      SBAR();
    }
    if (j == 0) cmask(C0, C1, 32 * wid + l31 - 4 * hh);
    float rm = xhalf_max(__builtin_fmaxf(max16(C0), max16(C1)));
    mhat = __builtin_fmaxf(rm, -1e30f);
#pragma unroll
    for (int r = 0; r < 16; ++r) { C0[r] -= mhat; C1[r] -= mhat; negm[r] = -mhat; }
    PIN(negm);
    if (GRP == 0) {
      SBAR(); WAIT_V(0); ST_READ(0); ENSURE_KV(2); ENSURE_KV(4);
      WAIT_L(0); BAR();
      if (tid == 0) {
        __hip_atomic_store(flags + FL_Q + 32 * (8 * h + j), 1u, __ATOMIC_RELAXED, __HIP_MEMORY_SCOPE_AGENT);
        if ((j & 3) >= 2) __hip_atomic_store(flags + FL_KV + 16 * (8 * h + j), 1u, __ATOMIC_RELAXED, __HIP_MEMORY_SCOPE_AGENT);
      }
      GLDS_T(VSRC(2), dt2 + L_V + 32768, 0);
    } else {
      GLDS_T(KSRC(4), dt2 + L_K, 1024);
    }
#pragma unroll
    for (int i = 0; i < 4; ++i) kq[i] = ldsr128(lds + L_K + 16384 + rdoff + i * 1024);
    float s0 = 0.f;
#pragma unroll
    for (int r = 0; r < 16; ++r) { C0[r] = EX2(C0[r]); s0 += C0[r]; }
#pragma unroll
    for (int r = 0; r < 8; ++r) C1[r] = EX2(C1[r]);
    lsum = s0;
    pw[0] = pack8(C0, 0); pw[1] = pack8(C0, 1);
#pragma unroll
    for (int d = 0; d < 4; ++d) o[d] = z;
  }
  const int ddm = (wid >> 1) + 1;
  const int gA = ddm > 3 ? 18 : (4 * jp + ddm - t2 > 0 ? n1 + 4 * jp + ddm - t2 : n1);
  auto stepg = [&](auto aq_, auto ap_, const int g) {
    constexpr bool AQ = decltype(aq_)::value != 0, AP = decltype(ap_)::value != 0;
    const bool seam = (g == n1);
    const int X = g < n1 ? j : jp;
    const int dd = TILE(g) - 4 * X;
    ldscp kb = lds + L_K + (g & 3) * 16384 + rdoff;
    ldscp vb = lds + L_V + ((g - 1) & 3) * 16384 + rdoff;
    ldscp kn = lds + L_K + ((g + 1) & 3) * 16384 + rdoff;
    const char* vs_n = VSRC(g + 2); const char* ks_n = KSRC(g + 4);
    const char* vs_p = VSRC(g + 1); const char* ks_p = KSRC(g + 3);
    const unsigned vd_n = dt2 + L_V + ((g + 2) & 3) * 16384, kd_n = dt2 + L_K + (g & 3) * 16384;
    const unsigned vd_p = dt2 + L_V + ((g + 1) & 3) * 16384, kd_p = dt2 + L_K + ((g - 1) & 3) * 16384;
    const bool gq = (GRP == 0) || g > 1;
    float lsum1 = 0.f, mhat1 = 0.f;
    if (seam) {
      mhat1 = mhat; mhat = 0.f;
#pragma unroll
      for (int r = 0; r < 16; ++r) negm[r] = 0.f;
      PIN(negm);
    }
    if (g >= 2 && g < 12 && wid == ((g - 2) & 7)) {
      const unsigned lc = (unsigned)(g - 2) * 64u + lane, gid = blockIdx.x * 592u + lc;
      const char* p = gid < 40960u ? pfw.wo + (size_t)gid * 128 : (gid < 96256u ? pfw.w1 + (size_t)(gid - 40960u) * 128 : pfw.w2 + (size_t)(gid - 96256u) * 128);
      if (lc < 592u && gid < 151552u) asm volatile("global_load_dword %0, %1, off" : "+v"(pfd) : "v"(p) : "memory");
    }
    ST_ISSUE();
    float sa = 0.f, sb = 0.f;
    SBAR();
#define KRD(u) kq[(u) & 3] = ldsr128(kb + ((u) + 4) * 1024)
#define VRD(v) vq[(v) & 3] = ldsr128(vb + (v) * 1024)
#define KPRE(i) kq[i] = ldsr128(kn + (i) * 1024)
#define BARRIER_G() do { if (GRP == 0) { ST_READ(3); ENSURE_KV(g + 4); if (wid == 0 && g == n1 - 2 && j != 0 && !(kvmask & 256u)) wait_word(flags + FL_Q + 32 * (8 * h + jp), 1u, flags + FL_TMO); \
        if (g == 17 && wid == 0 && lane == 0) *(LAS unsigned*)(lds + 131072) = kvmask; } \
      if (g < 17) WAIT_V(6); else WAIT_V(0); WAIT_L(0); BAR(); SBAR(); \
      if (GRP == 0 && (g == n1 + 2 || (g == 17 && n1 + 2 > 17)) && tid == 0) \
        __hip_atomic_store(flags + FL_P + 32 * (8 * h + j), 1u, __ATOMIC_RELAXED, __HIP_MEMORY_SCOPE_AGENT); } while (0)
    float fsc = 1.f; bool resc = false;
    {
#define Q_(x) do { if constexpr (AQ) { x; } } while (0)
#define P_(x) do { if constexpr (AP) { x; } } while (0)
      Q_(C0 = MFMA32(kq[0], qr[0], negm); KRD(0)); P_(C1[8] = EX2(C1[8]); sa += C1[0]; sb += C1[1]; sa += C1[2]; sb += C1[3]; PIN(C1); PIN(sa); PIN(sb)); SBAR();
      Q_(C0 = MFMA32(kq[1], qr[1], C0); KRD(1)); P_(C1[9] = EX2(C1[9]); sa += C1[4]; sb += C1[5]; sa += C1[6]; sb += C1[7]; PIN(C1); PIN(sa); PIN(sb)); SBAR();
      Q_(C0 = MFMA32(kq[2], qr[2], C0); KRD(2)); if (GRP == 0) GLDS_T(vs_p, vd_p, 1024); else if (gq) GLDS_T(ks_p, kd_p, 1024);
          P_(C1[10] = EX2(C1[10]); pw[2] = pack8(C1, 0); PIN(C1); PIN(pw[2])); SBAR();
      Q_(C0 = MFMA32(kq[3], qr[3], C0); KRD(3)); P_(C1[11] = EX2(C1[11]); C1[12] = EX2(C1[12]); sa += C1[8]; sb += C1[9]; PIN(C1); PIN(sa); PIN(sb)); SBAR();
      Q_(C0 = MFMA32(kq[0], qr[4], C0); KRD(4)); P_(C1[13] = EX2(C1[13]); C1[14] = EX2(C1[14]); sa += C1[10]; sb += C1[11]; PIN(C1); PIN(sa); PIN(sb)); SBAR();
      Q_(C0 = MFMA32(kq[1], qr[5], C0); KRD(5)); P_(C1[15] = EX2(C1[15]); sa += C1[12]; sb += C1[13]; sa += C1[14]; PIN(C1); PIN(sa); PIN(sb)); SBAR();
      Q_(C0 = MFMA32(kq[2], qr[6], C0); KRD(6)); P_(sb += C1[15]; pw[3] = pack8(C1, 1); PIN(pw[3]); PIN(sb)); SBAR();
      Q_(C0 = MFMA32(kq[3], qr[7], C0); KRD(7)); P_(lsum += sa + sb; PIN(lsum)); SBAR();
      Q_(C1 = MFMA32(kq[0], qr[0], negm); KRD(8)); if (GRP == 0) GLDS_T(ks_p, kd_p, 0); SBAR();
      Q_(C1 = MFMA32(kq[1], qr[1], C1); KRD(9)); SBAR();
      Q_(C1 = MFMA32(kq[2], qr[2], C1); KRD(10)); SBAR();
      float mxa = 0.f, mxb = 0.f;
      Q_(C1 = MFMA32(kq[3], qr[3], C1); KRD(11); mxa = max3f(C0[0], C0[1], C0[2]); mxb = max3f(C0[3], C0[4], C0[5]); PIN(mxa); PIN(mxb)); SBAR();
      if (GRP == 1) BARRIER_G();
      Q_(C1 = MFMA32(kq[0], qr[4], C1)); P_(VRD(0)); if (GRP == 1) GLDS_T(vs_n, vd_n, 0); Q_(mxa = max3f(mxa, C0[6], C0[7]); mxb = max3f(mxb, C0[8], C0[9]); PIN(mxa); PIN(mxb)); SBAR();
      Q_(C1 = MFMA32(kq[1], qr[5], C1)); P_(VRD(1)); Q_(mxa = max3f(mxa, C0[10], C0[11]); mxb = max3f(mxb, C0[12], C0[13]); PIN(mxa); PIN(mxb)); SBAR();
      Q_(C1 = MFMA32(kq[2], qr[6], C1)); P_(VRD(2)); Q_(mxa = max3f(mxa, C0[14], C0[15]); mxa = __builtin_fmaxf(mxa, mxb); PIN(mxa)); SBAR();
      Q_(C1 = MFMA32(kq[3], qr[7], C1)); P_(VRD(3)); SBAR();
      if (g == n1 - 1) {
        if (j == 0) wait_word(flags + FL_Q + 32 * (8 * h + jp), 1u, flags + FL_TMO);
        const char* qsrc = ws + WS_QF + (size_t)(h * 64 + 8 * jp + wid) * 8192;
        GLOAD1_SC1(qr[0], vl16, qsrc, 0); GLOAD1_SC1(qr[1], vl16, qsrc, 1024); GLOAD1_SC1(qr[2], vl16, qsrc, 2048); GLOAD1_SC1(qr[3], vl16, qsrc, 3072);
        GLOAD1_SC1(qr[4], vl16, qsrc + 4096, 0); GLOAD1_SC1(qr[5], vl16, qsrc + 4096, 1024); GLOAD1_SC1(qr[6], vl16, qsrc + 4096, 2048); GLOAD1_SC1(qr[7], vl16, qsrc + 4096, 3072);
      }
      SBAR();
      P_(o[0] = MFMA32(vq[0], pw[0], o[0]); VRD(4)); if (GRP == 1) GLDS_T(vs_n, vd_n, 1024); SBAR();
      P_(o[1] = MFMA32(vq[1], pw[0], o[1]); VRD(5)); SBAR();
      float mxc = 0.f, mxd = 0.f;
      P_(o[2] = MFMA32(vq[2], pw[0], o[2]); VRD(6)); if (GRP == 0) GLDS_T(ks_p, kd_p, 1024);
          Q_(mxc = max3f(C1[0], C1[1], C1[2]); mxd = max3f(C1[3], C1[4], C1[5]); mxc = max3f(mxc, C1[6], C1[7]); mxd = max3f(mxd, C1[8], C1[9]); PIN(mxc); PIN(mxd)); SBAR();
      P_(o[3] = MFMA32(vq[3], pw[0], o[3]); VRD(7)); Q_(mxc = max3f(mxc, C1[10], C1[11]); mxd = max3f(mxd, C1[12], C1[13]); mxc = max3f(mxc, C1[14], C1[15]); mxa = max3f(mxa, mxc, mxd); PIN(mxa)); SBAR();
      P_(o[0] = MFMA32(vq[0], pw[1], o[0]); VRD(8));
      MEMFENCE(); SBAR();
      if constexpr (AQ) {
        float rm = mxa;
        if (dd >= 0 && wid <= 2 * dd + 1) { cmask(C0, C1, 32 * wid + l31 - 64 * dd - 4 * hh); rm = __builtin_fmaxf(max16(C0), max16(C1)); }
        rm = xhalf_max(rm);
        if (__builtin_expect(seam || __any(rm > THR), 0)) {
          const float dl = seam ? __builtin_fmaxf(rm, -1e30f) : __builtin_fmaxf(rm, 0.f);
          mhat += dl;
#pragma unroll
          for (int r = 0; r < 16; ++r) { C0[r] -= dl; C1[r] -= dl; negm[r] = -mhat; }
          PIN(negm);
          if (seam) { lsum1 = lsum; lsum = 0.f; }
          else { fsc = EX2(-dl); lsum *= fsc; resc = true; }
        }
      } else {
        if (seam) { lsum1 = lsum; lsum = 0.f; mhat = -1e30f; }
      }
      SBAR();
      sa = 0.f; sb = 0.f;
      P_(o[1] = MFMA32(vq[1], pw[1], o[1]); VRD(9)); Q_(C0[0] = EX2(C0[0]); C0[1] = EX2(C0[1]); C0[2] = EX2(C0[2]); PIN(C0)); SBAR();
      P_(o[2] = MFMA32(vq[2], pw[1], o[2]); VRD(10)); Q_(C0[3] = EX2(C0[3]); C0[4] = EX2(C0[4]); sa += C0[0]; sb += C0[1]; PIN(C0); PIN(sa); PIN(sb)); SBAR();
      P_(o[3] = MFMA32(vq[3], pw[1], o[3]); VRD(11)); Q_(C0[5] = EX2(C0[5]); C0[6] = EX2(C0[6]); sa += C0[2]; sb += C0[3]; PIN(C0); PIN(sa); PIN(sb)); SBAR();
      P_(o[0] = MFMA32(vq[0], pw[2], o[0]); VRD(12)); if (GRP == 1) GLDS_T(ks_n, kd_n, 0); Q_(C0[7] = EX2(C0[7]); C0[8] = EX2(C0[8]); sa += C0[4]; sb += C0[5]; PIN(C0); PIN(sa); PIN(sb)); SBAR();
      P_(o[1] = MFMA32(vq[1], pw[2], o[1]); VRD(13)); Q_(pw[0] = pack8(C0, 0); C0[9] = EX2(C0[9]); PIN(pw[0]); PIN(C0)); SBAR();
      P_(o[2] = MFMA32(vq[2], pw[2], o[2]); VRD(14)); Q_(C0[10] = EX2(C0[10]); C0[11] = EX2(C0[11]); sa += C0[6]; sb += C0[7]; PIN(C0); PIN(sa); PIN(sb)); SBAR();
      P_(o[3] = MFMA32(vq[3], pw[2], o[3]); VRD(15)); Q_(C0[12] = EX2(C0[12]); C0[13] = EX2(C0[13]); sa += C0[8]; sb += C0[9]; PIN(C0); PIN(sa); PIN(sb)); SBAR();
      if (GRP == 0) BARRIER_G();
      P_(o[0] = MFMA32(vq[0], pw[3], o[0])); KPRE(0); if (GRP == 0) GLDS_T(vs_n, vd_n, 0); Q_(C0[14] = EX2(C0[14]); C0[15] = EX2(C0[15]); sa += C0[10]; sb += C0[11]; PIN(C0); PIN(sa); PIN(sb)); SBAR();
      P_(o[1] = MFMA32(vq[1], pw[3], o[1])); KPRE(1); Q_(C1[0] = EX2(C1[0]); C1[1] = EX2(C1[1]); sa += C0[12]; sb += C0[13]; PIN(C1); PIN(sa); PIN(sb)); SBAR();
      P_(o[2] = MFMA32(vq[2], pw[3], o[2])); KPRE(2); Q_(pw[1] = pack8(C0, 1); C1[2] = EX2(C1[2]); C1[3] = EX2(C1[3]); PIN(pw[1]); PIN(C1)); SBAR();
      P_(o[3] = MFMA32(vq[3], pw[3], o[3])); KPRE(3); Q_(C1[4] = EX2(C1[4]); C1[5] = EX2(C1[5]); sa += C0[14]; sb += C0[15]; PIN(C1); PIN(sa); PIN(sb)); SBAR();
      Q_(C1[6] = EX2(C1[6]); C1[7] = EX2(C1[7]); lsum += sa + sb; PIN(C1); PIN(lsum));
#undef Q_
#undef P_
    }
    if (resc) {
#pragma unroll
      for (int d = 0; d < 4; ++d) o[d] = o[d] * fsc;
    }
    if (seam) {
      const float lt = xhalf_sum(lsum1);
      const char* pst = ws + WS_PF + (size_t)((h * 8 + j) * 8 + wid) * 8192;
      static_for<4>([&](auto dd_) { constexpr int d = decltype(dd_)::value; gstore_sc1_c<(2 * d) * 1024>(vl16, pack8(o[d], 0), pst); gstore_sc1_c<(2 * d + 1) * 1024>(vl16, pack8(o[d], 1), pst); o[d] = z; });
      const unsigned long long sv = ((unsigned long long)__float_as_uint(lt) << 32) | __float_as_uint(mhat1);
      __hip_atomic_store((unsigned long long*)(ws + WS_PS) + (size_t)((h * 8 + j) * 8 + wid) * 64 + lane, sv, __ATOMIC_RELAXED, __HIP_MEMORY_SCOPE_AGENT);
    }
    if (g == n1 - 1) {
      asm volatile("s_waitcnt vmcnt(1)" : "+v"(qr[0]), "+v"(qr[1]), "+v"(qr[2]), "+v"(qr[3]), "+v"(qr[4]), "+v"(qr[5]), "+v"(qr[6]), "+v"(qr[7]) :: "memory");
    }
  };
  for (int g = 1; g < gA; ++g) stepg(IC<1>{}, IC<1>{}, g);
  if (gA < 18) stepg(IC<0>{}, IC<1>{}, gA);
  for (int g = gA + 1; g < 18; ++g) stepg(IC<0>{}, IC<0>{}, g);
  const bool actP = gA >= 18;
  asm volatile("" :: "v"(pfd));
  {
    const bool early = (rfl(*(const LAS unsigned*)(lds + 131072)) & 512u) != 0u;
    const char* psrc = upt(ws + WS_PF + (size_t)((h * 8 + jp) * 8 + wid) * 8192);
    const char* psrc2 = upt(psrc + 4096);
    const unsigned long long* svp = (const unsigned long long*)(ws + WS_PS) + (size_t)((h * 8 + jp) * 8 + wid) * 64 + lane;
    u32x4 qa[4], qb[4];
    u32x2 svv;
#define PLOADS() do { GLOAD1_SC1(qa[0], vl16, psrc, 0); GLOAD1_SC1(qa[1], vl16, psrc, 1024); GLOAD1_SC1(qa[2], vl16, psrc, 2048); GLOAD1_SC1(qa[3], vl16, psrc, 3072); \
      GLOAD1_SC1(qb[0], vl16, psrc2, 0); GLOAD1_SC1(qb[1], vl16, psrc2, 1024); GLOAD1_SC1(qb[2], vl16, psrc2, 2048); GLOAD1_SC1(qb[3], vl16, psrc2, 3072); \
      asm volatile("global_load_dwordx2 %0, %1, off sc1" : "=&v"(svv) : "v"(svp) : "memory"); } while (0)
    if (early) PLOADS();
    ldscp vb = lds + L_V + (17 & 3) * 16384 + rdoff;
    if (actP) {
#pragma unroll
    for (int i = 0; i < 4; ++i) vq[i] = ldsr128(vb + i * 1024);
    float sa = 0.f;
#pragma unroll
    for (int r = 8; r < 16; ++r) C1[r] = EX2(C1[r]);
#pragma unroll
    for (int r = 0; r < 16; ++r) sa += C1[r];
    lsum += sa;
    pw[2] = pack8(C1, 0); pw[3] = pack8(C1, 1);
    SBAR();
#pragma unroll
    for (int v = 0; v < 16; ++v) {
      o[v & 3] = MFMA32(vq[v & 3], pw[v >> 2], o[v & 3]);
      if (v + 4 < 16) vq[v & 3] = ldsr128(vb + (v + 4) * 1024);
      SBAR();
    }
    }
    lsum = xhalf_sum(lsum);
    if (!early) { wait_word(flags + FL_P + 32 * (8 * h + jp), 1u, flags + FL_TMO); PLOADS(); }
    WAITV_TIE4(0, qa); WAITV_TIE4(0, qb);
    asm volatile("" : "+v"(svv));
    const unsigned long long sv = ((unsigned long long)svv[1] << 32) | svv[0];
#undef PLOADS
    const float om = __uint_as_float((unsigned)sv), ol = __uint_as_float((unsigned)(sv >> 32));
    const float m = __builtin_fmaxf(mhat, om);
    const float f0 = EX2(mhat - m), f1 = EX2(om - m);
    const float inv = __builtin_amdgcn_rcpf(lsum * f0 + ol * f1);
    const float g0 = f0 * inv, g1 = f1 * inv;
    const char* cdst = upt(ws + WS_CTX + (size_t)((8 * jp + wid) * 32 + h) * 8192);
    static_for<8>([&](auto ii_) {
      constexpr int i = decltype(ii_)::value; constexpr int d = i >> 1, sp = i & 1;
      const u32x4 v4 = i < 4 ? qa[i & 3] : qb[i & 3];
      u32x4 w;
#pragma unroll
      for (int e = 0; e < 4; ++e) w[e] = cvtpk(o[d][8 * sp + 2 * e] * g0 + bflo(v4[e]) * g1, o[d][8 * sp + 2 * e + 1] * g0 + bfhi(v4[e]) * g1);
      gstore_sc1_c<i * 1024>(vl16, __builtin_bit_cast(bf16x8, w), cdst);
    });
  }
#undef KRD
#undef VRD
#undef KPRE
#undef BARRIER_G
#undef ENSURE_KV
#undef GLDS_T
#undef TILE
#undef KSRC
#undef VSRC
}

constexpr int T_LDS = 163840;
constexpr int FL_C = 28672;
constexpr int FL_P3 = 20480;
constexpr int FL_H = 22528;
constexpr int FL_U = 23552;
#define GLOAD4_SC1(d, voff, sbase) asm volatile("s_nop 4\n\tglobal_load_dwordx4 %0, %4, %5 sc1\n\tglobal_load_dwordx4 %1, %4, %5 offset:1024 sc1\n\tglobal_load_dwordx4 %2, %4, %5 offset:2048 sc1\n\tglobal_load_dwordx4 %3, %4, %5 offset:3072 sc1" \
    : "=&v"(d[0]), "=&v"(d[1]), "=&v"(d[2]), "=&v"(d[3]) : "v"(voff), "s"(sbase) : "memory")
#define GLDS_SC1(voff, sbase, ldst) do { unsigned keep_; asm volatile("s_mov_b32 %0, m0\n\ts_mov_b32 m0, %3\n\ts_nop 2\n\tglobal_load_lds_dwordx4 %1, %2 sc1\n\ts_mov_b32 m0, %0" \
    : "=&s"(keep_) : "v"(voff), "s"(sbase), "s"(ldst) : "memory"); } while (0)
template <int OFFS> DI void gstore_sc1_uc(unsigned voff, const u32x4& v, const void* sbase) {
  unsigned t;
  asm volatile("v_add_u32 %0, %4, %1\n\ts_nop 3\n\tglobal_store_dwordx4 %0, %2, %3 sc1\n\ts_nop 1" : "=&v"(t) : "v"(voff), "v"(v), "s"(sbase), "i"(OFFS) : "memory");
}
DI void gstore_sc1_u(unsigned voff, const u32x4& v, const void* sbase) {
  asm volatile("s_nop 4\n\tglobal_store_dwordx4 %0, %1, %2 sc1\n\ts_nop 1" :: "v"(voff), "v"(v), "s"(sbase) : "memory");
}
template <bool FIRST> struct MlpCfg {
  static constexpr int PITCH = FIRST ? FF * 2 : SEQ * 2;
  static constexpr int NKT = FIRST ? SEQ / 64 : FF / 64;
  static constexpr int NST = (NKT + 1) / 2;
};
template <bool FIRST> DI unsigned mlp_voffw(int wid, int lane) {
  const int row = 8 * wid + (lane >> 3); const int f = ((row >> 1) & 1) | (((row >> 3) & 1) << 1);
  return (unsigned)(row * MlpCfg<FIRST>::PITCH + (((lane & 7) ^ (f << 1)) << 4));
}
template <bool FIRST, int KT0, int NK> DI void mlp_wstage(unsigned lds0, const char* wb, unsigned voffW, int wid, int reg) {
  static_for<NK>([&](auto kk_) {
    constexpr int k = decltype(kk_)::value; constexpr int kt = (KT0 + k) < MlpCfg<FIRST>::NKT ? (KT0 + k) : MlpCfg<FIRST>::NKT - 1;
    glds16_c<kt * 64 * MlpCfg<FIRST>::PITCH>(voffW, wb, lds0 + reg + k * 8192 + wid * 1024);
  });
}
struct MlpW { ldscp p[4]; };
DI MlpW mlp_wptr(ldsp lds, int wid, int lane) {
  const int g = lane >> 4, q = (lane & 15) >> 2, p = lane & 3; const int f = (q >> 1) | ((g & 1) << 1);
  MlpW w;
#pragma unroll
  for (int ct = 0; ct < 4; ++ct) w.p[ct] = lds + (wid >> 1) * 8192 + 128 * (32 * (wid & 1) + 8 * g + q) + (((2 * ct + (p >> 1)) ^ (f << 1)) << 4) + 8 * (p & 1);
  return w;
}
DI void mlp_wrd(const MlpW& w, int off, bf16x8* dst) {
#pragma unroll
  for (int ct = 0; ct < 4; ++ct) dst[ct] = cat8(vtr(w.p[ct] + off), vtr(w.p[ct] + off + 512));
}
#define MLP_ALOAD(dst, base) asm volatile("s_nop 4\n\tglobal_load_dwordx4 %0, %5, %7 sc1\n\tglobal_load_dwordx4 %1, %5, %7 offset:1024 sc1\n\tglobal_load_dwordx4 %2, %5, %7 offset:2048 sc1\n\t" \
    "global_load_dwordx4 %3, %5, %7 offset:3072 sc1\n\tglobal_load_dwordx4 %4, %6, %7 sc1" \
    : "=&v"(dst[0]), "=&v"(dst[1]), "=&v"(dst[2]), "=&v"(dst[3]), "=&v"(dst[4]) : "v"(vl16), "v"(vl16b), "s"(base) : "memory")
template <bool FIRST, int R0, int R1, int LK0, int NW>
DI void mlp_main(ldsp lds, const char* __restrict__ ab, int wid, int lane, const MlpW& wp, const bf16x8 (&wr)[NW], f32x4 (&acc)[4][5]) {
  constexpr int NT = 2 * MlpCfg<FIRST>::NKT, NS = (NT + 7) / 8;
  const unsigned vl16 = lane * 16, vl16b = lane * 16 + 4096;
  u32x4 af[3][5];
  bf16x8 wl[2][4];
#pragma unroll
  for (int c = 0; c < 4; ++c)
#pragma unroll
    for (int d = 0; d < 5; ++d) acc[c][d] = (f32x4){0.f, 0.f, 0.f, 0.f};
  auto abase = [&](int s) { const int T = 8 * s + wid; return upt(ab + (size_t)(T < NT ? T : NT - 1) * 40960); };
  { const char* b0 = abase(0); MLP_ALOAD(af[0], b0); const char* b1 = abase(1); MLP_ALOAD(af[1], b1); }
  if constexpr (!(0 >= R0 && 0 < R1)) mlp_wrd(wp, (0 - LK0) * 8192, wl[0]);
  static_for<NS>([&](auto ss_) {
    constexpr int s = decltype(ss_)::value;
    if constexpr (s + 2 < NS) { const char* bn = abase(s + 2); MLP_ALOAD(af[(s + 2) % 3], bn); }
    if constexpr (s + 1 < NS && !(s + 1 >= R0 && s + 1 < R1)) mlp_wrd(wp, (4 * (s + 1) - LK0) * 8192, wl[(s + 1) & 1]);
    constexpr int ahead = (NS - 1 - s) < 2 ? (NS - 1 - s) : 2;
    asm volatile("s_waitcnt vmcnt(%5)" : "+v"(af[s % 3][0]), "+v"(af[s % 3][1]), "+v"(af[s % 3][2]), "+v"(af[s % 3][3]), "+v"(af[s % 3][4]) : "i"(5 * ahead) : "memory");
    SBAR();
    if (8 * s + 7 < NT || 8 * s + wid < NT) {
#pragma unroll
      for (int c = 0; c < 4; ++c) {
        const bf16x8 wfr = (s >= R0 && s < R1) ? wr[(s >= R0 && s < R1) ? (s - R0) * 4 + c : 0] : wl[s & 1][c];
#pragma unroll
        for (int d = 0; d < 5; ++d) {
          const bf16x8 a8 = __builtin_bit_cast(bf16x8, af[s % 3][d]);
          acc[c][d] = FIRST ? MFMA16(wfr, a8, acc[c][d]) : MFMA16(a8, wfr, acc[c][d]);
        }
      }
    }
    SBAR();
  });
}
DI void mlp_park(ldsp lds, int wid, int lane, f32x4 (&acc)[4][5]) {
  ldsp pw_ = lds + wid * 10240 + lane * 16;
  ldsp pr_ = lds + (wid ^ 4) * 10240 + lane * 16;
  if (wid < 4) {
#pragma unroll
    for (int c = 0; c < 2; ++c)
#pragma unroll
      for (int d = 0; d < 5; ++d) *(LAS f32x4*)(pw_ + (c * 5 + d) * 1024) = acc[2 + c][d];
  } else {
#pragma unroll
    for (int c = 0; c < 2; ++c)
#pragma unroll
      for (int d = 0; d < 5; ++d) *(LAS f32x4*)(pw_ + (c * 5 + d) * 1024) = acc[c][d];
  }
  WAIT_L(0); BAR();
  if (wid < 4) {
#pragma unroll
    for (int c = 0; c < 2; ++c)
#pragma unroll
      for (int d = 0; d < 5; ++d) { const f32x4 o = *(const LAS f32x4*)(pr_ + (c * 5 + d) * 1024); *(LAS f32x4*)(pr_ + (c * 5 + d) * 1024) = acc[c][d] + o; }
  } else {
#pragma unroll
    for (int c = 0; c < 2; ++c)
#pragma unroll
      for (int d = 0; d < 5; ++d) { const f32x4 o = *(const LAS f32x4*)(pr_ + (c * 5 + d) * 1024); *(LAS f32x4*)(pr_ + (c * 5 + d) * 1024) = acc[2 + c][d] + o; }
  }
  WAIT_L(0); BAR();
}
DI void mlp_gather(ldscp lds, int c2, int dt, int gD, int c15, float (&v)[8]) {
  ldscp p = lds + ((c2 < 2 ? 4 : 0) * 10 + (c2 & 1) * 5 + dt) * 1024 + (gD * 16 + c15) * 16;
#pragma unroll
  for (int hf = 0; hf < 2; ++hf) {
    f32x4 a = {0.f, 0.f, 0.f, 0.f};
#pragma unroll
    for (int q = 0; q < 4; ++q) a += *(const LAS f32x4*)(p + q * 10240 + hf * 256);
#pragma unroll
    for (int e = 0; e < 4; ++e) v[4 * hf + e] = a[e];
  }
}

DI void tail_phase(unsigned char* smem, const u16* __restrict__ x, const u16* __restrict__ wo, const u16* __restrict__ w1, const u16* __restrict__ b1,
                   const u16* __restrict__ w2, const u16* __restrict__ b2, char* __restrict__ ws, u16* __restrict__ out, int wid) {
  const int b = blockIdx.x, xcd = b & 7, r = b >> 3;
  ldsp lds = (ldsp)smem;
  const unsigned lds0 = (unsigned)(uintptr_t)smem;
  unsigned* flags = (unsigned*)(ws + WS_FLAGS);
  const int ksp = xcd >> 1, panel = (xcd & 1) * 8 + (r >> 2), ctile = r & 3;
  const int idx5 = xcd * 27 + r;
  const int wt5 = idx5 >> 3, dg5 = idx5 & 7;
  const int wt6 = xcd * 4 + (r >> 3), dg6 = r & 7;
  const bool do5 = r < 27;
  constexpr int P4S = 131072;
  bf16x8 wf[16];
  {
    const int lane = lane_id();
    const unsigned vl16 = lane * 16;
    const int kh = wid >> 2, st = wid & 3, hh = lane >> 5;
    unsigned voff[5];
#pragma unroll
    for (int i = 0; i < 5; ++i) {
      const int id = 2 * (5 * wid + i) + (lane >> 5), half = id / 40, rem = id % 40, rg = rem / 5, c = rem % 5;
      const int row = 64 * half + 8 * rg + ((lane >> 2) & 7);
      voff[i] = (unsigned)(row * 1280 + (32 * c + 8 * ((lane & 3) ^ ((row >> 2) & 3))) * 2);
    }
    const char* wb = (const char*)wo + (size_t)(ksp * 1024) * 1280 + ctile * 320;
    const unsigned dstw = lds0 + wid * 5120;
#define OPIECE(hd, i) glds16_c<(hd) * 128 * 1280>(voff[i], wb, dstw + ((hd) & 3) * 40960 + (i) * 1024)
#define STAGE_O(hd) do { OPIECE(hd, 0); OPIECE(hd, 1); OPIECE(hd, 2); OPIECE(hd, 3); OPIECE(hd, 4); } while (0)
    const char* cb = ws + WS_CTX + ((size_t)((4 * panel + st) * 32 + 8 * ksp) * 8 + 4 * kh) * 1024;
    int boff[2];
    { const int blk = (lane >> 4) & 1, q = (lane & 15) >> 2, p = lane & 3;
#pragma unroll
      for (int t = 0; t < 2; ++t) boff[t] = kh * 20480 + 2560 * t + 64 * (4 * hh + q) + (((2 * blk + (p >> 1)) ^ ((2 * t + hh) & 3)) << 4) + 8 * (p & 1); }
    ldscp bp0 = lds + boff[0], bp1 = lds + boff[1];
    u32x4 xf[3][4];
    f32x16 acc[5] = {};
    bf16x8 aq[6];
#define ARD(U) aq[(U) % 6] = cat8(vtr(bp0 + (((U) / 20) & 3) * 40960 + (((U) % 20) / 5) * 5120 + ((U) % 5) * 512), vtr(bp1 + (((U) / 20) & 3) * 40960 + (((U) % 20) / 5) * 5120 + ((U) % 5) * 512))
    STAGE_O(0); STAGE_O(1); STAGE_O(2);
    if (wid == 0) wait_word(flags + FL_C + 16 * ((panel >> 1) * 4 + ksp), 8u, flags + FL_TMO);
    BAR();
    GLOAD4_SC1(xf[0], vl16, cb); GLOAD4_SC1(xf[1], vl16, cb + 8192);
    WAIT_V(8); BAR();
#pragma unroll
    for (int U = 0; U < 5; ++U) ARD(U);
    static_for<8>([&](auto hh_) {
      constexpr int hd = decltype(hh_)::value;
      if constexpr (hd == 0) { WAITV_TIE4(4, xf[0]); }
      else if constexpr (hd <= 5) { WAITV_TIE4(9, xf[hd % 3]); }
      else if constexpr (hd == 6) { WAITV_TIE4(4, xf[hd % 3]); }
      else { WAITV_TIE4(0, xf[hd % 3]); }
      SBAR(); BAR(); SBAR();
      const char* xn_ = cb + (size_t)(hd + 2) * 8192;
      static_for<20>([&](auto uu_) {
        constexpr int u = decltype(uu_)::value;
        constexpr int U = 20 * hd + u;
        acc[u % 5] = MFMA32(aq[U % 6], __builtin_bit_cast(bf16x8, xf[hd % 3][u / 5]), acc[u % 5]);
        if constexpr (U + 5 < 160) ARD(U + 5);
        if constexpr (hd + 2 < 8) {
          if constexpr (u == 1) GLOAD1_SC1(xf[(hd + 2) % 3][0], vl16, xn_, 0);
          if constexpr (u == 3) GLOAD1_SC1(xf[(hd + 2) % 3][1], vl16, xn_, 1024);
          if constexpr (u == 5) GLOAD1_SC1(xf[(hd + 2) % 3][2], vl16, xn_, 2048);
          if constexpr (u == 7) GLOAD1_SC1(xf[(hd + 2) % 3][3], vl16, xn_, 3072);
        }
        if constexpr (hd + 3 < 8) {
          if constexpr (u == 9) OPIECE(hd + 3, 0);
          if constexpr (u == 11) OPIECE(hd + 3, 1);
          if constexpr (u == 13) OPIECE(hd + 3, 2);
          if constexpr (u == 15) OPIECE(hd + 3, 3);
          if constexpr (u == 17) OPIECE(hd + 3, 4);
        }
        SBAR();
      });
    });
#undef OPIECE
#undef STAGE_O
#undef ARD
    BAR();
    ldsp xw = lds + (kh == 0 ? 49152 : 0) + st * 12288 + lane * 16;
    ldscp xr = lds + (kh == 0 ? 0 : 49152) + st * 12288 + lane * 16;
    if (kh == 0) {
#pragma unroll
      for (int i = 0; i < 2; ++i)
#pragma unroll
        for (int g = 0; g < 4; ++g) *(LAS f32x4*)(xw + (i * 4 + g) * 1024) = (f32x4){acc[3 + i][4 * g], acc[3 + i][4 * g + 1], acc[3 + i][4 * g + 2], acc[3 + i][4 * g + 3]};
    } else {
#pragma unroll
      for (int i = 0; i < 3; ++i)
#pragma unroll
        for (int g = 0; g < 4; ++g) *(LAS f32x4*)(xw + (i * 4 + g) * 1024) = (f32x4){acc[i][4 * g], acc[i][4 * g + 1], acc[i][4 * g + 2], acc[i][4 * g + 3]};
    }
    WAIT_L(0); BAR();
    const char* sb = ws + WS_SLAB + ((size_t)((ksp * 64 + 4 * panel + st) * 20 + 5 * ctile) * 2) * 1024;
#define SLABST(I, T) static_for<2>([&](auto pp_) { \
          constexpr int sp = decltype(pp_)::value; \
          const f32x4 o0 = *(const LAS f32x4*)(xr + ((I) * 4 + 2 * sp) * 1024), o1 = *(const LAS f32x4*)(xr + ((I) * 4 + 2 * sp + 1) * 1024); \
          const u32x4 w = {cvtpk(acc[T][8 * sp] + o0[0], acc[T][8 * sp + 1] + o0[1]), cvtpk(acc[T][8 * sp + 2] + o0[2], acc[T][8 * sp + 3] + o0[3]), \
                           cvtpk(acc[T][8 * sp + 4] + o1[0], acc[T][8 * sp + 5] + o1[1]), cvtpk(acc[T][8 * sp + 6] + o1[2], acc[T][8 * sp + 7] + o1[3])}; \
          gstore_sc1_uc<((T) * 2 + sp) * 1024>(vl16, w, sb); })
    if (kh == 0) { SLABST(0, 0); SLABST(1, 1); SLABST(2, 2); }
    else { SLABST(0, 3); SLABST(1, 4); }
#undef SLABST
    WAIT_V(0); WAIT_L(0); BAR();
    if (wid == 0 && lane == 0) __hip_atomic_fetch_add(flags + FL_P3 + 64 * panel, 1u, __ATOMIC_RELAXED, __HIP_MEMORY_SCOPE_AGENT);
  }
  {
    const int lane = lane_id(), tid = wid * 64 + lane;
    const MlpW wp = mlp_wptr(lds, wid, lane);
    const char* w1b = (const char*)w1 + wt5 * 128;
    const unsigned voffW = mlp_voffw<true>(wid, lane);
    ldsp tr = lds + P4S;
    LAS float* red = (LAS float*)(lds + P4S + 10240);
    const int rr = tid & 7, pc = tid >> 3;
    const int r8 = ksp * 4 + ctile;
    const int s = 128 * panel + 8 * r8 + rr, st = s >> 5;
    int n0[2]; unsigned so[2];
    u32x2 xv[2][2];
#pragma unroll
    for (int i = 0; i < 2; ++i) {
      const int pi = pc + 64 * i; const int pcl = pi < 80 ? pi : 0;
      const int nt = pcl >> 2, sp = (pcl >> 1) & 1, hh = pcl & 1;
      n0[i] = 32 * nt + 16 * sp + 4 * hh;
      so[i] = (unsigned)((((st * 20 + nt) * 2 + sp) * 64 + (s & 31) + 32 * hh) * 16);
      xv[i][0] = *(const u32x2*)(x + (size_t)s * DM + n0[i]); xv[i][1] = *(const u32x2*)(x + (size_t)s * DM + n0[i] + 8);
    }
    asm volatile("" : "+v"(xv[0][0]), "+v"(xv[0][1]), "+v"(xv[1][0]), "+v"(xv[1][1]));
    if (do5) mlp_wstage<true, 0, 16>(lds0, w1b, voffW, wid, 0);
    if (wid == 0) wait_word(flags + FL_P3 + 64 * panel, 16u, flags + FL_TMO);
    BAR();
    u32x4 sv[2][4];
    const char* sl = ws + WS_SLAB;
#pragma unroll
    for (int i = 0; i < 2; ++i)
#pragma unroll
      for (int k = 0; k < 4; ++k) GLOAD1_SC1(sv[i][k], so[i], sl + (size_t)k * (64 * 20 * 2 * 1024), 0);
    WAITV_TIE4(0, sv[0]); WAITV_TIE4(0, sv[1]);
    float hv[2][8];
    unsigned pk[2][4];
    float sum = 0.f, ssq = 0.f;
#pragma unroll
    for (int i = 0; i < 2; ++i) {
      const bool ok = pc + 64 * i < 80;
#pragma unroll
      for (int e = 0; e < 4; ++e) {
        const float lo = (bflo(sv[i][0][e]) + bflo(sv[i][1][e])) + (bflo(sv[i][2][e]) + bflo(sv[i][3][e]));
        const float hi = (bfhi(sv[i][0][e]) + bfhi(sv[i][1][e])) + (bfhi(sv[i][2][e]) + bfhi(sv[i][3][e]));
        const unsigned xw_ = xv[i][e >> 1][e & 1];
        pk[i][e] = cvtpk(lo + bflo(xw_), hi + bfhi(xw_));
        hv[i][2 * e] = ok ? bflo(pk[i][e]) : 0.f; hv[i][2 * e + 1] = ok ? bfhi(pk[i][e]) : 0.f;
        sum += hv[i][2 * e] + hv[i][2 * e + 1];
      }
    }
    sum += __shfl_xor(sum, 8); sum += __shfl_xor(sum, 16); sum += __shfl_xor(sum, 32);
    const float cntw = (wid < 2) ? 128.f : 64.f;
    const float mw = sum / cntw;
#pragma unroll
    for (int i = 0; i < 2; ++i) {
      if (pc + 64 * i < 80) {
#pragma unroll
        for (int e = 0; e < 8; ++e) { const float d = hv[i][e] - mw; ssq += d * d; }
      }
    }
    ssq += __shfl_xor(ssq, 8); ssq += __shfl_xor(ssq, 16); ssq += __shfl_xor(ssq, 32);
    if (lane < 8) { red[(wid * 8 + lane) * 2] = mw; red[(wid * 8 + lane) * 2 + 1] = ssq; }
    WAIT_L(0); BAR();
    float mean = 0.f;
#pragma unroll
    for (int w = 0; w < 8; ++w) mean += red[(w * 8 + rr) * 2] * (w < 2 ? 128.f : 64.f);
    mean *= (1.0f / DM);
    float m2 = 0.f;
#pragma unroll
    for (int w = 0; w < 8; ++w) { const float dm = red[(w * 8 + rr) * 2] - mean; m2 += red[(w * 8 + rr) * 2 + 1] + dm * dm * (w < 2 ? 128.f : 64.f); }
    const float rstd = 1.0f / sqrtf(m2 * (1.0f / DM) + LN_EPS);
#pragma unroll
    for (int i = 0; i < 2; ++i) {
      if (pc + 64 * i < 80) {
        LAS u16* t16 = (LAS u16*)tr;
#pragma unroll
        for (int e = 0; e < 4; ++e) {
          const unsigned p = cvtpk((hv[i][2 * e] - mean) * rstd, (hv[i][2 * e + 1] - mean) * rstd);
          const int n = n0[i] + 8 * (e >> 1) + 2 * (e & 1);
          t16[n * 8 + rr] = (u16)(p & 0xffff); t16[(n + 1) * 8 + rr] = (u16)(p >> 16);
        }
      }
    }
    WAIT_L(0); BAR();
    const char* hdst = ws + WS_HNF + ((size_t)st * 40 * 64 + ((s >> 3) & 3) * 16) * 16;
    { const int d = tid; const u32x4 tv4 = *(const LAS u32x4*)(tr + d * 16); gstore_sc1_u((unsigned)(((d >> 4) * 64 + (d & 15)) * 16), tv4, hdst); }
    if (tid < 128) { const int d = 512 + tid; const u32x4 tv4 = *(const LAS u32x4*)(tr + d * 16); gstore_sc1_u((unsigned)(((d >> 4) * 64 + (d & 15)) * 16), tv4, hdst); }
#pragma unroll
    for (int i = 0; i < 2; ++i) {
      if (pc + 64 * i < 80) {
        unsigned long long* hp = (unsigned long long*)(ws + WS_H + ((size_t)s * DM + n0[i]) * 2);
        __hip_atomic_store(hp, ((unsigned long long)pk[i][1] << 32) | pk[i][0], __ATOMIC_RELAXED, __HIP_MEMORY_SCOPE_AGENT);
        __hip_atomic_store(hp + 2, ((unsigned long long)pk[i][3] << 32) | pk[i][2], __ATOMIC_RELAXED, __HIP_MEMORY_SCOPE_AGENT);
      }
    }
    if (do5) {
#pragma unroll
      for (int s4 = 0; s4 < 4; ++s4) mlp_wrd(wp, s4 * 32768, wf + 4 * s4);
    }
    WAIT_V(0); WAIT_L(0); BAR();
    if (tid == 0) __hip_atomic_fetch_add(flags + FL_H + 64 * (b & 7), 1u, __ATOMIC_RELAXED, __HIP_MEMORY_SCOPE_AGENT);
    if (do5) mlp_wstage<true, 16, 16>(lds0, w1b, voffW, wid, 0);
  }
  constexpr int RHI = 81920;
  {
    const int lane = lane_id(), tid = wid * 64 + lane;
    const char* w2b = (const char*)w2 + wt6 * 128;
    const unsigned voffW2 = mlp_voffw<false>(wid, lane);
    if (do5) {
      if (wid == 0) {
        for (unsigned spins = 0;; ++spins) {
          const unsigned v = __hip_atomic_load(flags + FL_H + 64 * (lane & 7), __ATOMIC_RELAXED, __HIP_MEMORY_SCOPE_AGENT);
          if (__all(v >= 32u)) break;
          if (spins > (1u << 16)) { __hip_atomic_store(flags + FL_TMO, 1u, __ATOMIC_RELAXED, __HIP_MEMORY_SCOPE_AGENT); break; }
          __builtin_amdgcn_s_sleep(1);
        }
      }
      WAIT_V(0); WAIT_L(0); BAR();
      f32x4 acc[4][5];
      { const MlpW wp = mlp_wptr(lds, wid, lane); mlp_main<true, 0, 4, 16, 16>(lds, ws + WS_HNF + (size_t)(5 * dg5) * 1024, wid, lane, wp, wf, acc); }
      BAR();
      mlp_park(lds, wid, lane, acc);
      {
        float v[2][8]; u32x4 bv[2];
#pragma unroll
        for (int k2 = 0; k2 < 2; ++k2) {
          const int it = k2 == 0 ? tid : (tid < 128 ? 512 + tid : tid);
          const int d15 = it & 15, gp = (it >> 4) & 3, fr = it >> 6, dt = fr % 5, Tp = fr / 5;
          bv[k2] = *(const u32x4*)(b1 + 64 * wt5 + 32 * Tp + 8 * gp);
          mlp_gather(lds, 2 * Tp + (gp >> 1), dt, 2 * (gp & 1), d15, v[k2]);
        }
#pragma unroll
        for (int k2 = 0; k2 < 2; ++k2) {
          if (k2 == 0 || tid < 128) {
            const int it = k2 == 0 ? tid : 512 + tid;
            const int d15 = it & 15, gp = (it >> 4) & 3, fr = it >> 6, dt = fr % 5, Tp = fr / 5;
            u32x4 o;
#pragma unroll
            for (int e = 0; e < 4; ++e) {
              float z[2] = {v[k2][2 * e] + bflo(bv[k2][e]), v[k2][2 * e + 1] + bfhi(bv[k2][e])};
#pragma unroll
              for (int k = 0; k < 2; ++k) {
                const float xx = z[k];
                const float a = xx * (1.0f + 0.044715f * xx * xx) * (-2.0f * 0.7978845608028654f * 1.4426950408889634f);
                z[k] = xx * __builtin_amdgcn_rcpf(1.0f + __builtin_amdgcn_exp2f(a));
              }
              o[e] = cvtpk(z[0], z[1]);
            }
            gstore_sc1_u((unsigned)((((2 * wt5 + Tp) * 40 + 5 * dg5 + dt) * 64 + gp * 16 + d15) * 16), o, ws + WS_UF);
          }
        }
      }
      mlp_wstage<false, 20, 8>(lds0, w2b, voffW2, wid, RHI);
      WAIT_V(8); WAIT_L(0); BAR();
      if (tid == 0) __hip_atomic_fetch_add(flags + FL_U + 64 * dg5, 1u, __ATOMIC_RELAXED, __HIP_MEMORY_SCOPE_AGENT);
    } else {
      WAIT_L(0); BAR();
      mlp_wstage<false, 20, 8>(lds0, w2b, voffW2, wid, RHI);
    }
  }
  {
    const int lane = lane_id(), tid = wid * 64 + lane;
    bf16x8 wg[8];
    const MlpW wp = mlp_wptr(lds, wid, lane);
    const char* w2b = (const char*)w2 + wt6 * 128;
    const unsigned voffW2 = mlp_voffw<false>(wid, lane);
    mlp_wstage<false, 0, 10>(lds0, w2b, voffW2, wid, 0);
    WAIT_V(10); BAR();
    mlp_wrd(wp, RHI, wg); mlp_wrd(wp, RHI + 32768, wg + 4);
    WAIT_L(0); BAR();
    mlp_wstage<false, 10, 10>(lds0, w2b, voffW2, wid, RHI);
    if (wid == 0) wait_word(flags + FL_U + 64 * dg6, 27u, flags + FL_TMO);
    WAIT_V(0); BAR();
    const char* ab = ws + WS_UF + (size_t)(5 * dg6) * 1024;
    f32x4 acc[4][5];
    mlp_main<false, 5, 7, 0, 8>(lds, ab, wid, lane, wp, wg, acc);
    u32x4 hv[2]; float bb[2]; size_t oo[2];
#pragma unroll
    for (int k2 = 0; k2 < 2; ++k2) {
      const int it = k2 == 0 ? tid : (tid < 128 ? 512 + tid : tid);
      const int sl = it / 10, c = it % 10, srow = 64 * wt6 + sl;
      oo[k2] = (size_t)srow * DM + 80 * dg6 + 8 * c;
      GLOAD1_SC1(hv[k2], (unsigned)(oo[k2] * 2), ws + WS_H, 0);
      bb[k2] = bflo((unsigned)b2[srow]);
    }
    BAR();
    mlp_park(lds, wid, lane, acc);
    float v[2][8];
#pragma unroll
    for (int k2 = 0; k2 < 2; ++k2) {
      const int it = k2 == 0 ? tid : (tid < 128 ? 512 + tid : tid);
      const int sl = it / 10, c = it % 10;
      mlp_gather(lds, sl >> 4, c >> 1, 2 * (c & 1), sl & 15, v[k2]);
    }
    asm volatile("s_waitcnt vmcnt(0)" : "+v"(hv[0]), "+v"(hv[1]) :: "memory");
#pragma unroll
    for (int k2 = 0; k2 < 2; ++k2) {
      if (k2 == 0 || tid < 128) {
        u32x4 ov;
#pragma unroll
        for (int e = 0; e < 4; ++e) ov[e] = cvtpk(v[k2][2 * e] + bb[k2] + bflo(hv[k2][e]), v[k2][2 * e + 1] + bb[k2] + bfhi(hv[k2][e]));
        *(u32x4*)(out + oo[k2]) = ov;
      }
    }
  }
}

constexpr int CODE_BYTES = 106496;
constexpr int qproj_ns(int c, int cls) {
  constexpr int tab[4][10] = {{3, 3, 3, 3, 2, 2, 0, 0, 0, 0}, {1, 2, 2, 2, 3, 3, 3, 0, 0, 0}, {0, 1, 1, 2, 2, 2, 2, 3, 3, 0}, {0, 0, 1, 1, 1, 2, 2, 3, 3, 3}};
  return (c < 0 || c > 9) ? 0 : tab[cls][c];
}
constexpr int qproj_sbase(int c, int cls) { int n = 0; for (int i = 0; i < c; ++i) n += qproj_ns(i, cls); return n; }
constexpr int qproj_nl(int c) { return (c + 2 < 10 ? 4 : 0) + (c + 3 < 10 ? 2 : 0); }
constexpr int qproj_allowed(int c, int cls) {
  return c == 0 ? 6 : (c == 1 ? qproj_nl(0) + qproj_ns(0, cls) : qproj_ns(c - 2, cls) + qproj_nl(c - 1) + qproj_ns(c - 1, cls));
}
__global__ __launch_bounds__(512, 2) void kF1(const u16* __restrict__ ckv, const u16* __restrict__ wuk, const u16* __restrict__ wuv, const u16* __restrict__ wq,
                                              const u16* __restrict__ wo, const u16* __restrict__ w1, const u16* __restrict__ w2,
                                              const u16* __restrict__ x, const u16* __restrict__ b1, const u16* __restrict__ b2, char* __restrict__ ws, u16* __restrict__ out) {
  extern __shared__ __attribute__((aligned(16))) unsigned char smem[];
  const int bidx = blockIdx.x, xcd = bidx & 7, rr_ = bidx >> 3;
  const int h = xcd * 4 + (rr_ >> 3), j = rr_ & 7;
  const int wid = rfl(threadIdx.x >> 6);
  ldsp lds = (ldsp)smem;
  const unsigned lds0 = (unsigned)(uintptr_t)smem;
  unsigned* flags = (unsigned*)(ws + WS_FLAGS);
  const char* code0 = (const char*)__builtin_amdgcn_s_getpc();
  unsigned cpf = 0;
#define CODE_TOUCH() do { const unsigned ln_ = (unsigned)rr_ * 32u + (threadIdx.x & 63u); if (wid == 7 && (threadIdx.x & 63u) < 32u && ln_ * 128u < (unsigned)CODE_BYTES) \
    asm volatile("global_load_dword %0, %1, off" : "+v"(cpf) : "v"(code0 + (size_t)ln_ * 128) : "memory"); } while (0)
  CODE_TOUCH();
  bf16x8 kvp[16];
  {
    const int wr = wid >> 2, wc = wid & 3;
    const int lane = threadIdx.x & 63, l31 = lane & 31, hh = lane >> 5;
    unsigned voffC, voffW;
    { const int row = 8 * wid + (lane >> 3); voffC = (unsigned)(row * 1024 + (((lane & 7) ^ ((row >> 1) & 7)) << 4)); }
    { const int row = 8 * (wid >> 1) + ((lane >> 2) & 7); const int chq = 2 * (wid & 1) + (lane >> 5);
      const int ch = 4 * chq + ((lane & 3) ^ ((row >> 2) & 3)); voffW = (unsigned)(row * 8192 + ch * 16); }
    const char* cbase = (const char*)ckv + (size_t)(256 * j) * 1024;
    const char* wkb = (const char*)wuk + h * 256;
    const char* wvb = (const char*)wuv + h * 256;
    const unsigned dstw = lds0 + wid * 1024;
#define CBUF(b, a) (((b) * 2 + (a)) * 16384)
#define WBUF(b, a) (65536 + ((b) * 2 + (a)) * 16384)
#define STAGE_C(b, a, kt) do { const char* s_ = cbase + (a) * 131072 + (kt) * 128; glds16(voffC, s_, dstw + CBUF(b, a)); glds16(voffC, s_ + 65536, dstw + CBUF(b, a) + 8192); } while (0)
#define STAGE_W(b, a, kt) do { const char* s_ = ((a) ? wvb : wkb) + (size_t)(kt) * 64 * 8192; glds16(voffW, s_, dstw + WBUF(b, a)); glds16(voffW, s_ + 32 * 8192, dstw + WBUF(b, a) + 8192); } while (0)
    int aoff[4], boff[2];
#pragma unroll
    for (int s = 0; s < 4; ++s) aoff[s] = 128 * (wr * 64 + l31) + (((2 * s + hh) ^ ((l31 >> 1) & 7)) << 4);
    { const int blk = (lane >> 4) & 1, q = (lane & 15) >> 2, p = lane & 3;
#pragma unroll
      for (int t = 0; t < 2; ++t) boff[t] = 2048 * hh + 512 * wc + 64 * (4 * t + q) + (((2 * blk + (p >> 1)) ^ ((2 * hh + t) & 3)) << 4) + 8 * (p & 1); }
#define LDA(dst, b, a) do { _Pragma("unroll") for (int m = 0; m < 2; ++m) _Pragma("unroll") for (int s = 0; s < 4; ++s) dst[m][s] = ldsr128(lds + CBUF(b, a) + m * 4096 + aoff[s]); } while (0)
#define LDB(dst, b, a) do { _Pragma("unroll") for (int s = 0; s < 4; ++s) dst[s] = cat8(vtr(lds + WBUF(b, a) + s * 4096 + boff[0]), vtr(lds + WBUF(b, a) + s * 4096 + boff[1])); } while (0)
#define MMAK(ai, At, Bt) do { __builtin_amdgcn_s_setprio(1); _Pragma("unroll") for (int s = 0; s < 4; ++s) _Pragma("unroll") for (int m = 0; m < 2; ++m) \
    accK[ai][m] = MFMA32(Bt[s], At[m][s], accK[ai][m]); __builtin_amdgcn_s_setprio(0); } while (0)
#define MMAV(ai, At, Bt) do { __builtin_amdgcn_s_setprio(1); _Pragma("unroll") for (int s = 0; s < 4; ++s) _Pragma("unroll") for (int m = 0; m < 2; ++m) \
    accV[ai][m] = MFMA32(At[m][s], Bt[s], accV[ai][m]); __builtin_amdgcn_s_setprio(0); } while (0)
    f32x16 accK[2][2] = {}, accV[2][2] = {};
    bf16x8 At[2][4], B0[4], B1[4];
    constexpr int nt = DC / 64;
    STAGE_W(0, 0, 0); STAGE_C(0, 0, 0); STAGE_W(0, 1, 0); STAGE_C(0, 1, 0);
    if (wr == 1) BAR();
    WAIT_V(4); BAR();
    STAGE_W(1, 0, 1); STAGE_C(1, 0, 1); STAGE_W(1, 1, 1);
    WAIT_V(6); BAR();
    unsigned pfd = 0;
    if (wid < 3) {
      const int ln = wid * 64 + lane;
      if (ln < 160) asm volatile("global_load_dword %0, %1, off" : "+v"(pfd) : "v"((const char*)wq + (size_t)(80 * j + (ln >> 1)) * 8192 + h * 256 + (ln & 1) * 128) : "memory");
    }
    for (int t = 0; t < nt - 2; t += 2) {
      LDB(B0, 0, 0); SBAR(); LDA(At, 0, 0); STAGE_C(1, 1, t + 1);
      WAIT_L(8); BAR(); WAIT_L(0); MMAK(0, At, B0); BAR(); SBAR();
      LDB(B1, 0, 1); STAGE_W(0, 0, t + 2);
      BAR(); WAIT_L(0); MMAV(0, At, B1); BAR();
      LDA(At, 0, 1); STAGE_C(0, 0, t + 2);
      BAR(); WAIT_L(0); MMAK(1, At, B0); BAR(); SBAR();
      STAGE_W(0, 1, t + 2);
      WAIT_V(6); BAR(); MMAV(1, At, B1); BAR();
      LDB(B0, 1, 0); SBAR(); LDA(At, 1, 0); STAGE_C(0, 1, t + 2);
      WAIT_L(8); BAR(); WAIT_L(0); MMAK(0, At, B0); BAR(); SBAR();
      LDB(B1, 1, 1); STAGE_W(1, 0, t + 3);
      BAR(); WAIT_L(0); MMAV(0, At, B1); BAR();
      LDA(At, 1, 1); STAGE_C(1, 0, t + 3);
      BAR(); WAIT_L(0); MMAK(1, At, B0); BAR(); SBAR();
      STAGE_W(1, 1, t + 3);
      WAIT_V(6); BAR(); MMAV(1, At, B1); BAR();
    }
    {
      const char* wqb_ = (const char*)wq + h * 256;
      glds16_c<0>(voffW, wqb_, lds0 + 131072 + wid * 1024); glds16_c<32 * 8192>(voffW, wqb_, lds0 + 131072 + 8192 + wid * 1024);
      glds16_c<64 * 8192>(voffW, wqb_, lds0 + 147456 + wid * 1024); glds16_c<96 * 8192>(voffW, wqb_, lds0 + 147456 + 8192 + wid * 1024); }
    { LDB(B0, 0, 0); WAIT_V(0); LDA(At, 0, 0); STAGE_C(1, 1, nt - 1);
      BAR(); WAIT_L(0); MMAK(0, At, B0); BAR();
      LDB(B1, 0, 1); BAR(); WAIT_L(0); MMAV(0, At, B1); BAR();
      LDA(At, 0, 1); WAIT_V(4); BAR(); WAIT_L(0); MMAK(1, At, B0); MMAV(1, At, B1); BAR(); }
    { LDB(B0, 1, 0); LDA(At, 1, 0); WAIT_V(2); BAR(); WAIT_L(0); MMAK(0, At, B0); BAR();
      LDB(B1, 1, 1); WAIT_V(0); BAR(); WAIT_L(0); MMAV(0, At, B1); BAR();
      LDA(At, 1, 1); BAR(); WAIT_L(0); MMAK(1, At, B0); MMAV(1, At, B1); BAR(); }
    if (wr == 0) BAR();
    asm volatile("" :: "v"(pfd), "v"(cpf));
#pragma unroll
    for (int ai = 0; ai < 2; ++ai)
#pragma unroll
      for (int m = 0; m < 2; ++m)
#pragma unroll
        for (int sp = 0; sp < 2; ++sp) {
          kvp[((ai * 2 + m) * 2 + sp) * 2] = pack8(accK[ai][m], sp);
          kvp[((ai * 2 + m) * 2 + sp) * 2 + 1] = pack8(accV[ai][m], sp);
          PIN(kvp[((ai * 2 + m) * 2 + sp) * 2]); PIN(kvp[((ai * 2 + m) * 2 + sp) * 2 + 1]);
        }
#undef CBUF
#undef WBUF
#undef STAGE_C
#undef STAGE_W
#undef LDA
#undef LDB
#undef MMAK
#undef MMAV
  }
  SBAR();
  bf16x8 qr[8];
  unsigned kvmask = 0;
  const int lane = lane_id(), tid = wid * 64 + lane;
  const unsigned vl16 = lane * 16;
  {
    const int hh = lane >> 5;
    const char* Kh = ws + WS_KF + (size_t)h * 64 * 8192;
    const char* Vh = ws + WS_VF + (size_t)h * 128 * 4096;
    const unsigned vt2 = (unsigned)(wid * 2048 + lane * 16);
    const unsigned dt2 = lds0 + wid * 2048;
    const int n1 = j >= 4 ? 16 : 4 * j + 2;
#define TILE(g) ((g) < n1 ? (g) : 16 + (g) - n1)
#define DMA_T(src, ldsoff) do { unsigned keep_; asm volatile("s_mov_b32 %0, m0\n\ts_mov_b32 m0, %3\n\ts_nop 2\n\tglobal_load_lds_dwordx4 %1, %2 sc1\n\tglobal_load_lds_dwordx4 %1, %2 offset:1024 sc1\n\ts_mov_b32 m0, %0" \
    : "=&s"(keep_) : "v"(vt2), "s"(src), "s"(dt2 + (ldsoff)) : "memory"); } while (0)
    const int wr = wid >> 2, wc = wid & 3, cls = j & 3;
    const char* kst = ws + WS_KF + ((size_t)(h * 64 + 8 * j + 2 * wr) * 8 + 2 * wc) * 1024;
    const char* vst = ws + WS_VF + ((size_t)(h * 128 + 2 * (8 * j + 2 * wr)) * 4 + wc) * 1024;
#define KVST(i) gstore_sc1_c<(((i) & 1) ? (((i) >> 3) * 8 + (((i) >> 2) & 1) * 2 + (((i) >> 1) & 1)) * 4096 : (((i) >> 3) * 4 + (((i) >> 2) & 1)) * 8192 + (((i) >> 1) & 1) * 1024)>(vl16, kvp[i], ((i) & 1) ? vst : kst)
    unsigned voffW;
    { const int row = 8 * (wid >> 1) + ((lane >> 2) & 7); const int chq = 2 * (wid & 1) + (lane >> 5);
      const int ch = 4 * chq + ((lane & 3) ^ ((row >> 2) & 3)); voffW = (unsigned)(row * 8192 + ch * 16); }
    const char* wqb = (const char*)wq + h * 256;
    const unsigned dstw = lds0 + L_Q + wid * 1024;
#define STAGE_Q1(c, i) glds16_c<(c) * 64 * 8192 + (i) * 32 * 8192>(voffW, wqb, dstw + (((c) + 2) & 3) * 16384 + (i) * 8192)
    const char* xb = ws + WS_XNF + (size_t)(8 * j + wid) * 40 * 1024;
    int boff[2];
    { const int blk = (lane >> 4) & 1, q = (lane & 15) >> 2, p = lane & 3;
#pragma unroll
      for (int t = 0; t < 2; ++t) boff[t] = L_Q + 2048 * hh + 64 * (4 * t + q) + (((2 * blk + (p >> 1)) ^ ((2 * hh + t) & 3)) << 4) + 8 * (p & 1); }
    u32x4 xf[3][4];
    f32x16 acc[4] = {};
    bf16x8 aq[6];
    unsigned fv = 0;
    ldscp bp0 = lds + boff[0], bp1 = lds + boff[1];
#define ARD(u) aq[(u) % 6] = cat8(vtr(bp0 + ((((u) >> 4) + 2) & 3) * 16384 + (((u) >> 2) & 3) * 4096 + ((u) & 3) * 512), vtr(bp1 + ((((u) >> 4) + 2) & 3) * 16384 + (((u) >> 2) & 3) * 4096 + ((u) & 3) * 512))
    GLOAD4(xf[0], vl16, xb);
    GLOAD4(xf[1], vl16, xb + 4096); STAGE_Q1(2, 0); STAGE_Q1(2, 1);
#pragma unroll
    for (int u = 0; u < 5; ++u) ARD(u);
    static_for<10>([&](auto cc_) {
      constexpr int c = decltype(cc_)::value;
#define QW(K) WAITVN(qproj_allowed(c, K))
      if (cls == 0) QW(0); else if (cls == 1) QW(1); else if (cls == 2) QW(2); else QW(3);
      asm volatile("" : "+v"(xf[c % 3][0]), "+v"(xf[c % 3][1]), "+v"(xf[c % 3][2]), "+v"(xf[c % 3][3]) :: "memory");
#undef QW
      if (c == 9 && wid == 0) {
        asm volatile("s_waitcnt vmcnt(0)" : "+v"(fv) :: "memory");
        kvmask = (unsigned)__builtin_amdgcn_ballot_w64(fv != 0u) & 0xffu;
        if (!(kvmask & 1u)) { wait_word(flags + FL_KV + 16 * (8 * h), 1u, flags + FL_TMO); kvmask |= 1u; }
        if (j == 0 && !(kvmask & 16u)) { wait_word(flags + FL_KV + 16 * (8 * h + 4), 1u, flags + FL_TMO); kvmask |= 16u; }
      }
      SBAR(); BAR();
      if (tid == 0 && ((cls == 0 && c == 8) || (cls == 1 && c == 9)))
        __hip_atomic_store(flags + FL_KV + 16 * (8 * h + j), 1u, __ATOMIC_RELAXED, __HIP_MEMORY_SCOPE_AGENT);
      SBAR();
      const char* xn_ = xb + (c + 2) * 4096;
      static_for<16>([&](auto ii_) {
        constexpr int i = decltype(ii_)::value;
        constexpr int u = 16 * c + i;
        acc[i & 3] = MFMA32(aq[u % 6], __builtin_bit_cast(bf16x8, xf[c % 3][i >> 2]), acc[i & 3]);
        if constexpr (u + 5 < 160) ARD(u + 5);
        if (c + 2 < 10) {
          if (i == 1) GLOAD1(xf[(c + 2) % 3][0], vl16, xn_, 0);
          if (i == 3) GLOAD1(xf[(c + 2) % 3][1], vl16, xn_, 1024);
          if (i == 5) GLOAD1(xf[(c + 2) % 3][2], vl16, xn_, 2048);
          if (i == 7) GLOAD1(xf[(c + 2) % 3][3], vl16, xn_, 3072);
        }
        if constexpr (c + 3 < 10) { if constexpr (i == 8) STAGE_Q1(c + 3, 0); if constexpr (i == 9) STAGE_Q1(c + 3, 1); }
        if constexpr (i >= 10) {
          static_for<4>([&](auto kk_) {
            constexpr int k = decltype(kk_)::value;
            constexpr int cnt = qproj_ns(c, k), base = qproj_sbase(c, k);
            if constexpr (((i - 10) & 1) == 0 && (i - 10) / 2 < cnt) { if (cls == k) KVST(base + (i - 10) / 2); }
          });
        }
        if (c == 8 && i == 8 && wid == 0)
          asm volatile("s_nop 4\n\tglobal_load_dword %0, %1, %2 sc1" : "+v"(fv) : "v"((unsigned)(FL_KV + 16 * (8 * h + (lane & 7))) * 4u), "s"(flags) : "memory");
        if (c == 9) {
          if (i == 0) DMA_T(Kh + (size_t)TILE(0) * 16384, L_K);
          if (i == 2) DMA_T(Vh + (size_t)TILE(0) * 16384, L_V);
          if (i == 4) DMA_T(Kh + (size_t)TILE(1) * 16384, L_K + 16384);
          if (i == 6) DMA_T(Vh + (size_t)TILE(1) * 16384, L_V + 16384);
          if (i == 8) DMA_T(Kh + (size_t)TILE(2) * 16384, L_K + 32768);
          if (i == 9) DMA_T(Kh + (size_t)TILE(3) * 16384, L_K + 49152);
        }
        SBAR();
      });
    });
#pragma unroll
    for (int dt = 0; dt < 4; ++dt) { acc[dt] = acc[dt] * QSCALE; qr[2 * dt] = pack8(acc[dt], 0); qr[2 * dt + 1] = pack8(acc[dt], 1); }
    if (cls == 3) WAIT_V(13); else WAIT_V(10);
    BAR();
    { const char* qst = ws + WS_QF + (size_t)(h * 64 + 8 * j + wid) * 8192;
      static_for<8>([&](auto ss_) { constexpr int s = decltype(ss_)::value; gstore_sc1_c<s * 1024>(vl16, qr[s], qst); }); }
#undef KVST
#undef STAGE_Q1
#undef ARD
#undef TILE
#undef DMA_T
  }
  const PfW pfw = {(const char*)wo, (const char*)w1, (const char*)w2};
  if (wid < 4) attn_main<0>(lds, lds0, ws, flags, h, j, wid, lane, tid, qr, kvmask, pfw);
  else attn_main<1>(lds, lds0, ws, flags, h, j, wid, lane, tid, qr, kvmask, pfw);
  WAIT_V(0); WAIT_L(0); BAR();
  if (tid == 0) __hip_atomic_fetch_add(flags + FL_C + 16 * ((7 - j) * 4 + (h >> 3)), 1u, __ATOMIC_RELAXED, __HIP_MEMORY_SCOPE_AGENT);
  tail_phase(smem, x, wo, w1, b1, w2, b2, ws, out, wid);
}

extern "C" void kernel_launch(void* const* d_in, const int* in_sizes, int n_in, void* d_out, int out_size, void* d_ws, size_t ws_size,
                              hipStream_t stream) {
  const u16* x = (const u16*)d_in[0];
  const u16* ckv = (const u16*)d_in[1];
  const u16* wuk = (const u16*)d_in[3];
  const u16* wuv = (const u16*)d_in[4];
  char* ws = (char*)d_ws;
  const u16* wq = (const u16*)d_in[2];
  const u16* wo = (const u16*)d_in[5];
  const u16* w1 = (const u16*)d_in[7];
  const u16* b1 = (const u16*)d_in[8];
  const u16* w2 = (const u16*)d_in[9];
  const u16* b2 = (const u16*)d_in[10];
  (void)hipFuncSetAttribute((const void*)kF1, hipFuncAttributeMaxDynamicSharedMemorySize, F1_LDS);
  hipLaunchKernelGGL(k0_ln, dim3(256), dim3(512), 0, stream, x, (u16*)(ws + WS_XNF), (unsigned*)(ws + WS_FLAGS));
  hipLaunchKernelGGL(kF1, dim3(256), dim3(512), F1_LDS, stream, ckv, wuk, wuv, wq, wo, w1, w2, x, b1, b2, ws, (u16*)d_out);
}
```

```cpp
#include <hip/hip_runtime.h>
#include <stdint.h>

#define LAS __attribute__((address_space(3)))
#define DI __device__ __forceinline__
typedef unsigned short u16;
typedef short bf16x8 __attribute__((ext_vector_type(8)));
typedef short s16x4 __attribute__((ext_vector_type(4)));
typedef float f32x2 __attribute__((ext_vector_type(2)));
typedef float f32x4 __attribute__((ext_vector_type(4)));
typedef float f32x16 __attribute__((ext_vector_type(16)));
typedef unsigned u32x2 __attribute__((ext_vector_type(2)));
typedef unsigned u32x4 __attribute__((ext_vector_type(4)));
typedef __bf16 bf16x2_t __attribute__((ext_vector_type(2)));
typedef LAS unsigned char* ldsp;
typedef LAS const unsigned char* ldscp;

constexpr int SEQ = 2048, DM = 640, DC = 512, NH = 32, HD = 128, FF = 1728;
constexpr float LN_EPS = 1e-5f;

constexpr size_t WS_FLAGS = 0;
constexpr size_t WS_XNF = 131072;
constexpr size_t WS_KF = WS_XNF + (size_t)SEQ * DM * 2;
constexpr size_t WS_VF = WS_KF + (size_t)NH * SEQ * HD * 2;
constexpr size_t WS_CTX = WS_VF + (size_t)NH * SEQ * HD * 2;
constexpr size_t WS_SLAB = WS_CTX + (size_t)NH * SEQ * HD * 2;
constexpr size_t WS_H = WS_SLAB + (size_t)4 * SEQ * DM * 4;
constexpr size_t WS_HNF = WS_H + (size_t)SEQ * DM * 2;
constexpr size_t WS_UF = WS_HNF + (size_t)SEQ * DM * 2;
constexpr size_t WS_QF = WS_UF + (size_t)DM * FF * 2;
constexpr size_t WS_PF = WS_QF + (size_t)NH * SEQ * HD * 2;
constexpr size_t WS_PS = WS_PF + (size_t)NH * SEQ * HD * 2;
constexpr size_t WS_END = WS_PS + (size_t)NH * 8 * 8 * 64 * 8;
constexpr int FL_TMO = 0;
constexpr int FL_KV = 24576;
constexpr int FL_Q = 4096;
constexpr int FL_P = 4096 + 8192;
constexpr int FL_WORDS = 32768;

#define SBAR() __builtin_amdgcn_sched_barrier(0)
#define BAR() asm volatile("s_barrier" ::: "memory")
#define WAIT_V(n) asm volatile("s_waitcnt vmcnt(" #n ")" ::: "memory")
#define WAIT_L(n) asm volatile("s_waitcnt lgkmcnt(" #n ")" ::: "memory")
#define MFMA32(a, b, c) __builtin_amdgcn_mfma_f32_32x32x16_bf16((a), (b), (c), 0, 0, 0)
#define MFMA16(a, b, c) __builtin_amdgcn_mfma_f32_16x16x32_bf16((a), (b), (c), 0, 0, 0)

DI unsigned cvtpk(float lo, float hi) { f32x2 v = {lo, hi}; bf16x2_t b = __builtin_convertvector(v, bf16x2_t); return __builtin_bit_cast(unsigned, b); }
DI float bflo(unsigned u) { return __uint_as_float(u << 16); }
DI float bfhi(unsigned u) { return __uint_as_float(u & 0xffff0000u); }
DI unsigned rfl(unsigned x) { return __builtin_amdgcn_readfirstlane(x); }
DI void glds16(unsigned voff, const void* sbase, unsigned lds_dst) {
  unsigned keep;
  asm volatile("s_mov_b32 %0, m0\n\ts_mov_b32 m0, %3\n\ts_nop 2\n\tglobal_load_lds_dwordx4 %1, %2\n\ts_mov_b32 m0, %0"
               : "=&s"(keep) : "v"(voff), "s"(sbase), "s"(lds_dst) : "memory");
}
DI void glds16x4(unsigned voff, const void* sbase, unsigned lds_dst) {
  unsigned keep;
  asm volatile("s_mov_b32 %0, m0\n\ts_mov_b32 m0, %3\n\ts_nop 2\n\tglobal_load_lds_dwordx4 %1, %2\n\tglobal_load_lds_dwordx4 %1, %2 offset:1024\n\t"
               "global_load_lds_dwordx4 %1, %2 offset:2048\n\tglobal_load_lds_dwordx4 %1, %2 offset:3072\n\ts_mov_b32 m0, %0"
               : "=&s"(keep) : "v"(voff), "s"(sbase), "s"(lds_dst) : "memory");
}
typedef short v4i16_t __attribute__((ext_vector_type(4)));
DI s16x4 vtr(ldscp p) { return __builtin_bit_cast(s16x4, __builtin_amdgcn_ds_read_tr16_b64_v4i16((LAS v4i16_t*)p)); }
DI bf16x8 cat8(s16x4 lo, s16x4 hi) { return __builtin_shufflevector(lo, hi, 0, 1, 2, 3, 4, 5, 6, 7); }
DI bf16x8 ldsr128(ldscp p) { return *(const LAS bf16x8*)p; }
DI bf16x8 pack8(const f32x16& x, int s) {
  u32x4 w = {cvtpk(x[8 * s], x[8 * s + 1]), cvtpk(x[8 * s + 2], x[8 * s + 3]), cvtpk(x[8 * s + 4], x[8 * s + 5]), cvtpk(x[8 * s + 6], x[8 * s + 7])};
  return __builtin_bit_cast(bf16x8, w);
}
DI float wave_sum(float v) {
#pragma unroll
  for (int o = 32; o >= 1; o >>= 1) v += __shfl_xor(v, o);
  return v;
}

constexpr int K0_TOUCH_BYTES = 73728;
__global__ __launch_bounds__(512) void k0_ln(const u16* __restrict__ x, u16* __restrict__ xnf, unsigned* __restrict__ flags) {
  const int w = threadIdx.x >> 6, lane = threadIdx.x & 63;
  const int q = blockIdx.x * 8 + w;
  unsigned cpf = 0;
  { const unsigned ln = (blockIdx.x >> 3) * 32u + (unsigned)lane;
    if (w == 0 && lane < 32 && ln * 128u < (unsigned)K0_TOUCH_BYTES)
      asm volatile("global_load_dword %0, %1, off" : "+v"(cpf) : "v"((const char*)__builtin_amdgcn_s_getpc() + (size_t)ln * 128) : "memory"); }
  if (threadIdx.x < 128) flags[blockIdx.x * 128 + threadIdx.x] = 0u;
  const u32x4* row = (const u32x4*)(x + (size_t)q * DM);
  u32x4 c0 = row[lane];
  u32x4 c1 = {0u, 0u, 0u, 0u};
  if (lane < 16) c1 = row[64 + lane];
  float v[16];
#pragma unroll
  for (int i = 0; i < 4; ++i) { v[2 * i] = bflo(c0[i]); v[2 * i + 1] = bfhi(c0[i]); v[8 + 2 * i] = bflo(c1[i]); v[8 + 2 * i + 1] = bfhi(c1[i]); }
  float s = 0.f;
#pragma unroll
  for (int i = 0; i < 16; ++i) s += v[i];
  const float mean = wave_sum(s) * (1.0f / DM);
  float ss = 0.f;
#pragma unroll
  for (int i = 0; i < 8; ++i) { const float d = v[i] - mean; ss += d * d; }
  if (lane < 16) {
#pragma unroll
    for (int i = 8; i < 16; ++i) { const float d = v[i] - mean; ss += d * d; }
  }
  const float rstd = 1.0f / sqrtf(wave_sum(ss) * (1.0f / DM) + LN_EPS);
  u32x4 o0, o1;
#pragma unroll
  for (int i = 0; i < 4; ++i) { o0[i] = cvtpk((v[2 * i] - mean) * rstd, (v[2 * i + 1] - mean) * rstd); o1[i] = cvtpk((v[8 + 2 * i] - mean) * rstd, (v[8 + 2 * i + 1] - mean) * rstd); }
  u32x4* dst = (u32x4*)xnf + (size_t)(q >> 5) * 40 * 64 + (q & 31);
  dst[(lane >> 1) * 64 + (lane & 1) * 32] = o0;
  if (lane < 16) dst[((64 + lane) >> 1) * 64 + (lane & 1) * 32] = o1;
  asm volatile("s_waitcnt vmcnt(0)" : "+v"(cpf) :: "memory");
}

constexpr int F1_LDS = 163840;
constexpr int L_K = 0, L_V = 65536, L_Q = 98304;
constexpr float QSCALE = 0.08838834764831845f * 1.4426950408889634f;
constexpr float THR = 16.f;
#define PIN(x) asm volatile("" : "+v"(x))
#define EX2(v) __builtin_amdgcn_exp2f(v)
#define MEMFENCE() asm volatile("" ::: "memory")
DI float max3f(float a, float b, float c) { return __builtin_fmaxf(__builtin_fmaxf(a, b), c); }
DI float xhalf_max(float m) { auto rr = __builtin_amdgcn_permlane32_swap(__float_as_uint(m), __float_as_uint(m), false, false); return __builtin_fmaxf(__uint_as_float(rr[0]), __uint_as_float(rr[1])); }
DI float xhalf_sum(float m) { auto rr = __builtin_amdgcn_permlane32_swap(__float_as_uint(m), __float_as_uint(m), false, false); return __uint_as_float(rr[0]) + __uint_as_float(rr[1]); }
DI float max16(const f32x16& p) {
  float a = max3f(p[0], p[1], p[2]), b = max3f(p[3], p[4], p[5]);
  a = max3f(a, p[6], p[7]); b = max3f(b, p[8], p[9]); a = max3f(a, p[10], p[11]); b = max3f(b, p[12], p[13]); a = max3f(a, p[14], p[15]);
  return __builtin_fmaxf(a, b);
}
DI void cmask(f32x16& p0, f32x16& p1, int dq) {
#pragma unroll
  for (int r = 0; r < 16; ++r) { const int c = (r & 3) + 8 * (r >> 2); if (c > dq) p0[r] = -__builtin_inff(); if (c + 32 > dq) p1[r] = -__builtin_inff(); }
}
#define GLOAD4(d, voff, sbase) asm volatile("s_nop 4\n\tglobal_load_dwordx4 %0, %4, %5\n\tglobal_load_dwordx4 %1, %4, %5 offset:1024\n\tglobal_load_dwordx4 %2, %4, %5 offset:2048\n\tglobal_load_dwordx4 %3, %4, %5 offset:3072" \
    : "=&v"(d[0]), "=&v"(d[1]), "=&v"(d[2]), "=&v"(d[3]) : "v"(voff), "s"(sbase) : "memory")
#define GLOAD1(d, voff, sbase, OFF) asm volatile("s_nop 4\n\tglobal_load_dwordx4 %0, %1, %2 offset:" #OFF : "=&v"(d) : "v"(voff), "s"(sbase) : "memory")
#define GLOAD1_SC1(d, voff, sbase, OFF) asm volatile("s_nop 4\n\tglobal_load_dwordx4 %0, %1, %2 offset:" #OFF " sc1" : "=&v"(d) : "v"(voff), "s"(sbase) : "memory")
#define WAITV_TIE4(n, d) asm volatile("s_waitcnt vmcnt(" #n ")" : "+v"(d[0]), "+v"(d[1]), "+v"(d[2]), "+v"(d[3]) :: "memory")
DI void gstore_sc1(unsigned voff, const bf16x8& v, const void* sbase) {
  asm volatile("s_nop 4\n\tglobal_store_dwordx4 %0, %1, %2 sc1\n\ts_nop 1" :: "v"(voff), "v"(v), "s"(sbase) : "memory");
}
template <int OFFS> DI void gstore_sc1_c(unsigned voff, const bf16x8& v, const void* sbase) {
  unsigned t;
  asm volatile("v_add_u32 %0, %4, %1\n\ts_nop 3\n\tglobal_store_dwordx4 %0, %2, %3 sc1\n\ts_nop 1" : "=&v"(t) : "v"(voff), "v"(v), "s"(sbase), "i"(OFFS) : "memory");
}
template <int OFFS> DI void glds16_c(unsigned voff, const void* sbase, unsigned lds_dst) {
  unsigned keep, t;
  asm volatile("s_mov_b32 %0, m0\n\ts_mov_b32 m0, %4\n\tv_add_u32 %1, %5, %2\n\ts_nop 1\n\tglobal_load_lds_dwordx4 %1, %3\n\ts_mov_b32 m0, %0"
               : "=&s"(keep), "=&v"(t) : "v"(voff), "s"(sbase), "s"(lds_dst), "i"(OFFS) : "memory");
}
#define GLDS_OFF(voff, sbase, ldst, OFF) do { unsigned keep_; asm volatile("s_mov_b32 %0, m0\n\ts_mov_b32 m0, %3\n\ts_nop 2\n\tglobal_load_lds_dwordx4 %1, %2 offset:" #OFF "\n\ts_mov_b32 m0, %0" \
    : "=&s"(keep_) : "v"(voff), "s"(sbase), "s"(ldst) : "memory"); } while (0)
#define WAITVN(n) asm volatile("s_waitcnt vmcnt(%0)" :: "i"(n) : "memory")
DI const char* upt(const char* p) {
  const unsigned long long v = (unsigned long long)p;
  return (const char*)(((unsigned long long)rfl((unsigned)(v >> 32)) << 32) | rfl((unsigned)v));
}
DI void wait_word(unsigned* word, unsigned want, unsigned* tmo) {
  for (unsigned spins = 0;; ++spins) {
    if ((unsigned)rfl(__hip_atomic_load(word, __ATOMIC_RELAXED, __HIP_MEMORY_SCOPE_AGENT)) >= want) break;
    if ((spins & 63u) == 63u) {
      if (spins > (1u << 13) || rfl(__hip_atomic_load(tmo, __ATOMIC_RELAXED, __HIP_MEMORY_SCOPE_AGENT)) != 0u) { __hip_atomic_store(tmo, 1u, __ATOMIC_RELAXED, __HIP_MEMORY_SCOPE_AGENT); break; }
    }
    __builtin_amdgcn_s_sleep(1);
  }
}
#define ACQUIRE() __builtin_amdgcn_fence(__ATOMIC_ACQUIRE, "agent")

template <int V> struct IC { static constexpr int value = V; };
template <int N, int I = 0, class F> DI void static_for(F&& f) { if constexpr (I < N) { f(IC<I>{}); static_for<N, I + 1>(f); } }
DI int lane_id() { unsigned l; asm volatile("v_mbcnt_lo_u32_b32 %0, -1, 0\n\tv_mbcnt_hi_u32_b32 %0, -1, %0" : "=v"(l)); return (int)l; }
struct PfW { const char* wo; const char* w1; const char* w2; };
template <int GRP>
DI void attn_main(ldsp lds, unsigned lds0, char* __restrict__ ws, unsigned* flags, int h, int j, int wid, int lane, int tid, bf16x8 (&qr)[8], unsigned kvmask, const PfW& pfw) {
  const int l31 = lane & 31, hh = lane >> 5;
  const unsigned vl16 = lane * 16;
  const char* Kh = ws + WS_KF + (size_t)h * 64 * 8192;
  const char* Vh = ws + WS_VF + (size_t)h * 128 * 4096;
  const unsigned vt2 = (unsigned)(wid * 2048 + lane * 16);
  const unsigned dt2 = lds0 + wid * 2048;
  const int n1 = j >= 4 ? 16 : 4 * j + 2;
  const int t2 = j >= 4 ? 4 * (8 - j) - 2 : 16;
  const int jp = 7 - j;
#define TILE(g) ((g) < n1 ? (g) : ((g) < 18 ? t2 + (g) - n1 : 0))
#define KSRC(g) (Kh + (size_t)TILE(g) * 16384)
#define VSRC(g) (Vh + (size_t)TILE(g) * 16384)
#define GLDS_T(src, dst, OFF) do { unsigned keep_; asm volatile("s_mov_b32 %0, m0\n\ts_mov_b32 m0, %3\n\ts_nop 2\n\tglobal_load_lds_dwordx4 %1, %2 offset:" #OFF " sc1\n\ts_mov_b32 m0, %0" \
    : "=&s"(keep_) : "v"(vt2), "s"(src), "s"(dst) : "memory"); } while (0)
  const unsigned stoff = lane < 8 ? (unsigned)(FL_KV + 16 * (8 * h + lane)) * 4u : (lane == 8 ? (unsigned)(FL_Q + 32 * (8 * h + jp)) * 4u : (unsigned)(FL_P + 32 * (8 * h + jp)) * 4u);
  unsigned fv = 0;
#define ST_ISSUE() do { if (wid == 0 && kvmask != 0x3ffu) asm volatile("s_nop 4\n\tglobal_load_dword %0, %1, %2 sc1" : "+v"(fv) : "v"(stoff), "s"(flags) : "memory"); } while (0)
#define ST_READ(N) do { if (wid == 0 && kvmask != 0x3ffu) { asm volatile("s_waitcnt vmcnt(" #N ")" : "+v"(fv) :: "memory"); kvmask |= (unsigned)__builtin_amdgcn_ballot_w64(fv != 0u) & 0x3ffu; } } while (0)
#define ENSURE_KV(g) do { if (wid == 0) { const int bk_ = TILE(g) >> 2; if (!((kvmask >> bk_) & 1u)) { wait_word(flags + FL_KV + 16 * (8 * h + bk_), 1u, flags + FL_TMO); kvmask |= 1u << bk_; } } } while (0)
  const int rdoff = lane * 16;
  bf16x8 kq[4], vq[4], pw[4];
  f32x16 C0, C1, o[4], negm;
  float mhat, lsum;
  const f32x16 z = {};
  unsigned pfd = 0;
#pragma unroll
  for (int i = 0; i < 4; ++i) kq[i] = ldsr128(lds + L_K + rdoff + i * 1024);
  {
    ST_ISSUE();
    ldscp kb = lds + L_K + rdoff;
#pragma unroll
    for (int u = 0; u < 16; ++u) {
      if (u < 8) C0 = MFMA32(kq[u & 3], qr[u & 7], u == 0 ? z : C0); else C1 = MFMA32(kq[u & 3], qr[u & 7], u == 8 ? z : C1);
      if (u + 4 < 16) kq[u & 3] = ldsr128(kb + (u + 4) * 1024);
      if (GRP == 1 && u == 11) {
        SBAR(); WAIT_V(0); WAIT_L(0); BAR();
        GLDS_T(VSRC(2), dt2 + L_V + 32768, 0); GLDS_T(VSRC(2), dt2 + L_V + 32768, 1024);
        GLDS_T(KSRC(4), dt2 + L_K, 0);
      }
      SBAR();
    }
    if (j == 0) cmask(C0, C1, 32 * wid + l31 - 4 * hh);
    float rm = xhalf_max(__builtin_fmaxf(max16(C0), max16(C1)));
    mhat = __builtin_fmaxf(rm, -1e30f);
#pragma unroll
    for (int r = 0; r < 16; ++r) { C0[r] -= mhat; C1[r] -= mhat; negm[r] = -mhat; }
    PIN(negm);
    if (GRP == 0) {
      SBAR(); WAIT_V(0); ST_READ(0); ENSURE_KV(2); ENSURE_KV(4);
      WAIT_L(0); BAR();
      if (tid == 0) {
        __hip_atomic_store(flags + FL_Q + 32 * (8 * h + j), 1u, __ATOMIC_RELAXED, __HIP_MEMORY_SCOPE_AGENT);
        if ((j & 3) >= 2) __hip_atomic_store(flags + FL_KV + 16 * (8 * h + j), 1u, __ATOMIC_RELAXED, __HIP_MEMORY_SCOPE_AGENT);
      }
      GLDS_T(VSRC(2), dt2 + L_V + 32768, 0);
    } else {
      GLDS_T(KSRC(4), dt2 + L_K, 1024);
    }
#pragma unroll
    for (int i = 0; i < 4; ++i) kq[i] = ldsr128(lds + L_K + 16384 + rdoff + i * 1024);
    float s0 = 0.f;
#pragma unroll
    for (int r = 0; r < 16; ++r) { C0[r] = EX2(C0[r]); s0 += C0[r]; }
#pragma unroll
    for (int r = 0; r < 8; ++r) C1[r] = EX2(C1[r]);
    lsum = s0;
    pw[0] = pack8(C0, 0); pw[1] = pack8(C0, 1);
#pragma unroll
    for (int d = 0; d < 4; ++d) o[d] = z;
  }
  const int ddm = (wid >> 1) + 1;
  const int gA = ddm > 3 ? 18 : (4 * jp + ddm - t2 > 0 ? n1 + 4 * jp + ddm - t2 : n1);
  auto stepg = [&](auto aq_, auto ap_, const int g) {
    constexpr bool AQ = decltype(aq_)::value != 0, AP = decltype(ap_)::value != 0;
    const bool seam = (g == n1);
    const int X = g < n1 ? j : jp;
    const int dd = TILE(g) - 4 * X;
    ldscp kb = lds + L_K + (g & 3) * 16384 + rdoff;
    ldscp vb = lds + L_V + ((g - 1) & 3) * 16384 + rdoff;
    ldscp kn = lds + L_K + ((g + 1) & 3) * 16384 + rdoff;
    const char* vs_n = VSRC(g + 2); const char* ks_n = KSRC(g + 4);
    const char* vs_p = VSRC(g + 1); const char* ks_p = KSRC(g + 3);
    const unsigned vd_n = dt2 + L_V + ((g + 2) & 3) * 16384, kd_n = dt2 + L_K + (g & 3) * 16384;
    const unsigned vd_p = dt2 + L_V + ((g + 1) & 3) * 16384, kd_p = dt2 + L_K + ((g - 1) & 3) * 16384;
    const bool gq = (GRP == 0) || g > 1;
    float lsum1 = 0.f, mhat1 = 0.f;
    if (seam) {
      mhat1 = mhat; mhat = 0.f;
#pragma unroll
      for (int r = 0; r < 16; ++r) negm[r] = 0.f;
      PIN(negm);
    }
    if (g >= 2 && g < 12 && wid == ((g - 2) & 7)) {
      const unsigned lc = (unsigned)(g - 2) * 64u + lane, gid = blockIdx.x * 592u + lc;
      const char* p = gid < 40960u ? pfw.wo + (size_t)gid * 128 : (gid < 96256u ? pfw.w1 + (size_t)(gid - 40960u) * 128 : pfw.w2 + (size_t)(gid - 96256u) * 128);
      if (lc < 592u && gid < 151552u) asm volatile("global_load_dword %0, %1, off" : "+v"(pfd) : "v"(p) : "memory");
    }
    ST_ISSUE();
    float sa = 0.f, sb = 0.f;
    SBAR();
#define KRD(u) kq[(u) & 3] = ldsr128(kb + ((u) + 4) * 1024)
#define VRD(v) vq[(v) & 3] = ldsr128(vb + (v) * 1024)
#define KPRE(i) kq[i] = ldsr128(kn + (i) * 1024)
#define BARRIER_G() do { if (GRP == 0) { ST_READ(3); ENSURE_KV(g + 4); if (wid == 0 && g == n1 - 2 && j != 0 && !(kvmask & 256u)) wait_word(flags + FL_Q + 32 * (8 * h + jp), 1u, flags + FL_TMO); \
        if (g == 17 && wid == 0 && lane == 0) *(LAS unsigned*)(lds + 131072) = kvmask; } \
      if (g < 17) WAIT_V(6); else WAIT_V(0); WAIT_L(0); BAR(); SBAR(); \
      if (GRP == 0 && (g == n1 + 2 || (g == 17 && n1 + 2 > 17)) && tid == 0) \
        __hip_atomic_store(flags + FL_P + 32 * (8 * h + j), 1u, __ATOMIC_RELAXED, __HIP_MEMORY_SCOPE_AGENT); } while (0)
    float fsc = 1.f; bool resc = false;
    {
#define Q_(x) do { if constexpr (AQ) { x; } } while (0)
#define P_(x) do { if constexpr (AP) { x; } } while (0)
      Q_(C0 = MFMA32(kq[0], qr[0], negm); KRD(0)); P_(C1[8] = EX2(C1[8]); sa += C1[0]; sb += C1[1]; sa += C1[2]; sb += C1[3]; PIN(C1); PIN(sa); PIN(sb)); SBAR();
      Q_(C0 = MFMA32(kq[1], qr[1], C0); KRD(1)); P_(C1[9] = EX2(C1[9]); sa += C1[4]; sb += C1[5]; sa += C1[6]; sb += C1[7]; PIN(C1); PIN(sa); PIN(sb)); SBAR();
      Q_(C0 = MFMA32(kq[2], qr[2], C0); KRD(2)); if (GRP == 0) GLDS_T(vs_p, vd_p, 1024); else if (gq) GLDS_T(ks_p, kd_p, 1024);
          P_(C1[10] = EX2(C1[10]); pw[2] = pack8(C1, 0); PIN(C1); PIN(pw[2])); SBAR();
      Q_(C0 = MFMA32(kq[3], qr[3], C0); KRD(3)); P_(C1[11] = EX2(C1[11]); C1[12] = EX2(C1[12]); sa += C1[8]; sb += C1[9]; PIN(C1); PIN(sa); PIN(sb)); SBAR();
      Q_(C0 = MFMA32(kq[0], qr[4], C0); KRD(4)); P_(C1[13] = EX2(C1[13]); C1[14] = EX2(C1[14]); sa += C1[10]; sb += C1[11]; PIN(C1); PIN(sa); PIN(sb)); SBAR();
      Q_(C0 = MFMA32(kq[1], qr[5], C0); KRD(5)); P_(C1[15] = EX2(C1[15]); sa += C1[12]; sb += C1[13]; sa += C1[14]; PIN(C1); PIN(sa); PIN(sb)); SBAR();
      Q_(C0 = MFMA32(kq[2], qr[6], C0); KRD(6)); P_(sb += C1[15]; pw[3] = pack8(C1, 1); PIN(pw[3]); PIN(sb)); SBAR();
      Q_(C0 = MFMA32(kq[3], qr[7], C0); KRD(7)); P_(lsum += sa + sb; PIN(lsum)); SBAR();
      Q_(C1 = MFMA32(kq[0], qr[0], negm); KRD(8)); if (GRP == 0) GLDS_T(ks_p, kd_p, 0); SBAR();
      Q_(C1 = MFMA32(kq[1], qr[1], C1); KRD(9)); SBAR();
      Q_(C1 = MFMA32(kq[2], qr[2], C1); KRD(10)); SBAR();
      float mxa = 0.f, mxb = 0.f;
      Q_(C1 = MFMA32(kq[3], qr[3], C1); KRD(11); mxa = max3f(C0[0], C0[1], C0[2]); mxb = max3f(C0[3], C0[4], C0[5]); PIN(mxa); PIN(mxb)); SBAR();
      if (GRP == 1) BARRIER_G();
      Q_(C1 = MFMA32(kq[0], qr[4], C1)); P_(VRD(0)); if (GRP == 1) GLDS_T(vs_n, vd_n, 0); Q_(mxa = max3f(mxa, C0[6], C0[7]); mxb = max3f(mxb, C0[8], C0[9]); PIN(mxa); PIN(mxb)); SBAR();
      Q_(C1 = MFMA32(kq[1], qr[5], C1)); P_(VRD(1)); Q_(mxa = max3f(mxa, C0[10], C0[11]); mxb = max3f(mxb, C0[12], C0[13]); PIN(mxa); PIN(mxb)); SBAR();
      Q_(C1 = MFMA32(kq[2], qr[6], C1)); P_(VRD(2)); Q_(mxa = max3f(mxa, C0[14], C0[15]); mxa = __builtin_fmaxf(mxa, mxb); PIN(mxa)); SBAR();
      Q_(C1 = MFMA32(kq[3], qr[7], C1)); P_(VRD(3)); SBAR();
      if (g == n1 - 1) {
        if (j == 0) wait_word(flags + FL_Q + 32 * (8 * h + jp), 1u, flags + FL_TMO);
        const char* qsrc = ws + WS_QF + (size_t)(h * 64 + 8 * jp + wid) * 8192;
        GLOAD1_SC1(qr[0], vl16, qsrc, 0); GLOAD1_SC1(qr[1], vl16, qsrc, 1024); GLOAD1_SC1(qr[2], vl16, qsrc, 2048); GLOAD1_SC1(qr[3], vl16, qsrc, 3072);
        GLOAD1_SC1(qr[4], vl16, qsrc + 4096, 0); GLOAD1_SC1(qr[5], vl16, qsrc + 4096, 1024); GLOAD1_SC1(qr[6], vl16, qsrc + 4096, 2048); GLOAD1_SC1(qr[7], vl16, qsrc + 4096, 3072);
      }
      SBAR();
      P_(o[0] = MFMA32(vq[0], pw[0], o[0]); VRD(4)); if (GRP == 1) GLDS_T(vs_n, vd_n, 1024); SBAR();
      P_(o[1] = MFMA32(vq[1], pw[0], o[1]); VRD(5)); SBAR();
      float mxc = 0.f, mxd = 0.f;
      P_(o[2] = MFMA32(vq[2], pw[0], o[2]); VRD(6)); if (GRP == 0) GLDS_T(ks_p, kd_p, 1024);
          Q_(mxc = max3f(C1[0], C1[1], C1[2]); mxd = max3f(C1[3], C1[4], C1[5]); mxc = max3f(mxc, C1[6], C1[7]); mxd = max3f(mxd, C1[8], C1[9]); PIN(mxc); PIN(mxd)); SBAR();
      P_(o[3] = MFMA32(vq[3], pw[0], o[3]); VRD(7)); Q_(mxc = max3f(mxc, C1[10], C1[11]); mxd = max3f(mxd, C1[12], C1[13]); mxc = max3f(mxc, C1[14], C1[15]); mxa = max3f(mxa, mxc, mxd); PIN(mxa)); SBAR();
      P_(o[0] = MFMA32(vq[0], pw[1], o[0]); VRD(8));
      MEMFENCE(); SBAR();
      if constexpr (AQ) {
        float rm = mxa;
        if (dd >= 0 && wid <= 2 * dd + 1) { cmask(C0, C1, 32 * wid + l31 - 64 * dd - 4 * hh); rm = __builtin_fmaxf(max16(C0), max16(C1)); }
        rm = xhalf_max(rm);
        if (__builtin_expect(seam || __any(rm > THR), 0)) {
          const float dl = seam ? __builtin_fmaxf(rm, -1e30f) : __builtin_fmaxf(rm, 0.f);
          mhat += dl;
#pragma unroll
          for (int r = 0; r < 16; ++r) { C0[r] -= dl; C1[r] -= dl; negm[r] = -mhat; }
          PIN(negm);
          if (seam) { lsum1 = lsum; lsum = 0.f; }
          else { fsc = EX2(-dl); lsum *= fsc; resc = true; }
        }
      } else {
        if (seam) { lsum1 = lsum; lsum = 0.f; mhat = -1e30f; }
      }
      SBAR();
      sa = 0.f; sb = 0.f;
      P_(o[1] = MFMA32(vq[1], pw[1], o[1]); VRD(9)); Q_(C0[0] = EX2(C0[0]); C0[1] = EX2(C0[1]); C0[2] = EX2(C0[2]); PIN(C0)); SBAR();
      P_(o[2] = MFMA32(vq[2], pw[1], o[2]); VRD(10)); Q_(C0[3] = EX2(C0[3]); C0[4] = EX2(C0[4]); sa += C0[0]; sb += C0[1]; PIN(C0); PIN(sa); PIN(sb)); SBAR();
      P_(o[3] = MFMA32(vq[3], pw[1], o[3]); VRD(11)); Q_(C0[5] = EX2(C0[5]); C0[6] = EX2(C0[6]); sa += C0[2]; sb += C0[3]; PIN(C0); PIN(sa); PIN(sb)); SBAR();
      P_(o[0] = MFMA32(vq[0], pw[2], o[0]); VRD(12)); if (GRP == 1) GLDS_T(ks_n, kd_n, 0); Q_(C0[7] = EX2(C0[7]); C0[8] = EX2(C0[8]); sa += C0[4]; sb += C0[5]; PIN(C0); PIN(sa); PIN(sb)); SBAR();
      P_(o[1] = MFMA32(vq[1], pw[2], o[1]); VRD(13)); Q_(pw[0] = pack8(C0, 0); C0[9] = EX2(C0[9]); PIN(pw[0]); PIN(C0)); SBAR();
      P_(o[2] = MFMA32(vq[2], pw[2], o[2]); VRD(14)); Q_(C0[10] = EX2(C0[10]); C0[11] = EX2(C0[11]); sa += C0[6]; sb += C0[7]; PIN(C0); PIN(sa); PIN(sb)); SBAR();
      P_(o[3] = MFMA32(vq[3], pw[2], o[3]); VRD(15)); Q_(C0[12] = EX2(C0[12]); C0[13] = EX2(C0[13]); sa += C0[8]; sb += C0[9]; PIN(C0); PIN(sa); PIN(sb)); SBAR();
      if (GRP == 0) BARRIER_G();
      P_(o[0] = MFMA32(vq[0], pw[3], o[0])); KPRE(0); if (GRP == 0) GLDS_T(vs_n, vd_n, 0); Q_(C0[14] = EX2(C0[14]); C0[15] = EX2(C0[15]); sa += C0[10]; sb += C0[11]; PIN(C0); PIN(sa); PIN(sb)); SBAR();
      P_(o[1] = MFMA32(vq[1], pw[3], o[1])); KPRE(1); Q_(C1[0] = EX2(C1[0]); C1[1] = EX2(C1[1]); sa += C0[12]; sb += C0[13]; PIN(C1); PIN(sa); PIN(sb)); SBAR();
      P_(o[2] = MFMA32(vq[2], pw[3], o[2])); KPRE(2); Q_(pw[1] = pack8(C0, 1); C1[2] = EX2(C1[2]); C1[3] = EX2(C1[3]); PIN(pw[1]); PIN(C1)); SBAR();
      P_(o[3] = MFMA32(vq[3], pw[3], o[3])); KPRE(3); Q_(C1[4] = EX2(C1[4]); C1[5] = EX2(C1[5]); sa += C0[14]; sb += C0[15]; PIN(C1); PIN(sa); PIN(sb)); SBAR();
      Q_(C1[6] = EX2(C1[6]); C1[7] = EX2(C1[7]); lsum += sa + sb; PIN(C1); PIN(lsum));
#undef Q_
#undef P_
    }
    if (resc) {
#pragma unroll
      for (int d = 0; d < 4; ++d) o[d] = o[d] * fsc;
    }
    if (seam) {
      const float lt = xhalf_sum(lsum1);
      const char* pst = ws + WS_PF + (size_t)((h * 8 + j) * 8 + wid) * 8192;
      static_for<4>([&](auto dd_) { constexpr int d = decltype(dd_)::value; gstore_sc1_c<(2 * d) * 1024>(vl16, pack8(o[d], 0), pst); gstore_sc1_c<(2 * d + 1) * 1024>(vl16, pack8(o[d], 1), pst); o[d] = z; });
      const unsigned long long sv = ((unsigned long long)__float_as_uint(lt) << 32) | __float_as_uint(mhat1);
      __hip_atomic_store((unsigned long long*)(ws + WS_PS) + (size_t)((h * 8 + j) * 8 + wid) * 64 + lane, sv, __ATOMIC_RELAXED, __HIP_MEMORY_SCOPE_AGENT);
    }
    if (g == n1 - 1) {
      asm volatile("s_waitcnt vmcnt(1)" : "+v"(qr[0]), "+v"(qr[1]), "+v"(qr[2]), "+v"(qr[3]), "+v"(qr[4]), "+v"(qr[5]), "+v"(qr[6]), "+v"(qr[7]) :: "memory");
    }
  };
  for (int g = 1; g < gA; ++g) stepg(IC<1>{}, IC<1>{}, g);
  if (gA < 18) stepg(IC<0>{}, IC<1>{}, gA);
  for (int g = gA + 1; g < 18; ++g) stepg(IC<0>{}, IC<0>{}, g);
  const bool actP = gA >= 18;
  asm volatile("" :: "v"(pfd));
  {
    const bool early = (rfl(*(const LAS unsigned*)(lds + 131072)) & 512u) != 0u;
    const char* psrc = upt(ws + WS_PF + (size_t)((h * 8 + jp) * 8 + wid) * 8192);
    const char* psrc2 = upt(psrc + 4096);
    const unsigned long long* svp = (const unsigned long long*)(ws + WS_PS) + (size_t)((h * 8 + jp) * 8 + wid) * 64 + lane;
    u32x4 qa[4], qb[4];
    u32x2 svv;
#define PLOADS() do { GLOAD1_SC1(qa[0], vl16, psrc, 0); GLOAD1_SC1(qa[1], vl16, psrc, 1024); GLOAD1_SC1(qa[2], vl16, psrc, 2048); GLOAD1_SC1(qa[3], vl16, psrc, 3072); \
      GLOAD1_SC1(qb[0], vl16, psrc2, 0); GLOAD1_SC1(qb[1], vl16, psrc2, 1024); GLOAD1_SC1(qb[2], vl16, psrc2, 2048); GLOAD1_SC1(qb[3], vl16, psrc2, 3072); \
      asm volatile("global_load_dwordx2 %0, %1, off sc1" : "=&v"(svv) : "v"(svp) : "memory"); } while (0)
    if (early) PLOADS();
    ldscp vb = lds + L_V + (17 & 3) * 16384 + rdoff;
    if (actP) {
#pragma unroll
    for (int i = 0; i < 4; ++i) vq[i] = ldsr128(vb + i * 1024);
    float sa = 0.f;
#pragma unroll
    for (int r = 8; r < 16; ++r) C1[r] = EX2(C1[r]);
#pragma unroll
    for (int r = 0; r < 16; ++r) sa += C1[r];
    lsum += sa;
    pw[2] = pack8(C1, 0); pw[3] = pack8(C1, 1);
    SBAR();
#pragma unroll
    for (int v = 0; v < 16; ++v) {
      o[v & 3] = MFMA32(vq[v & 3], pw[v >> 2], o[v & 3]);
      if (v + 4 < 16) vq[v & 3] = ldsr128(vb + (v + 4) * 1024);
      SBAR();
    }
    }
    lsum = xhalf_sum(lsum);
    if (!early) { wait_word(flags + FL_P + 32 * (8 * h + jp), 1u, flags + FL_TMO); PLOADS(); }
    WAITV_TIE4(0, qa); WAITV_TIE4(0, qb);
    asm volatile("" : "+v"(svv));
    const unsigned long long sv = ((unsigned long long)svv[1] << 32) | svv[0];
#undef PLOADS
    const float om = __uint_as_float((unsigned)sv), ol = __uint_as_float((unsigned)(sv >> 32));
    const float m = __builtin_fmaxf(mhat, om);
    const float f0 = EX2(mhat - m), f1 = EX2(om - m);
    const float inv = __builtin_amdgcn_rcpf(lsum * f0 + ol * f1);
    const float g0 = f0 * inv, g1 = f1 * inv;
    const char* cdst = upt(ws + WS_CTX + (size_t)((8 * jp + wid) * 32 + h) * 8192);
    static_for<8>([&](auto ii_) {
      constexpr int i = decltype(ii_)::value; constexpr int d = i >> 1, sp = i & 1;
      const u32x4 v4 = i < 4 ? qa[i & 3] : qb[i & 3];
      u32x4 w;
#pragma unroll
      for (int e = 0; e < 4; ++e) w[e] = cvtpk(o[d][8 * sp + 2 * e] * g0 + bflo(v4[e]) * g1, o[d][8 * sp + 2 * e + 1] * g0 + bfhi(v4[e]) * g1);
      gstore_sc1_c<i * 1024>(vl16, __builtin_bit_cast(bf16x8, w), cdst);
    });
  }
#undef KRD
#undef VRD
#undef KPRE
#undef BARRIER_G
#undef ENSURE_KV
#undef GLDS_T
#undef TILE
#undef KSRC
#undef VSRC
}

constexpr int T_LDS = 163840;
constexpr int FL_C = 28672;
constexpr int FL_P3 = 20480;
constexpr int FL_H = 22528;
constexpr int FL_U = 23552;
#define GLOAD4_SC1(d, voff, sbase) asm volatile("s_nop 4\n\tglobal_load_dwordx4 %0, %4, %5 sc1\n\tglobal_load_dwordx4 %1, %4, %5 offset:1024 sc1\n\tglobal_load_dwordx4 %2, %4, %5 offset:2048 sc1\n\tglobal_load_dwordx4 %3, %4, %5 offset:3072 sc1" \
    : "=&v"(d[0]), "=&v"(d[1]), "=&v"(d[2]), "=&v"(d[3]) : "v"(voff), "s"(sbase) : "memory")
#define GLDS_SC1(voff, sbase, ldst) do { unsigned keep_; asm volatile("s_mov_b32 %0, m0\n\ts_mov_b32 m0, %3\n\ts_nop 2\n\tglobal_load_lds_dwordx4 %1, %2 sc1\n\ts_mov_b32 m0, %0" \
    : "=&s"(keep_) : "v"(voff), "s"(sbase), "s"(ldst) : "memory"); } while (0)
template <int OFFS> DI void gstore_sc1_uc(unsigned voff, const u32x4& v, const void* sbase) {
  unsigned t;
  asm volatile("v_add_u32 %0, %4, %1\n\ts_nop 3\n\tglobal_store_dwordx4 %0, %2, %3 sc1\n\ts_nop 1" : "=&v"(t) : "v"(voff), "v"(v), "s"(sbase), "i"(OFFS) : "memory");
}
DI void gstore_sc1_u(unsigned voff, const u32x4& v, const void* sbase) {
  asm volatile("s_nop 4\n\tglobal_store_dwordx4 %0, %1, %2 sc1\n\ts_nop 1" :: "v"(voff), "v"(v), "s"(sbase) : "memory");
}
template <bool FIRST> struct MlpCfg {
  static constexpr int PITCH = FIRST ? FF * 2 : SEQ * 2;
  static constexpr int NKT = FIRST ? SEQ / 64 : FF / 64;
  static constexpr int NST = (NKT + 1) / 2;
};
template <bool FIRST> DI unsigned mlp_voffw(int wid, int lane) {
  const int row = 8 * wid + (lane >> 3); const int f = ((row >> 1) & 1) | (((row >> 3) & 1) << 1);
  return (unsigned)(row * MlpCfg<FIRST>::PITCH + (((lane & 7) ^ (f << 1)) << 4));
}
template <bool FIRST, int KT0, int NK> DI void mlp_wstage(unsigned lds0, const char* wb, unsigned voffW, int wid, int reg) {
  static_for<NK>([&](auto kk_) {
    constexpr int k = decltype(kk_)::value; constexpr int kt = (KT0 + k) < MlpCfg<FIRST>::NKT ? (KT0 + k) : MlpCfg<FIRST>::NKT - 1;
    glds16_c<kt * 64 * MlpCfg<FIRST>::PITCH>(voffW, wb, lds0 + reg + k * 8192 + wid * 1024);
  });
}
struct MlpW { ldscp p[4]; };
DI MlpW mlp_wptr(ldsp lds, int wid, int lane) {
  const int g = lane >> 4, q = (lane & 15) >> 2, p = lane & 3; const int f = (q >> 1) | ((g & 1) << 1);
  MlpW w;
#pragma unroll
  for (int ct = 0; ct < 4; ++ct) w.p[ct] = lds + (wid >> 1) * 8192 + 128 * (32 * (wid & 1) + 8 * g + q) + (((2 * ct + (p >> 1)) ^ (f << 1)) << 4) + 8 * (p & 1);
  return w;
}
DI void mlp_wrd(const MlpW& w, int off, bf16x8* dst) {
#pragma unroll
  for (int ct = 0; ct < 4; ++ct) dst[ct] = cat8(vtr(w.p[ct] + off), vtr(w.p[ct] + off + 512));
}
#define MLP_ALOAD(dst, base) asm volatile("s_nop 4\n\tglobal_load_dwordx4 %0, %5, %7 sc1\n\tglobal_load_dwordx4 %1, %5, %7 offset:1024 sc1\n\tglobal_load_dwordx4 %2, %5, %7 offset:2048 sc1\n\t" \
    "global_load_dwordx4 %3, %5, %7 offset:3072 sc1\n\tglobal_load_dwordx4 %4, %6, %7 sc1" \
    : "=&v"(dst[0]), "=&v"(dst[1]), "=&v"(dst[2]), "=&v"(dst[3]), "=&v"(dst[4]) : "v"(vl16), "v"(vl16b), "s"(base) : "memory")
template <bool FIRST, int R0, int R1, int LK0, int NW>
DI void mlp_main(ldsp lds, const char* __restrict__ ab, int wid, int lane, const MlpW& wp, const bf16x8 (&wr)[NW], f32x4 (&acc)[4][5]) {
  constexpr int NT = 2 * MlpCfg<FIRST>::NKT, NS = (NT + 7) / 8;
  const unsigned vl16 = lane * 16, vl16b = lane * 16 + 4096;
  u32x4 af[3][5];
  bf16x8 wl[2][4];
#pragma unroll
  for (int c = 0; c < 4; ++c)
#pragma unroll
    for (int d = 0; d < 5; ++d) acc[c][d] = (f32x4){0.f, 0.f, 0.f, 0.f};
  auto abase = [&](int s) { const int T = 8 * s + wid; return upt(ab + (size_t)(T < NT ? T : NT - 1) * 40960); };
  { const char* b0 = abase(0); MLP_ALOAD(af[0], b0); const char* b1 = abase(1); MLP_ALOAD(af[1], b1); }
  if constexpr (!(0 >= R0 && 0 < R1)) mlp_wrd(wp, (0 - LK0) * 8192, wl[0]);
  static_for<NS>([&](auto ss_) {
    constexpr int s = decltype(ss_)::value;
    if constexpr (s + 2 < NS) { const char* bn = abase(s + 2); MLP_ALOAD(af[(s + 2) % 3], bn); }
    if constexpr (s + 1 < NS && !(s + 1 >= R0 && s + 1 < R1)) mlp_wrd(wp, (4 * (s + 1) - LK0) * 8192, wl[(s + 1) & 1]);
    constexpr int ahead = (NS - 1 - s) < 2 ? (NS - 1 - s) : 2;
    asm volatile("s_waitcnt vmcnt(%5)" : "+v"(af[s % 3][0]), "+v"(af[s % 3][1]), "+v"(af[s % 3][2]), "+v"(af[s % 3][3]), "+v"(af[s % 3][4]) : "i"(5 * ahead) : "memory");
    SBAR();
    if (8 * s + 7 < NT || 8 * s + wid < NT) {
#pragma unroll
      for (int c = 0; c < 4; ++c) {
        const bf16x8 wfr = (s >= R0 && s < R1) ? wr[(s >= R0 && s < R1) ? (s - R0) * 4 + c : 0] : wl[s & 1][c];
#pragma unroll
        for (int d = 0; d < 5; ++d) {
          const bf16x8 a8 = __builtin_bit_cast(bf16x8, af[s % 3][d]);
          acc[c][d] = FIRST ? MFMA16(wfr, a8, acc[c][d]) : MFMA16(a8, wfr, acc[c][d]);
        }
      }
    }
    SBAR();
  });
}
DI void mlp_park(ldsp lds, int wid, int lane, f32x4 (&acc)[4][5]) {
  ldsp pw_ = lds + wid * 10240 + lane * 16;
  ldsp pr_ = lds + (wid ^ 4) * 10240 + lane * 16;
  if (wid < 4) {
#pragma unroll
    for (int c = 0; c < 2; ++c)
#pragma unroll
      for (int d = 0; d < 5; ++d) *(LAS f32x4*)(pw_ + (c * 5 + d) * 1024) = acc[2 + c][d];
  } else {
#pragma unroll
    for (int c = 0; c < 2; ++c)
#pragma unroll
      for (int d = 0; d < 5; ++d) *(LAS f32x4*)(pw_ + (c * 5 + d) * 1024) = acc[c][d];
  }
  WAIT_L(0); BAR();
  if (wid < 4) {
#pragma unroll
    for (int c = 0; c < 2; ++c)
#pragma unroll
      for (int d = 0; d < 5; ++d) { const f32x4 o = *(const LAS f32x4*)(pr_ + (c * 5 + d) * 1024); *(LAS f32x4*)(pr_ + (c * 5 + d) * 1024) = acc[c][d] + o; }
  } else {
#pragma unroll
    for (int c = 0; c < 2; ++c)
#pragma unroll
      for (int d = 0; d < 5; ++d) { const f32x4 o = *(const LAS f32x4*)(pr_ + (c * 5 + d) * 1024); *(LAS f32x4*)(pr_ + (c * 5 + d) * 1024) = acc[2 + c][d] + o; }
  }
  WAIT_L(0); BAR();
}
DI void mlp_gather(ldscp lds, int c2, int dt, int gD, int c15, float (&v)[8]) {
  ldscp p = lds + ((c2 < 2 ? 4 : 0) * 10 + (c2 & 1) * 5 + dt) * 1024 + (gD * 16 + c15) * 16;
#pragma unroll
  for (int hf = 0; hf < 2; ++hf) {
    f32x4 a = {0.f, 0.f, 0.f, 0.f};
#pragma unroll
    for (int q = 0; q < 4; ++q) a += *(const LAS f32x4*)(p + q * 10240 + hf * 256);
#pragma unroll
    for (int e = 0; e < 4; ++e) v[4 * hf + e] = a[e];
  }
}

DI void tail_phase(unsigned char* smem, const u16* __restrict__ x, const u16* __restrict__ wo, const u16* __restrict__ w1, const u16* __restrict__ b1,
                   const u16* __restrict__ w2, const u16* __restrict__ b2, char* __restrict__ ws, u16* __restrict__ out, int wid) {
  const int b = blockIdx.x, xcd = b & 7, r = b >> 3;
  ldsp lds = (ldsp)smem;
  const unsigned lds0 = (unsigned)(uintptr_t)smem;
  unsigned* flags = (unsigned*)(ws + WS_FLAGS);
  const int ksp = xcd >> 1, panel = (xcd & 1) * 8 + (r >> 2), ctile = r & 3;
  const int idx5 = xcd * 27 + r;
  const int wt5 = idx5 >> 3, dg5 = idx5 & 7;
  const int wt6 = xcd * 4 + (r >> 3), dg6 = r & 7;
  const bool do5 = r < 27;
  constexpr int P4S = 131072;
  bf16x8 wf[16];
  {
    const int lane = lane_id();
    const unsigned vl16 = lane * 16;
    const int kh = wid >> 2, st = wid & 3, hh = lane >> 5;
    unsigned voff[5];
#pragma unroll
    for (int i = 0; i < 5; ++i) {
      const int id = 2 * (5 * wid + i) + (lane >> 5), half = id / 40, rem = id % 40, rg = rem / 5, c = rem % 5;
      const int row = 64 * half + 8 * rg + ((lane >> 2) & 7);
      voff[i] = (unsigned)(row * 1280 + (32 * c + 8 * ((lane & 3) ^ ((row >> 2) & 3))) * 2);
    }
    const char* wb = (const char*)wo + (size_t)(ksp * 1024) * 1280 + ctile * 320;
    const unsigned dstw = lds0 + wid * 5120;
#define OPIECE(hd, i) glds16_c<(hd) * 128 * 1280>(voff[i], wb, dstw + ((hd) & 3) * 40960 + (i) * 1024)
#define STAGE_O(hd) do { OPIECE(hd, 0); OPIECE(hd, 1); OPIECE(hd, 2); OPIECE(hd, 3); OPIECE(hd, 4); } while (0)
    const char* cb = ws + WS_CTX + ((size_t)((4 * panel + st) * 32 + 8 * ksp) * 8 + 4 * kh) * 1024;
    int boff[2];
    { const int blk = (lane >> 4) & 1, q = (lane & 15) >> 2, p = lane & 3;
#pragma unroll
      for (int t = 0; t < 2; ++t) boff[t] = kh * 20480 + 2560 * t + 64 * (4 * hh + q) + (((2 * blk + (p >> 1)) ^ ((2 * t + hh) & 3)) << 4) + 8 * (p & 1); }
    ldscp bp0 = lds + boff[0], bp1 = lds + boff[1];
    u32x4 xf[3][4];
    f32x16 acc[5] = {};
    bf16x8 aq[6];
#define ARD(U) aq[(U) % 6] = cat8(vtr(bp0 + (((U) / 20) & 3) * 40960 + (((U) % 20) / 5) * 5120 + ((U) % 5) * 512), vtr(bp1 + (((U) / 20) & 3) * 40960 + (((U) % 20) / 5) * 5120 + ((U) % 5) * 512))
    STAGE_O(0); STAGE_O(1); STAGE_O(2);
    if (wid == 0) wait_word(flags + FL_C + 16 * ((panel >> 1) * 4 + ksp), 8u, flags + FL_TMO);
    BAR();
    GLOAD4_SC1(xf[0], vl16, cb); GLOAD4_SC1(xf[1], vl16, cb + 8192);
    WAIT_V(8); BAR();
#pragma unroll
    for (int U = 0; U < 5; ++U) ARD(U);
    static_for<8>([&](auto hh_) {
      constexpr int hd = decltype(hh_)::value;
      if constexpr (hd == 0) { WAITV_TIE4(4, xf[0]); }
      else if constexpr (hd <= 5) { WAITV_TIE4(9, xf[hd % 3]); }
      else if constexpr (hd == 6) { WAITV_TIE4(4, xf[hd % 3]); }
      else { WAITV_TIE4(0, xf[hd % 3]); }
      SBAR(); BAR(); SBAR();
      const char* xn_ = cb + (size_t)(hd + 2) * 8192;
      static_for<20>([&](auto uu_) {
        constexpr int u = decltype(uu_)::value;
        constexpr int U = 20 * hd + u;
        acc[u % 5] = MFMA32(aq[U % 6], __builtin_bit_cast(bf16x8, xf[hd % 3][u / 5]), acc[u % 5]);
        if constexpr (U + 5 < 160) ARD(U + 5);
        if constexpr (hd + 2 < 8) {
          if constexpr (u == 1) GLOAD1_SC1(xf[(hd + 2) % 3][0], vl16, xn_, 0);
          if constexpr (u == 3) GLOAD1_SC1(xf[(hd + 2) % 3][1], vl16, xn_, 1024);
          if constexpr (u == 5) GLOAD1_SC1(xf[(hd + 2) % 3][2], vl16, xn_, 2048);
          if constexpr (u == 7) GLOAD1_SC1(xf[(hd + 2) % 3][3], vl16, xn_, 3072);
        }
        if constexpr (hd + 3 < 8) {
          if constexpr (u == 9) OPIECE(hd + 3, 0);
          if constexpr (u == 11) OPIECE(hd + 3, 1);
          if constexpr (u == 13) OPIECE(hd + 3, 2);
          if constexpr (u == 15) OPIECE(hd + 3, 3);
          if constexpr (u == 17) OPIECE(hd + 3, 4);
        }
        SBAR();
      });
    });
#undef OPIECE
#undef STAGE_O
#undef ARD
    BAR();
    ldsp xw = lds + (kh == 0 ? 49152 : 0) + st * 12288 + lane * 16;
    ldscp xr = lds + (kh == 0 ? 0 : 49152) + st * 12288 + lane * 16;
    if (kh == 0) {
#pragma unroll
      for (int i = 0; i < 2; ++i)
#pragma unroll
        for (int g = 0; g < 4; ++g) *(LAS f32x4*)(xw + (i * 4 + g) * 1024) = (f32x4){acc[3 + i][4 * g], acc[3 + i][4 * g + 1], acc[3 + i][4 * g + 2], acc[3 + i][4 * g + 3]};
    } else {
#pragma unroll
      for (int i = 0; i < 3; ++i)
#pragma unroll
        for (int g = 0; g < 4; ++g) *(LAS f32x4*)(xw + (i * 4 + g) * 1024) = (f32x4){acc[i][4 * g], acc[i][4 * g + 1], acc[i][4 * g + 2], acc[i][4 * g + 3]};
    }
    WAIT_L(0); BAR();
    const char* sb = ws + WS_SLAB + ((size_t)((ksp * 64 + 4 * panel + st) * 20 + 5 * ctile) * 2) * 1024;
#define SLABST(I, T) static_for<2>([&](auto pp_) { \
          constexpr int sp = decltype(pp_)::value; \
          const f32x4 o0 = *(const LAS f32x4*)(xr + ((I) * 4 + 2 * sp) * 1024), o1 = *(const LAS f32x4*)(xr + ((I) * 4 + 2 * sp + 1) * 1024); \
          const u32x4 w = {cvtpk(acc[T][8 * sp] + o0[0], acc[T][8 * sp + 1] + o0[1]), cvtpk(acc[T][8 * sp + 2] + o0[2], acc[T][8 * sp + 3] + o0[3]), \
                           cvtpk(acc[T][8 * sp + 4] + o1[0], acc[T][8 * sp + 5] + o1[1]), cvtpk(acc[T][8 * sp + 6] + o1[2], acc[T][8 * sp + 7] + o1[3])}; \
          gstore_sc1_uc<((T) * 2 + sp) * 1024>(vl16, w, sb); })
    if (kh == 0) { SLABST(0, 0); SLABST(1, 1); SLABST(2, 2); }
    else { SLABST(0, 3); SLABST(1, 4); }
#undef SLABST
    WAIT_V(0); WAIT_L(0); BAR();
    if (wid == 0 && lane == 0) __hip_atomic_fetch_add(flags + FL_P3 + 64 * panel, 1u, __ATOMIC_RELAXED, __HIP_MEMORY_SCOPE_AGENT);
  }
  {
    const int lane = lane_id(), tid = wid * 64 + lane;
    const MlpW wp = mlp_wptr(lds, wid, lane);
    const char* w1b = (const char*)w1 + wt5 * 128;
    const unsigned voffW = mlp_voffw<true>(wid, lane);
    ldsp tr = lds + P4S;
    LAS float* red = (LAS float*)(lds + P4S + 10240);
    const int rr = tid & 7, pc = tid >> 3;
    const int r8 = ksp * 4 + ctile;
    const int s = 128 * panel + 8 * r8 + rr, st = s >> 5;
    int n0[2]; unsigned so[2];
    u32x2 xv[2][2];
#pragma unroll
    for (int i = 0; i < 2; ++i) {
      const int pi = pc + 64 * i; const int pcl = pi < 80 ? pi : 0;
      const int nt = pcl >> 2, sp = (pcl >> 1) & 1, hh = pcl & 1;
      n0[i] = 32 * nt + 16 * sp + 4 * hh;
      so[i] = (unsigned)((((st * 20 + nt) * 2 + sp) * 64 + (s & 31) + 32 * hh) * 16);
      xv[i][0] = *(const u32x2*)(x + (size_t)s * DM + n0[i]); xv[i][1] = *(const u32x2*)(x + (size_t)s * DM + n0[i] + 8);
    }
    asm volatile("" : "+v"(xv[0][0]), "+v"(xv[0][1]), "+v"(xv[1][0]), "+v"(xv[1][1]));
    if (do5) mlp_wstage<true, 0, 16>(lds0, w1b, voffW, wid, 0);
    if (wid == 0) wait_word(flags + FL_P3 + 64 * panel, 16u, flags + FL_TMO);
    BAR();
    u32x4 sv[2][4];
    const char* sl = ws + WS_SLAB;
#pragma unroll
    for (int i = 0; i < 2; ++i)
#pragma unroll
      for (int k = 0; k < 4; ++k) GLOAD1_SC1(sv[i][k], so[i], sl + (size_t)k * (64 * 20 * 2 * 1024), 0);
    WAITV_TIE4(0, sv[0]); WAITV_TIE4(0, sv[1]);
    float hv[2][8];
    unsigned pk[2][4];
    float sum = 0.f, ssq = 0.f;
#pragma unroll
    for (int i = 0; i < 2; ++i) {
      const bool ok = pc + 64 * i < 80;
#pragma unroll
      for (int e = 0; e < 4; ++e) {
        const float lo = (bflo(sv[i][0][e]) + bflo(sv[i][1][e])) + (bflo(sv[i][2][e]) + bflo(sv[i][3][e]));
        const float hi = (bfhi(sv[i][0][e]) + bfhi(sv[i][1][e])) + (bfhi(sv[i][2][e]) + bfhi(sv[i][3][e]));
        const unsigned xw_ = xv[i][e >> 1][e & 1];
        pk[i][e] = cvtpk(lo + bflo(xw_), hi + bfhi(xw_));
        hv[i][2 * e] = ok ? bflo(pk[i][e]) : 0.f; hv[i][2 * e + 1] = ok ? bfhi(pk[i][e]) : 0.f;
        sum += hv[i][2 * e] + hv[i][2 * e + 1];
      }
    }
    sum += __shfl_xor(sum, 8); sum += __shfl_xor(sum, 16); sum += __shfl_xor(sum, 32);
    const float cntw = (wid < 2) ? 128.f : 64.f;
    const float mw = sum / cntw;
#pragma unroll
    for (int i = 0; i < 2; ++i) {
      if (pc + 64 * i < 80) {
#pragma unroll
        for (int e = 0; e < 8; ++e) { const float d = hv[i][e] - mw; ssq += d * d; }
      }
    }
    ssq += __shfl_xor(ssq, 8); ssq += __shfl_xor(ssq, 16); ssq += __shfl_xor(ssq, 32);
    if (lane < 8) { red[(wid * 8 + lane) * 2] = mw; red[(wid * 8 + lane) * 2 + 1] = ssq; }
    WAIT_L(0); BAR();
    float mean = 0.f;
#pragma unroll
    for (int w = 0; w < 8; ++w) mean += red[(w * 8 + rr) * 2] * (w < 2 ? 128.f : 64.f);
    mean *= (1.0f / DM);
    float m2 = 0.f;
#pragma unroll
    for (int w = 0; w < 8; ++w) { const float dm = red[(w * 8 + rr) * 2] - mean; m2 += red[(w * 8 + rr) * 2 + 1] + dm * dm * (w < 2 ? 128.f : 64.f); }
    const float rstd = 1.0f / sqrtf(m2 * (1.0f / DM) + LN_EPS);
#pragma unroll
    for (int i = 0; i < 2; ++i) {
      if (pc + 64 * i < 80) {
        LAS u16* t16 = (LAS u16*)tr;
#pragma unroll
        for (int e = 0; e < 4; ++e) {
          const unsigned p = cvtpk((hv[i][2 * e] - mean) * rstd, (hv[i][2 * e + 1] - mean) * rstd);
          const int n = n0[i] + 8 * (e >> 1) + 2 * (e & 1);
          t16[n * 8 + rr] = (u16)(p & 0xffff); t16[(n + 1) * 8 + rr] = (u16)(p >> 16);
        }
      }
    }
    WAIT_L(0); BAR();
    const char* hdst = ws + WS_HNF + ((size_t)st * 40 * 64 + ((s >> 3) & 3) * 16) * 16;
    { const int d = tid; const u32x4 tv4 = *(const LAS u32x4*)(tr + d * 16); gstore_sc1_u((unsigned)(((d >> 4) * 64 + (d & 15)) * 16), tv4, hdst); }
    if (tid < 128) { const int d = 512 + tid; const u32x4 tv4 = *(const LAS u32x4*)(tr + d * 16); gstore_sc1_u((unsigned)(((d >> 4) * 64 + (d & 15)) * 16), tv4, hdst); }
#pragma unroll
    for (int i = 0; i < 2; ++i) {
      if (pc + 64 * i < 80) {
        unsigned long long* hp = (unsigned long long*)(ws + WS_H + ((size_t)s * DM + n0[i]) * 2);
        __hip_atomic_store(hp, ((unsigned long long)pk[i][1] << 32) | pk[i][0], __ATOMIC_RELAXED, __HIP_MEMORY_SCOPE_AGENT);
        __hip_atomic_store(hp + 2, ((unsigned long long)pk[i][3] << 32) | pk[i][2], __ATOMIC_RELAXED, __HIP_MEMORY_SCOPE_AGENT);
      }
    }
    if (do5) {
#pragma unroll
      for (int s4 = 0; s4 < 4; ++s4) mlp_wrd(wp, s4 * 32768, wf + 4 * s4);
    }
    WAIT_V(0); WAIT_L(0); BAR();
    if (tid == 0) __hip_atomic_fetch_add(flags + FL_H + 64 * (b & 7), 1u, __ATOMIC_RELAXED, __HIP_MEMORY_SCOPE_AGENT);
    if (do5) mlp_wstage<true, 16, 16>(lds0, w1b, voffW, wid, 0);
  }
  constexpr int RHI = 81920;
  {
    const int lane = lane_id(), tid = wid * 64 + lane;
    const char* w2b = (const char*)w2 + wt6 * 128;
    const unsigned voffW2 = mlp_voffw<false>(wid, lane);
    if (do5) {
      if (wid == 0) {
        for (unsigned spins = 0;; ++spins) {
          const unsigned v = __hip_atomic_load(flags + FL_H + 64 * (lane & 7), __ATOMIC_RELAXED, __HIP_MEMORY_SCOPE_AGENT);
          if (__all(v >= 32u)) break;
          if ((spins & 63u) == 63u && (spins > (1u << 13) || rfl(__hip_atomic_load(flags + FL_TMO, __ATOMIC_RELAXED, __HIP_MEMORY_SCOPE_AGENT)) != 0u)) { __hip_atomic_store(flags + FL_TMO, 1u, __ATOMIC_RELAXED, __HIP_MEMORY_SCOPE_AGENT); break; }
          __builtin_amdgcn_s_sleep(1);
        }
      }
      WAIT_V(0); WAIT_L(0); BAR();
      f32x4 acc[4][5];
      { const MlpW wp = mlp_wptr(lds, wid, lane); mlp_main<true, 0, 4, 16, 16>(lds, ws + WS_HNF + (size_t)(5 * dg5) * 1024, wid, lane, wp, wf, acc); }
      BAR();
      mlp_park(lds, wid, lane, acc);
      {
        float v[2][8]; u32x4 bv[2];
#pragma unroll
        for (int k2 = 0; k2 < 2; ++k2) {
          const int it = k2 == 0 ? tid : (tid < 128 ? 512 + tid : tid);
          const int d15 = it & 15, gp = (it >> 4) & 3, fr = it >> 6, dt = fr % 5, Tp = fr / 5;
          bv[k2] = *(const u32x4*)(b1 + 64 * wt5 + 32 * Tp + 8 * gp);
          mlp_gather(lds, 2 * Tp + (gp >> 1), dt, 2 * (gp & 1), d15, v[k2]);
        }
#pragma unroll
        for (int k2 = 0; k2 < 2; ++k2) {
          if (k2 == 0 || tid < 128) {
            const int it = k2 == 0 ? tid : 512 + tid;
            const int d15 = it & 15, gp = (it >> 4) & 3, fr = it >> 6, dt = fr % 5, Tp = fr / 5;
            u32x4 o;
#pragma unroll
            for (int e = 0; e < 4; ++e) {
              float z[2] = {v[k2][2 * e] + bflo(bv[k2][e]), v[k2][2 * e + 1] + bfhi(bv[k2][e])};
#pragma unroll
              for (int k = 0; k < 2; ++k) {
                const float xx = z[k];
                const float a = xx * (1.0f + 0.044715f * xx * xx) * (-2.0f * 0.7978845608028654f * 1.4426950408889634f);
                z[k] = xx * __builtin_amdgcn_rcpf(1.0f + __builtin_amdgcn_exp2f(a));
              }
              o[e] = cvtpk(z[0], z[1]);
            }
            gstore_sc1_u((unsigned)((((2 * wt5 + Tp) * 40 + 5 * dg5 + dt) * 64 + gp * 16 + d15) * 16), o, ws + WS_UF);
          }
        }
      }
      mlp_wstage<false, 20, 8>(lds0, w2b, voffW2, wid, RHI);
      WAIT_V(8); WAIT_L(0); BAR();
      if (tid == 0) __hip_atomic_fetch_add(flags + FL_U + 64 * dg5, 1u, __ATOMIC_RELAXED, __HIP_MEMORY_SCOPE_AGENT);
    } else {
      WAIT_L(0); BAR();
      mlp_wstage<false, 20, 8>(lds0, w2b, voffW2, wid, RHI);
    }
  }
  {
    const int lane = lane_id(), tid = wid * 64 + lane;
    bf16x8 wg[8];
    const MlpW wp = mlp_wptr(lds, wid, lane);
    const char* w2b = (const char*)w2 + wt6 * 128;
    const unsigned voffW2 = mlp_voffw<false>(wid, lane);
    mlp_wstage<false, 0, 10>(lds0, w2b, voffW2, wid, 0);
    WAIT_V(10); BAR();
    mlp_wrd(wp, RHI, wg); mlp_wrd(wp, RHI + 32768, wg + 4);
    WAIT_L(0); BAR();
    mlp_wstage<false, 10, 10>(lds0, w2b, voffW2, wid, RHI);
    if (wid == 0) wait_word(flags + FL_U + 64 * dg6, 27u, flags + FL_TMO);
    WAIT_V(0); BAR();
    const char* ab = ws + WS_UF + (size_t)(5 * dg6) * 1024;
    f32x4 acc[4][5];
    mlp_main<false, 5, 7, 0, 8>(lds, ab, wid, lane, wp, wg, acc);
    u32x4 hv[2]; float bb[2]; size_t oo[2];
#pragma unroll
    for (int k2 = 0; k2 < 2; ++k2) {
      const int it = k2 == 0 ? tid : (tid < 128 ? 512 + tid : tid);
      const int sl = it / 10, c = it % 10, srow = 64 * wt6 + sl;
      oo[k2] = (size_t)srow * DM + 80 * dg6 + 8 * c;
      GLOAD1_SC1(hv[k2], (unsigned)(oo[k2] * 2), ws + WS_H, 0);
      bb[k2] = bflo((unsigned)b2[srow]);
    }
    BAR();
    mlp_park(lds, wid, lane, acc);
    float v[2][8];
#pragma unroll
    for (int k2 = 0; k2 < 2; ++k2) {
      const int it = k2 == 0 ? tid : (tid < 128 ? 512 + tid : tid);
      const int sl = it / 10, c = it % 10;
      mlp_gather(lds, sl >> 4, c >> 1, 2 * (c & 1), sl & 15, v[k2]);
    }
    asm volatile("s_waitcnt vmcnt(0)" : "+v"(hv[0]), "+v"(hv[1]) :: "memory");
#pragma unroll
    for (int k2 = 0; k2 < 2; ++k2) {
      if (k2 == 0 || tid < 128) {
        u32x4 ov;
#pragma unroll
        for (int e = 0; e < 4; ++e) ov[e] = cvtpk(v[k2][2 * e] + bb[k2] + bflo(hv[k2][e]), v[k2][2 * e + 1] + bb[k2] + bfhi(hv[k2][e]));
        *(u32x4*)(out + oo[k2]) = ov;
      }
    }
  }
}

constexpr int CODE_BYTES = 73728;
constexpr int qproj_ns(int c, int cls) {
  constexpr int tab[4][10] = {{3, 3, 3, 3, 2, 2, 0, 0, 0, 0}, {1, 2, 2, 2, 3, 3, 3, 0, 0, 0}, {0, 1, 1, 2, 2, 2, 2, 3, 3, 0}, {0, 0, 1, 1, 1, 2, 2, 3, 3, 3}};
  return (c < 0 || c > 9) ? 0 : tab[cls][c];
}
constexpr int qproj_sbase(int c, int cls) { int n = 0; for (int i = 0; i < c; ++i) n += qproj_ns(i, cls); return n; }
constexpr int qproj_nl(int c) { return (c + 2 < 10 ? 4 : 0) + (c + 3 < 10 ? 2 : 0); }
constexpr int qproj_allowed(int c, int cls) {
  return c == 0 ? 6 : (c == 1 ? qproj_nl(0) + qproj_ns(0, cls) : qproj_ns(c - 2, cls) + qproj_nl(c - 1) + qproj_ns(c - 1, cls));
}
__global__ __launch_bounds__(512, 2) void kF1(const u16* __restrict__ ckv, const u16* __restrict__ wuk, const u16* __restrict__ wuv, const u16* __restrict__ wq,
                                              const u16* __restrict__ wo, const u16* __restrict__ w1, const u16* __restrict__ w2,
                                              const u16* __restrict__ x, const u16* __restrict__ b1, const u16* __restrict__ b2, char* __restrict__ ws, u16* __restrict__ out) {
  extern __shared__ __attribute__((aligned(16))) unsigned char smem[];
  const int bidx = blockIdx.x, xcd = bidx & 7, rr_ = bidx >> 3;
  const int h = xcd * 4 + (rr_ >> 3), j = rr_ & 7;
  const int wid = rfl(threadIdx.x >> 6);
  ldsp lds = (ldsp)smem;
  const unsigned lds0 = (unsigned)(uintptr_t)smem;
  unsigned* flags = (unsigned*)(ws + WS_FLAGS);
  const char* code0 = (const char*)__builtin_amdgcn_s_getpc();
  unsigned cpf = 0;
#define CODE_TOUCH() do { const unsigned ln_ = (unsigned)rr_ * 32u + (threadIdx.x & 63u); if (wid == 7 && (threadIdx.x & 63u) < 32u && ln_ * 128u < (unsigned)CODE_BYTES) \
    asm volatile("global_load_dword %0, %1, off" : "+v"(cpf) : "v"(code0 + (size_t)ln_ * 128) : "memory"); } while (0)
  CODE_TOUCH();
  bf16x8 kvp[16];
  {
    const int wr = wid >> 2, wc = wid & 3;
    const int lane = threadIdx.x & 63, l31 = lane & 31, hh = lane >> 5;
    unsigned voffC, voffW;
    { const int row = 8 * wid + (lane >> 3); voffC = (unsigned)(row * 1024 + (((lane & 7) ^ ((row >> 1) & 7)) << 4)); }
    { const int row = 8 * (wid >> 1) + ((lane >> 2) & 7); const int chq = 2 * (wid & 1) + (lane >> 5);
      const int ch = 4 * chq + ((lane & 3) ^ ((row >> 2) & 3)); voffW = (unsigned)(row * 8192 + ch * 16); }
    const char* cbase = (const char*)ckv + (size_t)(256 * j) * 1024;
    const char* wkb = (const char*)wuk + h * 256;
    const char* wvb = (const char*)wuv + h * 256;
    const unsigned dstw = lds0 + wid * 1024;
#define CBUF(b, a) (((b) * 2 + (a)) * 16384)
#define WBUF(b, a) (65536 + ((b) * 2 + (a)) * 16384)
#define STAGE_C(b, a, kt) do { const char* s_ = cbase + (a) * 131072 + (kt) * 128; glds16(voffC, s_, dstw + CBUF(b, a)); glds16(voffC, s_ + 65536, dstw + CBUF(b, a) + 8192); } while (0)
#define STAGE_W(b, a, kt) do { const char* s_ = ((a) ? wvb : wkb) + (size_t)(kt) * 64 * 8192; glds16(voffW, s_, dstw + WBUF(b, a)); glds16(voffW, s_ + 32 * 8192, dstw + WBUF(b, a) + 8192); } while (0)
    int aoff[4], boff[2];
#pragma unroll
    for (int s = 0; s < 4; ++s) aoff[s] = 128 * (wr * 64 + l31) + (((2 * s + hh) ^ ((l31 >> 1) & 7)) << 4);
    { const int blk = (lane >> 4) & 1, q = (lane & 15) >> 2, p = lane & 3;
#pragma unroll
      for (int t = 0; t < 2; ++t) boff[t] = 2048 * hh + 512 * wc + 64 * (4 * t + q) + (((2 * blk + (p >> 1)) ^ ((2 * hh + t) & 3)) << 4) + 8 * (p & 1); }
#define LDA(dst, b, a) do { _Pragma("unroll") for (int m = 0; m < 2; ++m) _Pragma("unroll") for (int s = 0; s < 4; ++s) dst[m][s] = ldsr128(lds + CBUF(b, a) + m * 4096 + aoff[s]); } while (0)
#define LDB(dst, b, a) do { _Pragma("unroll") for (int s = 0; s < 4; ++s) dst[s] = cat8(vtr(lds + WBUF(b, a) + s * 4096 + boff[0]), vtr(lds + WBUF(b, a) + s * 4096 + boff[1])); } while (0)
#define MMAK(ai, At, Bt) do { __builtin_amdgcn_s_setprio(1); _Pragma("unroll") for (int s = 0; s < 4; ++s) _Pragma("unroll") for (int m = 0; m < 2; ++m) \
    accK[ai][m] = MFMA32(Bt[s], At[m][s], accK[ai][m]); __builtin_amdgcn_s_setprio(0); } while (0)
#define MMAV(ai, At, Bt) do { __builtin_amdgcn_s_setprio(1); _Pragma("unroll") for (int s = 0; s < 4; ++s) _Pragma("unroll") for (int m = 0; m < 2; ++m) \
    accV[ai][m] = MFMA32(At[m][s], Bt[s], accV[ai][m]); __builtin_amdgcn_s_setprio(0); } while (0)
    f32x16 accK[2][2] = {}, accV[2][2] = {};
    bf16x8 At[2][4], B0[4], B1[4];
    constexpr int nt = DC / 64;
    STAGE_W(0, 0, 0); STAGE_C(0, 0, 0); STAGE_W(0, 1, 0); STAGE_C(0, 1, 0);
    if (wr == 1) BAR();
    WAIT_V(4); BAR();
    STAGE_W(1, 0, 1); STAGE_C(1, 0, 1); STAGE_W(1, 1, 1);
    WAIT_V(6); BAR();
    unsigned pfd = 0;
    if (wid < 3) {
      const int ln = wid * 64 + lane;
      if (ln < 160) asm volatile("global_load_dword %0, %1, off" : "+v"(pfd) : "v"((const char*)wq + (size_t)(80 * j + (ln >> 1)) * 8192 + h * 256 + (ln & 1) * 128) : "memory");
    }
    for (int t = 0; t < nt - 2; t += 2) {
      LDB(B0, 0, 0); SBAR(); LDA(At, 0, 0); STAGE_C(1, 1, t + 1);
      WAIT_L(8); BAR(); WAIT_L(0); MMAK(0, At, B0); BAR(); SBAR();
      LDB(B1, 0, 1); STAGE_W(0, 0, t + 2);
      BAR(); WAIT_L(0); MMAV(0, At, B1); BAR();
      LDA(At, 0, 1); STAGE_C(0, 0, t + 2);
      BAR(); WAIT_L(0); MMAK(1, At, B0); BAR(); SBAR();
      STAGE_W(0, 1, t + 2);
      WAIT_V(6); BAR(); MMAV(1, At, B1); BAR();
      LDB(B0, 1, 0); SBAR(); LDA(At, 1, 0); STAGE_C(0, 1, t + 2);
      WAIT_L(8); BAR(); WAIT_L(0); MMAK(0, At, B0); BAR(); SBAR();
      LDB(B1, 1, 1); STAGE_W(1, 0, t + 3);
      BAR(); WAIT_L(0); MMAV(0, At, B1); BAR();
      LDA(At, 1, 1); STAGE_C(1, 0, t + 3);
      BAR(); WAIT_L(0); MMAK(1, At, B0); BAR(); SBAR();
      STAGE_W(1, 1, t + 3);
      WAIT_V(6); BAR(); MMAV(1, At, B1); BAR();
    }
    {
      const char* wqb_ = (const char*)wq + h * 256;
      glds16_c<0>(voffW, wqb_, lds0 + 131072 + wid * 1024); glds16_c<32 * 8192>(voffW, wqb_, lds0 + 131072 + 8192 + wid * 1024);
      glds16_c<64 * 8192>(voffW, wqb_, lds0 + 147456 + wid * 1024); glds16_c<96 * 8192>(voffW, wqb_, lds0 + 147456 + 8192 + wid * 1024); }
    { LDB(B0, 0, 0); WAIT_V(0); LDA(At, 0, 0); STAGE_C(1, 1, nt - 1);
      BAR(); WAIT_L(0); MMAK(0, At, B0); BAR();
      LDB(B1, 0, 1); BAR(); WAIT_L(0); MMAV(0, At, B1); BAR();
      LDA(At, 0, 1); WAIT_V(4); BAR(); WAIT_L(0); MMAK(1, At, B0); MMAV(1, At, B1); BAR(); }
    { LDB(B0, 1, 0); LDA(At, 1, 0); WAIT_V(2); BAR(); WAIT_L(0); MMAK(0, At, B0); BAR();
      LDB(B1, 1, 1); WAIT_V(0); BAR(); WAIT_L(0); MMAV(0, At, B1); BAR();
      LDA(At, 1, 1); BAR(); WAIT_L(0); MMAK(1, At, B0); MMAV(1, At, B1); BAR(); }
    if (wr == 0) BAR();
    asm volatile("" :: "v"(pfd), "v"(cpf));
#pragma unroll
    for (int ai = 0; ai < 2; ++ai)
#pragma unroll
      for (int m = 0; m < 2; ++m)
#pragma unroll
        for (int sp = 0; sp < 2; ++sp) {
          kvp[((ai * 2 + m) * 2 + sp) * 2] = pack8(accK[ai][m], sp);
          kvp[((ai * 2 + m) * 2 + sp) * 2 + 1] = pack8(accV[ai][m], sp);
          PIN(kvp[((ai * 2 + m) * 2 + sp) * 2]); PIN(kvp[((ai * 2 + m) * 2 + sp) * 2 + 1]);
        }
#undef CBUF
#undef WBUF
#undef STAGE_C
#undef STAGE_W
#undef LDA
#undef LDB
#undef MMAK
#undef MMAV
  }
  SBAR();
  bf16x8 qr[8];
  unsigned kvmask = 0;
  const int lane = lane_id(), tid = wid * 64 + lane;
  const unsigned vl16 = lane * 16;
  {
    const int hh = lane >> 5;
    const char* Kh = ws + WS_KF + (size_t)h * 64 * 8192;
    const char* Vh = ws + WS_VF + (size_t)h * 128 * 4096;
    const unsigned vt2 = (unsigned)(wid * 2048 + lane * 16);
    const unsigned dt2 = lds0 + wid * 2048;
    const int n1 = j >= 4 ? 16 : 4 * j + 2;
#define TILE(g) ((g) < n1 ? (g) : 16 + (g) - n1)
#define DMA_T(src, ldsoff) do { unsigned keep_; asm volatile("s_mov_b32 %0, m0\n\ts_mov_b32 m0, %3\n\ts_nop 2\n\tglobal_load_lds_dwordx4 %1, %2 sc1\n\tglobal_load_lds_dwordx4 %1, %2 offset:1024 sc1\n\ts_mov_b32 m0, %0" \
    : "=&s"(keep_) : "v"(vt2), "s"(src), "s"(dt2 + (ldsoff)) : "memory"); } while (0)
    const int wr = wid >> 2, wc = wid & 3, cls = j & 3;
    const char* kst = ws + WS_KF + ((size_t)(h * 64 + 8 * j + 2 * wr) * 8 + 2 * wc) * 1024;
    const char* vst = ws + WS_VF + ((size_t)(h * 128 + 2 * (8 * j + 2 * wr)) * 4 + wc) * 1024;
#define KVST(i) gstore_sc1_c<(((i) & 1) ? (((i) >> 3) * 8 + (((i) >> 2) & 1) * 2 + (((i) >> 1) & 1)) * 4096 : (((i) >> 3) * 4 + (((i) >> 2) & 1)) * 8192 + (((i) >> 1) & 1) * 1024)>(vl16, kvp[i], ((i) & 1) ? vst : kst)
    unsigned voffW;
    { const int row = 8 * (wid >> 1) + ((lane >> 2) & 7); const int chq = 2 * (wid & 1) + (lane >> 5);
      const int ch = 4 * chq + ((lane & 3) ^ ((row >> 2) & 3)); voffW = (unsigned)(row * 8192 + ch * 16); }
    const char* wqb = (const char*)wq + h * 256;
    const unsigned dstw = lds0 + L_Q + wid * 1024;
#define STAGE_Q1(c, i) glds16_c<(c) * 64 * 8192 + (i) * 32 * 8192>(voffW, wqb, dstw + (((c) + 2) & 3) * 16384 + (i) * 8192)
    const char* xb = ws + WS_XNF + (size_t)(8 * j + wid) * 40 * 1024;
    int boff[2];
    { const int blk = (lane >> 4) & 1, q = (lane & 15) >> 2, p = lane & 3;
#pragma unroll
      for (int t = 0; t < 2; ++t) boff[t] = L_Q + 2048 * hh + 64 * (4 * t + q) + (((2 * blk + (p >> 1)) ^ ((2 * hh + t) & 3)) << 4) + 8 * (p & 1); }
    u32x4 xf[3][4];
    f32x16 acc[4] = {};
    bf16x8 aq[6];
    unsigned fv = 0;
    ldscp bp0 = lds + boff[0], bp1 = lds + boff[1];
#define ARD(u) aq[(u) % 6] = cat8(vtr(bp0 + ((((u) >> 4) + 2) & 3) * 16384 + (((u) >> 2) & 3) * 4096 + ((u) & 3) * 512), vtr(bp1 + ((((u) >> 4) + 2) & 3) * 16384 + (((u) >> 2) & 3) * 4096 + ((u) & 3) * 512))
    GLOAD4(xf[0], vl16, xb);
    GLOAD4(xf[1], vl16, xb + 4096); STAGE_Q1(2, 0); STAGE_Q1(2, 1);
#pragma unroll
    for (int u = 0; u < 5; ++u) ARD(u);
    static_for<10>([&](auto cc_) {
      constexpr int c = decltype(cc_)::value;
#define QW(K) WAITVN(qproj_allowed(c, K))
      if (cls == 0) QW(0); else if (cls == 1) QW(1); else if (cls == 2) QW(2); else QW(3);
      asm volatile("" : "+v"(xf[c % 3][0]), "+v"(xf[c % 3][1]), "+v"(xf[c % 3][2]), "+v"(xf[c % 3][3]) :: "memory");
#undef QW
      if (c == 9 && wid == 0) {
        asm volatile("s_waitcnt vmcnt(0)" : "+v"(fv) :: "memory");
        kvmask = (unsigned)__builtin_amdgcn_ballot_w64(fv != 0u) & 0xffu;
        if (!(kvmask & 1u)) { wait_word(flags + FL_KV + 16 * (8 * h), 1u, flags + FL_TMO); kvmask |= 1u; }
        if (j == 0 && !(kvmask & 16u)) { wait_word(flags + FL_KV + 16 * (8 * h + 4), 1u, flags + FL_TMO); kvmask |= 16u; }
      }
      SBAR(); BAR();
      if (tid == 0 && ((cls == 0 && c == 8) || (cls == 1 && c == 9)))
        __hip_atomic_store(flags + FL_KV + 16 * (8 * h + j), 1u, __ATOMIC_RELAXED, __HIP_MEMORY_SCOPE_AGENT);
      SBAR();
      const char* xn_ = xb + (c + 2) * 4096;
      static_for<16>([&](auto ii_) {
        constexpr int i = decltype(ii_)::value;
        constexpr int u = 16 * c + i;
        acc[i & 3] = MFMA32(aq[u % 6], __builtin_bit_cast(bf16x8, xf[c % 3][i >> 2]), acc[i & 3]);
        if constexpr (u + 5 < 160) ARD(u + 5);
        if (c + 2 < 10) {
          if (i == 1) GLOAD1(xf[(c + 2) % 3][0], vl16, xn_, 0);
          if (i == 3) GLOAD1(xf[(c + 2) % 3][1], vl16, xn_, 1024);
          if (i == 5) GLOAD1(xf[(c + 2) % 3][2], vl16, xn_, 2048);
          if (i == 7) GLOAD1(xf[(c + 2) % 3][3], vl16, xn_, 3072);
        }
        if constexpr (c + 3 < 10) { if constexpr (i == 8) STAGE_Q1(c + 3, 0); if constexpr (i == 9) STAGE_Q1(c + 3, 1); }
        if constexpr (i >= 10) {
          static_for<4>([&](auto kk_) {
            constexpr int k = decltype(kk_)::value;
            constexpr int cnt = qproj_ns(c, k), base = qproj_sbase(c, k);
            if constexpr (((i - 10) & 1) == 0 && (i - 10) / 2 < cnt) { if (cls == k) KVST(base + (i - 10) / 2); }
          });
        }
        if (c == 8 && i == 8 && wid == 0)
          asm volatile("s_nop 4\n\tglobal_load_dword %0, %1, %2 sc1" : "+v"(fv) : "v"((unsigned)(FL_KV + 16 * (8 * h + (lane & 7))) * 4u), "s"(flags) : "memory");
        if (c == 9) {
          if (i == 0) DMA_T(Kh + (size_t)TILE(0) * 16384, L_K);
          if (i == 2) DMA_T(Vh + (size_t)TILE(0) * 16384, L_V);
          if (i == 4) DMA_T(Kh + (size_t)TILE(1) * 16384, L_K + 16384);
          if (i == 6) DMA_T(Vh + (size_t)TILE(1) * 16384, L_V + 16384);
          if (i == 8) DMA_T(Kh + (size_t)TILE(2) * 16384, L_K + 32768);
          if (i == 9) DMA_T(Kh + (size_t)TILE(3) * 16384, L_K + 49152);
        }
        SBAR();
      });
    });
#pragma unroll
    for (int dt = 0; dt < 4; ++dt) { acc[dt] = acc[dt] * QSCALE; qr[2 * dt] = pack8(acc[dt], 0); qr[2 * dt + 1] = pack8(acc[dt], 1); }
    if (cls == 3) WAIT_V(13); else WAIT_V(10);
    BAR();
    { const char* qst = ws + WS_QF + (size_t)(h * 64 + 8 * j + wid) * 8192;
      static_for<8>([&](auto ss_) { constexpr int s = decltype(ss_)::value; gstore_sc1_c<s * 1024>(vl16, qr[s], qst); }); }
#undef KVST
#undef STAGE_Q1
#undef ARD
#undef TILE
#undef DMA_T
  }
  const PfW pfw = {(const char*)wo, (const char*)w1, (const char*)w2};
  if (wid < 4) attn_main<0>(lds, lds0, ws, flags, h, j, wid, lane, tid, qr, kvmask, pfw);
  else attn_main<1>(lds, lds0, ws, flags, h, j, wid, lane, tid, qr, kvmask, pfw);
  WAIT_V(0); WAIT_L(0); BAR();
  if (tid == 0) __hip_atomic_fetch_add(flags + FL_C + 16 * ((7 - j) * 4 + (h >> 3)), 1u, __ATOMIC_RELAXED, __HIP_MEMORY_SCOPE_AGENT);
  tail_phase(smem, x, wo, w1, b1, w2, b2, ws, out, wid);
}

extern "C" void kernel_launch(void* const* d_in, const int* in_sizes, int n_in, void* d_out, int out_size, void* d_ws, size_t ws_size,
                              hipStream_t stream) {
  const u16* x = (const u16*)d_in[0];
  const u16* ckv = (const u16*)d_in[1];
  const u16* wuk = (const u16*)d_in[3];
  const u16* wuv = (const u16*)d_in[4];
  char* ws = (char*)d_ws;
  const u16* wq = (const u16*)d_in[2];
  const u16* wo = (const u16*)d_in[5];
  const u16* w1 = (const u16*)d_in[7];
  const u16* b1 = (const u16*)d_in[8];
  const u16* w2 = (const u16*)d_in[9];
  const u16* b2 = (const u16*)d_in[10];
  (void)hipFuncSetAttribute((const void*)kF1, hipFuncAttributeMaxDynamicSharedMemorySize, F1_LDS);
  hipLaunchKernelGGL(k0_ln, dim3(256), dim3(512), 0, stream, x, (u16*)(ws + WS_XNF), (unsigned*)(ws + WS_FLAGS));
  hipLaunchKernelGGL(kF1, dim3(256), dim3(512), F1_LDS, stream, ckv, wuk, wuv, wq, wo, w1, w2, x, b1, b2, ws, (u16*)d_out);
}
```

```cpp
#include <hip/hip_runtime.h>
#include <stdint.h>

#define LAS __attribute__((address_space(3)))
#define DI __device__ __forceinline__
typedef unsigned short u16;
typedef short bf16x8 __attribute__((ext_vector_type(8)));
typedef short s16x4 __attribute__((ext_vector_type(4)));
typedef float f32x2 __attribute__((ext_vector_type(2)));
typedef float f32x4 __attribute__((ext_vector_type(4)));
typedef float f32x16 __attribute__((ext_vector_type(16)));
typedef unsigned u32x2 __attribute__((ext_vector_type(2)));
typedef unsigned u32x4 __attribute__((ext_vector_type(4)));
typedef __bf16 bf16x2_t __attribute__((ext_vector_type(2)));
typedef LAS unsigned char* ldsp;
typedef LAS const unsigned char* ldscp;

constexpr int SEQ = 2048, DM = 640, DC = 512, NH = 32, HD = 128, FF = 1728;
constexpr float LN_EPS = 1e-5f;

constexpr size_t WS_FLAGS = 0;
constexpr size_t WS_XNF = 131072;
constexpr size_t WS_KF = WS_XNF + (size_t)SEQ * DM * 2;
constexpr size_t WS_VF = WS_KF + (size_t)NH * SEQ * HD * 2;
constexpr size_t WS_CTX = WS_VF + (size_t)NH * SEQ * HD * 2;
constexpr size_t WS_SLAB = WS_CTX + (size_t)NH * SEQ * HD * 2;
constexpr size_t WS_H = WS_SLAB + (size_t)4 * SEQ * DM * 4;
constexpr size_t WS_HNF = WS_H + (size_t)SEQ * DM * 2;
constexpr size_t WS_UF = WS_HNF + (size_t)SEQ * DM * 2;
constexpr size_t WS_QF = WS_UF + (size_t)DM * FF * 2;
constexpr size_t WS_PF = WS_QF + (size_t)NH * SEQ * HD * 2;
constexpr size_t WS_PS = WS_PF + (size_t)NH * SEQ * HD * 2;
constexpr size_t WS_END = WS_PS + (size_t)NH * 8 * 8 * 64 * 8;
constexpr int FL_TMO = 0;
constexpr int FL_KV = 24576;
constexpr int FL_Q = 4096;
constexpr int FL_P = 4096 + 8192;
constexpr int FL_WORDS = 32768;

#define SBAR() __builtin_amdgcn_sched_barrier(0)
#define BAR() asm volatile("s_barrier" ::: "memory")
#define WAIT_V(n) asm volatile("s_waitcnt vmcnt(" #n ")" ::: "memory")
#define WAIT_L(n) asm volatile("s_waitcnt lgkmcnt(" #n ")" ::: "memory")
#define MFMA32(a, b, c) __builtin_amdgcn_mfma_f32_32x32x16_bf16((a), (b), (c), 0, 0, 0)
#define MFMA16(a, b, c) __builtin_amdgcn_mfma_f32_16x16x32_bf16((a), (b), (c), 0, 0, 0)

DI unsigned cvtpk(float lo, float hi) { f32x2 v = {lo, hi}; bf16x2_t b = __builtin_convertvector(v, bf16x2_t); return __builtin_bit_cast(unsigned, b); }
DI float bflo(unsigned u) { return __uint_as_float(u << 16); }
DI float bfhi(unsigned u) { return __uint_as_float(u & 0xffff0000u); }
DI unsigned rfl(unsigned x) { return __builtin_amdgcn_readfirstlane(x); }
DI void glds16(unsigned voff, const void* sbase, unsigned lds_dst) {
  unsigned keep;
  asm volatile("s_mov_b32 %0, m0\n\ts_mov_b32 m0, %3\n\ts_nop 2\n\tglobal_load_lds_dwordx4 %1, %2\n\ts_mov_b32 m0, %0"
               : "=&s"(keep) : "v"(voff), "s"(sbase), "s"(lds_dst) : "memory");
}
typedef short v4i16_t __attribute__((ext_vector_type(4)));
DI s16x4 vtr(ldscp p) { return __builtin_bit_cast(s16x4, __builtin_amdgcn_ds_read_tr16_b64_v4i16((LAS v4i16_t*)p)); }
DI bf16x8 cat8(s16x4 lo, s16x4 hi) { return __builtin_shufflevector(lo, hi, 0, 1, 2, 3, 4, 5, 6, 7); }
DI bf16x8 ldsr128(ldscp p) { return *(const LAS bf16x8*)p; }
DI bf16x8 pack8(const f32x16& x, int s) {
  u32x4 w = {cvtpk(x[8 * s], x[8 * s + 1]), cvtpk(x[8 * s + 2], x[8 * s + 3]), cvtpk(x[8 * s + 4], x[8 * s + 5]), cvtpk(x[8 * s + 6], x[8 * s + 7])};
  return __builtin_bit_cast(bf16x8, w);
}
DI float wave_sum(float v) {
#pragma unroll
  for (int o = 32; o >= 1; o >>= 1) v += __shfl_xor(v, o);
  return v;
}

constexpr int K0_TOUCH_BYTES = 73728;
__global__ __launch_bounds__(512) void k0_ln(const u16* __restrict__ x, u16* __restrict__ xnf, unsigned* __restrict__ flags) {
  const int w = threadIdx.x >> 6, lane = threadIdx.x & 63;
  const int q = blockIdx.x * 8 + w;
  unsigned cpf = 0;
  { const unsigned ln = (blockIdx.x >> 3) * 32u + (unsigned)lane;
    if (w == 0 && lane < 32 && ln * 128u < (unsigned)K0_TOUCH_BYTES)
      asm volatile("global_load_dword %0, %1, off" : "+v"(cpf) : "v"((const char*)__builtin_amdgcn_s_getpc() + (size_t)ln * 128) : "memory"); }
  if (threadIdx.x < 128) flags[blockIdx.x * 128 + threadIdx.x] = 0u;
  const u32x4* row = (const u32x4*)(x + (size_t)q * DM);
  u32x4 c0 = row[lane];
  u32x4 c1 = {0u, 0u, 0u, 0u};
  if (lane < 16) c1 = row[64 + lane];
  float v[16];
#pragma unroll
  for (int i = 0; i < 4; ++i) { v[2 * i] = bflo(c0[i]); v[2 * i + 1] = bfhi(c0[i]); v[8 + 2 * i] = bflo(c1[i]); v[8 + 2 * i + 1] = bfhi(c1[i]); }
  float s = 0.f;
#pragma unroll
  for (int i = 0; i < 16; ++i) s += v[i];
  const float mean = wave_sum(s) * (1.0f / DM);
  float ss = 0.f;
#pragma unroll
  for (int i = 0; i < 8; ++i) { const float d = v[i] - mean; ss += d * d; }
  if (lane < 16) {
#pragma unroll
    for (int i = 8; i < 16; ++i) { const float d = v[i] - mean; ss += d * d; }
  }
  const float rstd = 1.0f / sqrtf(wave_sum(ss) * (1.0f / DM) + LN_EPS);
  u32x4 o0, o1;
#pragma unroll
  for (int i = 0; i < 4; ++i) { o0[i] = cvtpk((v[2 * i] - mean) * rstd, (v[2 * i + 1] - mean) * rstd); o1[i] = cvtpk((v[8 + 2 * i] - mean) * rstd, (v[8 + 2 * i + 1] - mean) * rstd); }
  u32x4* dst = (u32x4*)xnf + (size_t)(q >> 5) * 40 * 64 + (q & 31);
  dst[(lane >> 1) * 64 + (lane & 1) * 32] = o0;
  if (lane < 16) dst[((64 + lane) >> 1) * 64 + (lane & 1) * 32] = o1;
  asm volatile("s_waitcnt vmcnt(0)" : "+v"(cpf) :: "memory");
}

constexpr int F1_LDS = 163840;
constexpr int L_K = 0, L_V = 65536, L_Q = 98304;
constexpr float QSCALE = 0.08838834764831845f * 1.4426950408889634f;
constexpr float THR = 16.f;
#define PIN(x) asm volatile("" : "+v"(x))
#define EX2(v) __builtin_amdgcn_exp2f(v)
#define MEMFENCE() asm volatile("" ::: "memory")
DI float max3f(float a, float b, float c) { return __builtin_fmaxf(__builtin_fmaxf(a, b), c); }
DI float xhalf_max(float m) { auto rr = __builtin_amdgcn_permlane32_swap(__float_as_uint(m), __float_as_uint(m), false, false); return __builtin_fmaxf(__uint_as_float(rr[0]), __uint_as_float(rr[1])); }
DI float xhalf_sum(float m) { auto rr = __builtin_amdgcn_permlane32_swap(__float_as_uint(m), __float_as_uint(m), false, false); return __uint_as_float(rr[0]) + __uint_as_float(rr[1]); }
DI float max16(const f32x16& p) {
  float a = max3f(p[0], p[1], p[2]), b = max3f(p[3], p[4], p[5]);
  a = max3f(a, p[6], p[7]); b = max3f(b, p[8], p[9]); a = max3f(a, p[10], p[11]); b = max3f(b, p[12], p[13]); a = max3f(a, p[14], p[15]);
  return __builtin_fmaxf(a, b);
}
DI void cmask(f32x16& p0, f32x16& p1, int dq) {
#pragma unroll
  for (int r = 0; r < 16; ++r) { const int c = (r & 3) + 8 * (r >> 2); if (c > dq) p0[r] = -__builtin_inff(); if (c + 32 > dq) p1[r] = -__builtin_inff(); }
}
#define GLOAD4(d, voff, sbase) asm volatile("s_nop 4\n\tglobal_load_dwordx4 %0, %4, %5\n\tglobal_load_dwordx4 %1, %4, %5 offset:1024\n\tglobal_load_dwordx4 %2, %4, %5 offset:2048\n\tglobal_load_dwordx4 %3, %4, %5 offset:3072" \
    : "=&v"(d[0]), "=&v"(d[1]), "=&v"(d[2]), "=&v"(d[3]) : "v"(voff), "s"(sbase) : "memory")
#define GLOAD1(d, voff, sbase, OFF) asm volatile("s_nop 4\n\tglobal_load_dwordx4 %0, %1, %2 offset:" #OFF : "=&v"(d) : "v"(voff), "s"(sbase) : "memory")
#define GLOAD1_SC1(d, voff, sbase, OFF) asm volatile("s_nop 4\n\tglobal_load_dwordx4 %0, %1, %2 offset:" #OFF " sc1" : "=&v"(d) : "v"(voff), "s"(sbase) : "memory")
#define WAITV_TIE4(n, d) asm volatile("s_waitcnt vmcnt(" #n ")" : "+v"(d[0]), "+v"(d[1]), "+v"(d[2]), "+v"(d[3]) :: "memory")
template <int OFFS> DI void gstore_sc1_c(unsigned voff, const bf16x8& v, const void* sbase) {
  unsigned t;
  asm volatile("v_add_u32 %0, %4, %1\n\ts_nop 3\n\tglobal_store_dwordx4 %0, %2, %3 sc1\n\ts_nop 1" : "=&v"(t) : "v"(voff), "v"(v), "s"(sbase), "i"(OFFS) : "memory");
}
template <int OFFS> DI void glds16_c(unsigned voff, const void* sbase, unsigned lds_dst) {
  unsigned keep, t;
  asm volatile("s_mov_b32 %0, m0\n\ts_mov_b32 m0, %4\n\tv_add_u32 %1, %5, %2\n\ts_nop 1\n\tglobal_load_lds_dwordx4 %1, %3\n\ts_mov_b32 m0, %0"
               : "=&s"(keep), "=&v"(t) : "v"(voff), "s"(sbase), "s"(lds_dst), "i"(OFFS) : "memory");
}
#define WAITVN(n) asm volatile("s_waitcnt vmcnt(%0)" :: "i"(n) : "memory")
DI const char* upt(const char* p) {
  const unsigned long long v = (unsigned long long)p;
  return (const char*)(((unsigned long long)rfl((unsigned)(v >> 32)) << 32) | rfl((unsigned)v));
}
DI void wait_word(unsigned* word, unsigned want, unsigned* tmo) {
  for (unsigned spins = 0;; ++spins) {
    if ((unsigned)rfl(__hip_atomic_load(word, __ATOMIC_RELAXED, __HIP_MEMORY_SCOPE_AGENT)) >= want) break;
    if ((spins & 63u) == 63u) {
      if (spins > (1u << 15) || rfl(__hip_atomic_load(tmo, __ATOMIC_RELAXED, __HIP_MEMORY_SCOPE_AGENT)) != 0u) { __hip_atomic_store(tmo, 1u, __ATOMIC_RELAXED, __HIP_MEMORY_SCOPE_AGENT); break; }
    }
    __builtin_amdgcn_s_sleep(1);
  }
}

template <int V> struct IC { static constexpr int value = V; };
template <int N, int I = 0, class F> DI void static_for(F&& f) { if constexpr (I < N) { f(IC<I>{}); static_for<N, I + 1>(f); } }
DI int lane_id() { unsigned l; asm volatile("v_mbcnt_lo_u32_b32 %0, -1, 0\n\tv_mbcnt_hi_u32_b32 %0, -1, %0" : "=v"(l)); return (int)l; }
struct PfW { const char* wo; const char* w1; const char* w2; };
template <int GRP>
DI void attn_main(ldsp lds, unsigned lds0, char* __restrict__ ws, unsigned* flags, int h, int j, int wid, int lane, int tid, bf16x8 (&qr)[8], unsigned kvmask, const PfW& pfw) {
  const int l31 = lane & 31, hh = lane >> 5;
  const unsigned vl16 = lane * 16;
  const char* Kh = ws + WS_KF + (size_t)h * 64 * 8192;
  const char* Vh = ws + WS_VF + (size_t)h * 128 * 4096;
  const unsigned vt2 = (unsigned)(wid * 2048 + lane * 16);
  const unsigned dt2 = lds0 + wid * 2048;
  const int n1 = j >= 4 ? 16 : 4 * j + 2;
  const int t2 = j >= 4 ? 4 * (8 - j) - 2 : 16;
  const int jp = 7 - j;
#define TILE(g) ((g) < n1 ? (g) : ((g) < 18 ? t2 + (g) - n1 : 0))
#define KSRC(g) (Kh + (size_t)TILE(g) * 16384)
#define VSRC(g) (Vh + (size_t)TILE(g) * 16384)
#define GLDS_T(src, dst, OFF) do { unsigned keep_; asm volatile("s_mov_b32 %0, m0\n\ts_mov_b32 m0, %3\n\ts_nop 2\n\tglobal_load_lds_dwordx4 %1, %2 offset:" #OFF " sc1\n\ts_mov_b32 m0, %0" \
    : "=&s"(keep_) : "v"(vt2), "s"(src), "s"(dst) : "memory"); } while (0)
  const unsigned stoff = lane < 8 ? (unsigned)(FL_KV + 16 * (8 * h + lane)) * 4u : (lane == 8 ? (unsigned)(FL_Q + 32 * (8 * h + jp)) * 4u : (unsigned)(FL_P + 32 * (8 * h + jp)) * 4u);
  unsigned fv = 0;
#define ST_ISSUE() do { if (wid == 0 && kvmask != 0x3ffu) asm volatile("s_nop 4\n\tglobal_load_dword %0, %1, %2 sc1" : "+v"(fv) : "v"(stoff), "s"(flags) : "memory"); } while (0)
#define ST_READ(N) do { if (wid == 0 && kvmask != 0x3ffu) { asm volatile("s_waitcnt vmcnt(" #N ")" : "+v"(fv) :: "memory"); kvmask |= (unsigned)__builtin_amdgcn_ballot_w64(fv != 0u) & 0x3ffu; } } while (0)
#define ENSURE_KV(g) do { if (wid == 0) { const int bk_ = TILE(g) >> 2; if (!((kvmask >> bk_) & 1u)) { wait_word(flags + FL_KV + 16 * (8 * h + bk_), 1u, flags + FL_TMO); kvmask |= 1u << bk_; } } } while (0)
  const int rdoff = lane * 16;
  bf16x8 kq[4], vq[4], pw[4];
  f32x16 C0, C1, o[4], negm;
  float mhat, lsum;
  const f32x16 z = {};
  unsigned pfd = 0;
#pragma unroll
  for (int i = 0; i < 4; ++i) kq[i] = ldsr128(lds + L_K + rdoff + i * 1024);
  {
    ST_ISSUE();
    ldscp kb = lds + L_K + rdoff;
#pragma unroll
    for (int u = 0; u < 16; ++u) {
      if (u < 8) C0 = MFMA32(kq[u & 3], qr[u & 7], u == 0 ? z : C0); else C1 = MFMA32(kq[u & 3], qr[u & 7], u == 8 ? z : C1);
      if (u + 4 < 16) kq[u & 3] = ldsr128(kb + (u + 4) * 1024);
      if (GRP == 1 && u == 11) {
        SBAR(); WAIT_V(0); WAIT_L(0); BAR();
        GLDS_T(VSRC(2), dt2 + L_V + 32768, 0); GLDS_T(VSRC(2), dt2 + L_V + 32768, 1024);
        GLDS_T(KSRC(4), dt2 + L_K, 0);
      }
      SBAR();
    }
    if (j == 0) cmask(C0, C1, 32 * wid + l31 - 4 * hh);
    float rm = xhalf_max(__builtin_fmaxf(max16(C0), max16(C1)));
    mhat = __builtin_fmaxf(rm, -1e30f);
#pragma unroll
    for (int r = 0; r < 16; ++r) { C0[r] -= mhat; C1[r] -= mhat; negm[r] = -mhat; }
    PIN(negm);
    if (GRP == 0) {
      SBAR(); WAIT_V(0); ST_READ(0); ENSURE_KV(2); ENSURE_KV(4);
      WAIT_L(0); BAR();
      if (tid == 0) {
        __hip_atomic_store(flags + FL_Q + 32 * (8 * h + j), 1u, __ATOMIC_RELAXED, __HIP_MEMORY_SCOPE_AGENT);
        if ((j & 3) >= 2) __hip_atomic_store(flags + FL_KV + 16 * (8 * h + j), 1u, __ATOMIC_RELAXED, __HIP_MEMORY_SCOPE_AGENT);
      }
      GLDS_T(VSRC(2), dt2 + L_V + 32768, 0);
    } else {
      GLDS_T(KSRC(4), dt2 + L_K, 1024);
    }
#pragma unroll
    for (int i = 0; i < 4; ++i) kq[i] = ldsr128(lds + L_K + 16384 + rdoff + i * 1024);
    float s0 = 0.f;
#pragma unroll
    for (int r = 0; r < 16; ++r) { C0[r] = EX2(C0[r]); s0 += C0[r]; }
#pragma unroll
    for (int r = 0; r < 8; ++r) C1[r] = EX2(C1[r]);
    lsum = s0;
    pw[0] = pack8(C0, 0); pw[1] = pack8(C0, 1);
#pragma unroll
    for (int d = 0; d < 4; ++d) o[d] = z;
  }
  const int ddm = (wid >> 1) + 1;
  const int gA = ddm > 3 ? 18 : (4 * jp + ddm - t2 > 0 ? n1 + 4 * jp + ddm - t2 : n1);
  auto stepg = [&](auto aq_, auto ap_, const int g) {
    constexpr bool AQ = decltype(aq_)::value != 0, AP = decltype(ap_)::value != 0;
    const bool seam = (g == n1);
    const int X = g < n1 ? j : jp;
    const int dd = TILE(g) - 4 * X;
    ldscp kb = lds + L_K + (g & 3) * 16384 + rdoff;
    ldscp vb = lds + L_V + ((g - 1) & 3) * 16384 + rdoff;
    ldscp kn = lds + L_K + ((g + 1) & 3) * 16384 + rdoff;
    const char* vs_n = VSRC(g + 2); const char* ks_n = KSRC(g + 4);
    const char* vs_p = VSRC(g + 1); const char* ks_p = KSRC(g + 3);
    const unsigned vd_n = dt2 + L_V + ((g + 2) & 3) * 16384, kd_n = dt2 + L_K + (g & 3) * 16384;
    const unsigned vd_p = dt2 + L_V + ((g + 1) & 3) * 16384, kd_p = dt2 + L_K + ((g - 1) & 3) * 16384;
    const bool gq = (GRP == 0) || g > 1;
    float lsum1 = 0.f, mhat1 = 0.f;
    if (seam) {
      mhat1 = mhat; mhat = 0.f;
#pragma unroll
      for (int r = 0; r < 16; ++r) negm[r] = 0.f;
      PIN(negm);
    }
    if (g >= 2 && g < 12 && wid == ((g - 2) & 7)) {
      const unsigned lc = (unsigned)(g - 2) * 64u + lane, gid = blockIdx.x * 592u + lc;
      const char* p = gid < 40960u ? pfw.wo + (size_t)gid * 128 : (gid < 96256u ? pfw.w1 + (size_t)(gid - 40960u) * 128 : pfw.w2 + (size_t)(gid - 96256u) * 128);
      if (lc < 592u && gid < 151552u) asm volatile("global_load_dword %0, %1, off" : "+v"(pfd) : "v"(p) : "memory");
    }
    ST_ISSUE();
    float sa = 0.f, sb = 0.f;
    SBAR();
#define KRD(u) kq[(u) & 3] = ldsr128(kb + ((u) + 4) * 1024)
#define VRD(v) vq[(v) & 3] = ldsr128(vb + (v) * 1024)
#define KPRE(i) kq[i] = ldsr128(kn + (i) * 1024)
#define BARRIER_G() do { if (GRP == 0) { ST_READ(3); ENSURE_KV(g + 4); if (wid == 0 && g == n1 - 2 && j != 0 && !(kvmask & 256u)) wait_word(flags + FL_Q + 32 * (8 * h + jp), 1u, flags + FL_TMO); \
        if (g == 17 && wid == 0 && lane == 0) *(LAS unsigned*)(lds + 131072) = kvmask; } \
      if (g == n1 + 2) WAIT_V(5); else if (g < 17) WAIT_V(6); else WAIT_V(0);        \
      WAIT_L(0); BAR(); SBAR(); \
      if (GRP == 0 && (g == n1 + 2 || (g == 17 && n1 + 2 > 17)) && tid == 0) \
        __hip_atomic_store(flags + FL_P + 32 * (8 * h + j), 1u, __ATOMIC_RELAXED, __HIP_MEMORY_SCOPE_AGENT); } while (0)
    float fsc = 1.f; bool resc = false;
    {
#define Q_(x) do { if constexpr (AQ) { x; } } while (0)
#define P_(x) do { if constexpr (AP) { x; } } while (0)
      Q_(C0 = MFMA32(kq[0], qr[0], negm); KRD(0)); P_(C1[8] = EX2(C1[8]); sa += C1[0]; sb += C1[1]; sa += C1[2]; sb += C1[3]; PIN(C1); PIN(sa); PIN(sb)); SBAR();
      Q_(C0 = MFMA32(kq[1], qr[1], C0); KRD(1)); P_(C1[9] = EX2(C1[9]); sa += C1[4]; sb += C1[5]; sa += C1[6]; sb += C1[7]; PIN(C1); PIN(sa); PIN(sb)); SBAR();
      Q_(C0 = MFMA32(kq[2], qr[2], C0); KRD(2)); if (GRP == 0) GLDS_T(vs_p, vd_p, 1024); else if (gq) GLDS_T(ks_p, kd_p, 1024);
          P_(C1[10] = EX2(C1[10]); pw[2] = pack8(C1, 0); PIN(C1); PIN(pw[2])); SBAR();
      Q_(C0 = MFMA32(kq[3], qr[3], C0); KRD(3)); P_(C1[11] = EX2(C1[11]); C1[12] = EX2(C1[12]); sa += C1[8]; sb += C1[9]; PIN(C1); PIN(sa); PIN(sb)); SBAR();
      Q_(C0 = MFMA32(kq[0], qr[4], C0); KRD(4)); P_(C1[13] = EX2(C1[13]); C1[14] = EX2(C1[14]); sa += C1[10]; sb += C1[11]; PIN(C1); PIN(sa); PIN(sb)); SBAR();
      Q_(C0 = MFMA32(kq[1], qr[5], C0); KRD(5)); P_(C1[15] = EX2(C1[15]); sa += C1[12]; sb += C1[13]; sa += C1[14]; PIN(C1); PIN(sa); PIN(sb)); SBAR();
      Q_(C0 = MFMA32(kq[2], qr[6], C0); KRD(6)); P_(sb += C1[15]; pw[3] = pack8(C1, 1); PIN(pw[3]); PIN(sb)); SBAR();
      Q_(C0 = MFMA32(kq[3], qr[7], C0); KRD(7)); P_(lsum += sa + sb; PIN(lsum)); SBAR();
      Q_(C1 = MFMA32(kq[0], qr[0], negm); KRD(8)); if (GRP == 0) GLDS_T(ks_p, kd_p, 0); SBAR();
      Q_(C1 = MFMA32(kq[1], qr[1], C1); KRD(9)); SBAR();
      Q_(C1 = MFMA32(kq[2], qr[2], C1); KRD(10)); SBAR();
      float mxa = 0.f, mxb = 0.f;
      Q_(C1 = MFMA32(kq[3], qr[3], C1); KRD(11); mxa = max3f(C0[0], C0[1], C0[2]); mxb = max3f(C0[3], C0[4], C0[5]); PIN(mxa); PIN(mxb)); SBAR();
      if (GRP == 1) BARRIER_G();
      Q_(C1 = MFMA32(kq[0], qr[4], C1)); P_(VRD(0)); if (GRP == 1) GLDS_T(vs_n, vd_n, 0); Q_(mxa = max3f(mxa, C0[6], C0[7]); mxb = max3f(mxb, C0[8], C0[9]); PIN(mxa); PIN(mxb)); SBAR();
      Q_(C1 = MFMA32(kq[1], qr[5], C1)); P_(VRD(1)); Q_(mxa = max3f(mxa, C0[10], C0[11]); mxb = max3f(mxb, C0[12], C0[13]); PIN(mxa); PIN(mxb)); SBAR();
      Q_(C1 = MFMA32(kq[2], qr[6], C1)); P_(VRD(2)); Q_(mxa = max3f(mxa, C0[14], C0[15]); mxa = __builtin_fmaxf(mxa, mxb); PIN(mxa)); SBAR();
      Q_(C1 = MFMA32(kq[3], qr[7], C1)); P_(VRD(3)); SBAR();
      if (g == n1 - 1) {
        if (j == 0) wait_word(flags + FL_Q + 32 * (8 * h + jp), 1u, flags + FL_TMO);
        const char* qsrc = ws + WS_QF + (size_t)(h * 64 + 8 * jp + wid) * 8192;
        GLOAD1_SC1(qr[0], vl16, qsrc, 0); GLOAD1_SC1(qr[1], vl16, qsrc, 1024); GLOAD1_SC1(qr[2], vl16, qsrc, 2048); GLOAD1_SC1(qr[3], vl16, qsrc, 3072);
        GLOAD1_SC1(qr[4], vl16, qsrc + 4096, 0); GLOAD1_SC1(qr[5], vl16, qsrc + 4096, 1024); GLOAD1_SC1(qr[6], vl16, qsrc + 4096, 2048); GLOAD1_SC1(qr[7], vl16, qsrc + 4096, 3072);
      }
      SBAR();
      P_(o[0] = MFMA32(vq[0], pw[0], o[0]); VRD(4)); if (GRP == 1) GLDS_T(vs_n, vd_n, 1024); SBAR();
      P_(o[1] = MFMA32(vq[1], pw[0], o[1]); VRD(5)); SBAR();
      float mxc = 0.f, mxd = 0.f;
      P_(o[2] = MFMA32(vq[2], pw[0], o[2]); VRD(6)); if (GRP == 0) GLDS_T(ks_p, kd_p, 1024);
          Q_(mxc = max3f(C1[0], C1[1], C1[2]); mxd = max3f(C1[3], C1[4], C1[5]); mxc = max3f(mxc, C1[6], C1[7]); mxd = max3f(mxd, C1[8], C1[9]); PIN(mxc); PIN(mxd)); SBAR();
      P_(o[3] = MFMA32(vq[3], pw[0], o[3]); VRD(7)); Q_(mxc = max3f(mxc, C1[10], C1[11]); mxd = max3f(mxd, C1[12], C1[13]); mxc = max3f(mxc, C1[14], C1[15]); mxa = max3f(mxa, mxc, mxd); PIN(mxa)); SBAR();
      P_(o[0] = MFMA32(vq[0], pw[1], o[0]); VRD(8));
      MEMFENCE(); SBAR();
      if constexpr (AQ) {
        float rm = mxa;
        if (dd >= 0 && wid <= 2 * dd + 1) { cmask(C0, C1, 32 * wid + l31 - 64 * dd - 4 * hh); rm = __builtin_fmaxf(max16(C0), max16(C1)); }
        rm = xhalf_max(rm);
        if (__builtin_expect(seam || __any(rm > THR), 0)) {
          const float dl = seam ? __builtin_fmaxf(rm, -1e30f) : __builtin_fmaxf(rm, 0.f);
          mhat += dl;
#pragma unroll
          for (int r = 0; r < 16; ++r) { C0[r] -= dl; C1[r] -= dl; negm[r] = -mhat; }
          PIN(negm);
          if (seam) { lsum1 = lsum; lsum = 0.f; }
          else { fsc = EX2(-dl); lsum *= fsc; resc = true; }
        }
      } else {
        if (seam) { lsum1 = lsum; lsum = 0.f; mhat = -1e30f; }
      }
      SBAR();
      sa = 0.f; sb = 0.f;
      P_(o[1] = MFMA32(vq[1], pw[1], o[1]); VRD(9)); Q_(C0[0] = EX2(C0[0]); C0[1] = EX2(C0[1]); C0[2] = EX2(C0[2]); PIN(C0)); SBAR();
      P_(o[2] = MFMA32(vq[2], pw[1], o[2]); VRD(10)); Q_(C0[3] = EX2(C0[3]); C0[4] = EX2(C0[4]); sa += C0[0]; sb += C0[1]; PIN(C0); PIN(sa); PIN(sb)); SBAR();
      P_(o[3] = MFMA32(vq[3], pw[1], o[3]); VRD(11)); Q_(C0[5] = EX2(C0[5]); C0[6] = EX2(C0[6]); sa += C0[2]; sb += C0[3]; PIN(C0); PIN(sa); PIN(sb)); SBAR();
      P_(o[0] = MFMA32(vq[0], pw[2], o[0]); VRD(12)); if (GRP == 1) GLDS_T(ks_n, kd_n, 0); Q_(C0[7] = EX2(C0[7]); C0[8] = EX2(C0[8]); sa += C0[4]; sb += C0[5]; PIN(C0); PIN(sa); PIN(sb)); SBAR();
      P_(o[1] = MFMA32(vq[1], pw[2], o[1]); VRD(13)); Q_(pw[0] = pack8(C0, 0); C0[9] = EX2(C0[9]); PIN(pw[0]); PIN(C0)); SBAR();
      P_(o[2] = MFMA32(vq[2], pw[2], o[2]); VRD(14)); Q_(C0[10] = EX2(C0[10]); C0[11] = EX2(C0[11]); sa += C0[6]; sb += C0[7]; PIN(C0); PIN(sa); PIN(sb)); SBAR();
      P_(o[3] = MFMA32(vq[3], pw[2], o[3]); VRD(15)); Q_(C0[12] = EX2(C0[12]); C0[13] = EX2(C0[13]); sa += C0[8]; sb += C0[9]; PIN(C0); PIN(sa); PIN(sb)); SBAR();
      if (GRP == 0) BARRIER_G();
      P_(o[0] = MFMA32(vq[0], pw[3], o[0])); KPRE(0); if (GRP == 0) GLDS_T(vs_n, vd_n, 0); Q_(C0[14] = EX2(C0[14]); C0[15] = EX2(C0[15]); sa += C0[10]; sb += C0[11]; PIN(C0); PIN(sa); PIN(sb)); SBAR();
      P_(o[1] = MFMA32(vq[1], pw[3], o[1])); KPRE(1); Q_(C1[0] = EX2(C1[0]); C1[1] = EX2(C1[1]); sa += C0[12]; sb += C0[13]; PIN(C1); PIN(sa); PIN(sb)); SBAR();
      P_(o[2] = MFMA32(vq[2], pw[3], o[2])); KPRE(2); Q_(pw[1] = pack8(C0, 1); C1[2] = EX2(C1[2]); C1[3] = EX2(C1[3]); PIN(pw[1]); PIN(C1)); SBAR();
      P_(o[3] = MFMA32(vq[3], pw[3], o[3])); KPRE(3); Q_(C1[4] = EX2(C1[4]); C1[5] = EX2(C1[5]); sa += C0[14]; sb += C0[15]; PIN(C1); PIN(sa); PIN(sb)); SBAR();
      Q_(C1[6] = EX2(C1[6]); C1[7] = EX2(C1[7]); lsum += sa + sb; PIN(C1); PIN(lsum));
#undef Q_
#undef P_
    }
    if (resc) {
#pragma unroll
      for (int d = 0; d < 4; ++d) o[d] = o[d] * fsc;
    }
    if (seam) {
      const float lt = xhalf_sum(lsum1);
      const char* pst = ws + WS_PF + (size_t)((h * 8 + j) * 8 + wid) * 8192;
      static_for<4>([&](auto dd_) { constexpr int d = decltype(dd_)::value; gstore_sc1_c<(2 * d) * 1024>(vl16, pack8(o[d], 0), pst); gstore_sc1_c<(2 * d + 1) * 1024>(vl16, pack8(o[d], 1), pst); o[d] = z; });
      const unsigned long long sv = ((unsigned long long)__float_as_uint(lt) << 32) | __float_as_uint(mhat1);
      __hip_atomic_store((unsigned long long*)(ws + WS_PS) + (size_t)((h * 8 + j) * 8 + wid) * 64 + lane, sv, __ATOMIC_RELAXED, __HIP_MEMORY_SCOPE_AGENT);
    }
    if (g == n1 - 1) {
      asm volatile("s_waitcnt vmcnt(1)" : "+v"(qr[0]), "+v"(qr[1]), "+v"(qr[2]), "+v"(qr[3]), "+v"(qr[4]), "+v"(qr[5]), "+v"(qr[6]), "+v"(qr[7]) :: "memory");
    }
  };
  for (int g = 1; g < gA; ++g) stepg(IC<1>{}, IC<1>{}, g);
  if (gA < 18) stepg(IC<0>{}, IC<1>{}, gA);
  for (int g = gA + 1; g < 18; ++g) stepg(IC<0>{}, IC<0>{}, g);
  const bool actP = gA >= 18;
  asm volatile("" :: "v"(pfd));
  {
    const bool early = (rfl(*(const LAS unsigned*)(lds + 131072)) & 512u) != 0u;
    const char* psrc = upt(ws + WS_PF + (size_t)((h * 8 + jp) * 8 + wid) * 8192);
    const char* psrc2 = upt(psrc + 4096);
    const unsigned long long* svp = (const unsigned long long*)(ws + WS_PS) + (size_t)((h * 8 + jp) * 8 + wid) * 64 + lane;
    u32x4 qa[4], qb[4];
    u32x2 svv;
#define PLOADS() do { GLOAD1_SC1(qa[0], vl16, psrc, 0); GLOAD1_SC1(qa[1], vl16, psrc, 1024); GLOAD1_SC1(qa[2], vl16, psrc, 2048); GLOAD1_SC1(qa[3], vl16, psrc, 3072); \
      GLOAD1_SC1(qb[0], vl16, psrc2, 0); GLOAD1_SC1(qb[1], vl16, psrc2, 1024); GLOAD1_SC1(qb[2], vl16, psrc2, 2048); GLOAD1_SC1(qb[3], vl16, psrc2, 3072); \
      asm volatile("global_load_dwordx2 %0, %1, off sc1" : "=&v"(svv) : "v"(svp) : "memory"); } while (0)
    if (early) PLOADS();
    ldscp vb = lds + L_V + (17 & 3) * 16384 + rdoff;
    if (actP) {
#pragma unroll
    for (int i = 0; i < 4; ++i) vq[i] = ldsr128(vb + i * 1024);
    float sa = 0.f;
#pragma unroll
    for (int r = 8; r < 16; ++r) C1[r] = EX2(C1[r]);
#pragma unroll
    for (int r = 0; r < 16; ++r) sa += C1[r];
    lsum += sa;
    pw[2] = pack8(C1, 0); pw[3] = pack8(C1, 1);
    SBAR();
#pragma unroll
    for (int v = 0; v < 16; ++v) {
      o[v & 3] = MFMA32(vq[v & 3], pw[v >> 2], o[v & 3]);
      if (v + 4 < 16) vq[v & 3] = ldsr128(vb + (v + 4) * 1024);
      SBAR();
    }
    }
    lsum = xhalf_sum(lsum);
    if (!early) { wait_word(flags + FL_P + 32 * (8 * h + jp), 1u, flags + FL_TMO); PLOADS(); }
    WAITV_TIE4(0, qa); WAITV_TIE4(0, qb);
    asm volatile("" : "+v"(svv));
    const unsigned long long sv = ((unsigned long long)svv[1] << 32) | svv[0];
#undef PLOADS
    const float om = __uint_as_float((unsigned)sv), ol = __uint_as_float((unsigned)(sv >> 32));
    const float m = __builtin_fmaxf(mhat, om);
    const float f0 = EX2(mhat - m), f1 = EX2(om - m);
    const float inv = __builtin_amdgcn_rcpf(lsum * f0 + ol * f1);
    const float g0 = f0 * inv, g1 = f1 * inv;
    const char* cdst = upt(ws + WS_CTX + (size_t)((8 * jp + wid) * 32 + h) * 8192);
    static_for<8>([&](auto ii_) {
      constexpr int i = decltype(ii_)::value; constexpr int d = i >> 1, sp = i & 1;
      const u32x4 v4 = i < 4 ? qa[i & 3] : qb[i & 3];
      u32x4 w;
#pragma unroll
      for (int e = 0; e < 4; ++e) w[e] = cvtpk(o[d][8 * sp + 2 * e] * g0 + bflo(v4[e]) * g1, o[d][8 * sp + 2 * e + 1] * g0 + bfhi(v4[e]) * g1);
      gstore_sc1_c<i * 1024>(vl16, __builtin_bit_cast(bf16x8, w), cdst);
    });
  }
#undef KRD
#undef VRD
#undef KPRE
#undef BARRIER_G
#undef ENSURE_KV
#undef GLDS_T
#undef TILE
#undef KSRC
#undef VSRC
}

constexpr int FL_C = 28672;
constexpr int FL_P3 = 20480;
constexpr int FL_H = 22528;
constexpr int FL_U = 23552;
#define GLOAD4_SC1(d, voff, sbase) asm volatile("s_nop 4\n\tglobal_load_dwordx4 %0, %4, %5 sc1\n\tglobal_load_dwordx4 %1, %4, %5 offset:1024 sc1\n\tglobal_load_dwordx4 %2, %4, %5 offset:2048 sc1\n\tglobal_load_dwordx4 %3, %4, %5 offset:3072 sc1" \
    : "=&v"(d[0]), "=&v"(d[1]), "=&v"(d[2]), "=&v"(d[3]) : "v"(voff), "s"(sbase) : "memory")
template <int OFFS> DI void gstore_sc1_uc(unsigned voff, const u32x4& v, const void* sbase) {
  unsigned t;
  asm volatile("v_add_u32 %0, %4, %1\n\ts_nop 3\n\tglobal_store_dwordx4 %0, %2, %3 sc1\n\ts_nop 1" : "=&v"(t) : "v"(voff), "v"(v), "s"(sbase), "i"(OFFS) : "memory");
}
DI void gstore_sc1_u(unsigned voff, const u32x4& v, const void* sbase) {
  asm volatile("s_nop 4\n\tglobal_store_dwordx4 %0, %1, %2 sc1\n\ts_nop 1" :: "v"(voff), "v"(v), "s"(sbase) : "memory");
}
template <bool FIRST> struct MlpCfg {
  static constexpr int PITCH = FIRST ? FF * 2 : SEQ * 2;
  static constexpr int NKT = FIRST ? SEQ / 64 : FF / 64;
};
template <bool FIRST> DI unsigned mlp_voffw(int wid, int lane) {
  const int row = 8 * wid + (lane >> 3); const int f = ((row >> 1) & 1) | (((row >> 3) & 1) << 1);
  return (unsigned)(row * MlpCfg<FIRST>::PITCH + (((lane & 7) ^ (f << 1)) << 4));
}
template <bool FIRST, int KT0, int NK> DI void mlp_wstage(unsigned lds0, const char* wb, unsigned voffW, int wid, int reg) {
  static_for<NK>([&](auto kk_) {
    constexpr int k = decltype(kk_)::value; constexpr int kt = (KT0 + k) < MlpCfg<FIRST>::NKT ? (KT0 + k) : MlpCfg<FIRST>::NKT - 1;
    glds16_c<kt * 64 * MlpCfg<FIRST>::PITCH>(voffW, wb, lds0 + reg + k * 8192 + wid * 1024);
  });
}
struct MlpW { ldscp p[4]; };
DI MlpW mlp_wptr(ldsp lds, int wid, int lane) {
  const int g = lane >> 4, q = (lane & 15) >> 2, p = lane & 3; const int f = (q >> 1) | ((g & 1) << 1);
  MlpW w;
#pragma unroll
  for (int ct = 0; ct < 4; ++ct) w.p[ct] = lds + (wid >> 1) * 8192 + 128 * (32 * (wid & 1) + 8 * g + q) + (((2 * ct + (p >> 1)) ^ (f << 1)) << 4) + 8 * (p & 1);
  return w;
}
DI void mlp_wrd(const MlpW& w, int off, bf16x8* dst) {
#pragma unroll
  for (int ct = 0; ct < 4; ++ct) dst[ct] = cat8(vtr(w.p[ct] + off), vtr(w.p[ct] + off + 512));
}
#define MLP_ALOAD(dst, base) asm volatile("s_nop 4\n\tglobal_load_dwordx4 %0, %5, %7 sc1\n\tglobal_load_dwordx4 %1, %5, %7 offset:1024 sc1\n\tglobal_load_dwordx4 %2, %5, %7 offset:2048 sc1\n\t" \
    "global_load_dwordx4 %3, %5, %7 offset:3072 sc1\n\tglobal_load_dwordx4 %4, %6, %7 sc1" \
    : "=&v"(dst[0]), "=&v"(dst[1]), "=&v"(dst[2]), "=&v"(dst[3]), "=&v"(dst[4]) : "v"(vl16), "v"(vl16b), "s"(base) : "memory")
template <bool FIRST, int R0, int R1, int LK0, int NW>
DI void mlp_main(ldsp lds, const char* __restrict__ ab, int wid, int lane, const MlpW& wp, const bf16x8 (&wr)[NW], f32x4 (&acc)[4][5]) {
  constexpr int NT = 2 * MlpCfg<FIRST>::NKT, NS = (NT + 7) / 8;
  const unsigned vl16 = lane * 16, vl16b = lane * 16 + 4096;
  u32x4 af[3][5];
  bf16x8 wl[2][4];
#pragma unroll
  for (int c = 0; c < 4; ++c)
#pragma unroll
    for (int d = 0; d < 5; ++d) acc[c][d] = (f32x4){0.f, 0.f, 0.f, 0.f};
  auto abase = [&](int s) { const int T = 8 * s + wid; return upt(ab + (size_t)(T < NT ? T : NT - 1) * 40960); };
  { const char* b0 = abase(0); MLP_ALOAD(af[0], b0); const char* b1 = abase(1); MLP_ALOAD(af[1], b1); }
  if constexpr (!(0 >= R0 && 0 < R1)) mlp_wrd(wp, (0 - LK0) * 8192, wl[0]);
  static_for<NS>([&](auto ss_) {
    constexpr int s = decltype(ss_)::value;
    if constexpr (s + 2 < NS) { const char* bn = abase(s + 2); MLP_ALOAD(af[(s + 2) % 3], bn); }
    if constexpr (s + 1 < NS && !(s + 1 >= R0 && s + 1 < R1)) mlp_wrd(wp, (4 * (s + 1) - LK0) * 8192, wl[(s + 1) & 1]);
    constexpr int ahead = (NS - 1 - s) < 2 ? (NS - 1 - s) : 2;
    asm volatile("s_waitcnt vmcnt(%5)" : "+v"(af[s % 3][0]), "+v"(af[s % 3][1]), "+v"(af[s % 3][2]), "+v"(af[s % 3][3]), "+v"(af[s % 3][4]) : "i"(5 * ahead) : "memory");
    SBAR();
    if (8 * s + 7 < NT || 8 * s + wid < NT) {
#pragma unroll
      for (int c = 0; c < 4; ++c) {
        const bf16x8 wfr = (s >= R0 && s < R1) ? wr[(s >= R0 && s < R1) ? (s - R0) * 4 + c : 0] : wl[s & 1][c];
#pragma unroll
        for (int d = 0; d < 5; ++d) {
          const bf16x8 a8 = __builtin_bit_cast(bf16x8, af[s % 3][d]);
          acc[c][d] = FIRST ? MFMA16(wfr, a8, acc[c][d]) : MFMA16(a8, wfr, acc[c][d]);
        }
      }
    }
    SBAR();
  });
}
DI void mlp_park(ldsp lds, int wid, int lane, f32x4 (&acc)[4][5]) {
  ldsp pw_ = lds + wid * 10240 + lane * 16;
  ldsp pr_ = lds + (wid ^ 4) * 10240 + lane * 16;
  if (wid < 4) {
#pragma unroll
    for (int c = 0; c < 2; ++c)
#pragma unroll
      for (int d = 0; d < 5; ++d) *(LAS f32x4*)(pw_ + (c * 5 + d) * 1024) = acc[2 + c][d];
  } else {
#pragma unroll
    for (int c = 0; c < 2; ++c)
#pragma unroll
      for (int d = 0; d < 5; ++d) *(LAS f32x4*)(pw_ + (c * 5 + d) * 1024) = acc[c][d];
  }
  WAIT_L(0); BAR();
  if (wid < 4) {
#pragma unroll
    for (int c = 0; c < 2; ++c)
#pragma unroll
      for (int d = 0; d < 5; ++d) { const f32x4 o = *(const LAS f32x4*)(pr_ + (c * 5 + d) * 1024); *(LAS f32x4*)(pr_ + (c * 5 + d) * 1024) = acc[c][d] + o; }
  } else {
#pragma unroll
    for (int c = 0; c < 2; ++c)
#pragma unroll
      for (int d = 0; d < 5; ++d) { const f32x4 o = *(const LAS f32x4*)(pr_ + (c * 5 + d) * 1024); *(LAS f32x4*)(pr_ + (c * 5 + d) * 1024) = acc[2 + c][d] + o; }
  }
  WAIT_L(0); BAR();
}
DI void mlp_gather(ldscp lds, int c2, int dt, int gD, int c15, float (&v)[8]) {
  ldscp p = lds + ((c2 < 2 ? 4 : 0) * 10 + (c2 & 1) * 5 + dt) * 1024 + (gD * 16 + c15) * 16;
#pragma unroll
  for (int hf = 0; hf < 2; ++hf) {
    f32x4 a = {0.f, 0.f, 0.f, 0.f};
#pragma unroll
    for (int q = 0; q < 4; ++q) a += *(const LAS f32x4*)(p + q * 10240 + hf * 256);
#pragma unroll
    for (int e = 0; e < 4; ++e) v[4 * hf + e] = a[e];
  }
}

DI void tail_phase(unsigned char* smem, const u16* __restrict__ x, const u16* __restrict__ wo, const u16* __restrict__ w1, const u16* __restrict__ b1,
                   const u16* __restrict__ w2, const u16* __restrict__ b2, char* __restrict__ ws, u16* __restrict__ out, int wid) {
  const int b = blockIdx.x, xcd = b & 7, r = b >> 3;
  ldsp lds = (ldsp)smem;
  const unsigned lds0 = (unsigned)(uintptr_t)smem;
  unsigned* flags = (unsigned*)(ws + WS_FLAGS);
  const int ksp = xcd >> 1, panel = (xcd & 1) * 8 + (r >> 2), ctile = r & 3;
  const int idx5 = xcd * 27 + r;
  const int wt5 = idx5 >> 3, dg5 = idx5 & 7;
  const int wt6 = xcd * 4 + (r >> 3), dg6 = r & 7;
  const bool do5 = r < 27;
  constexpr int P4S = 131072;
  bf16x8 wf[16];
  {
    const int lane = lane_id();
    const unsigned vl16 = lane * 16;
    const int kh = wid >> 2, st = wid & 3, hh = lane >> 5;
    unsigned voff[5];
#pragma unroll
    for (int i = 0; i < 5; ++i) {
      const int id = 2 * (5 * wid + i) + (lane >> 5), half = id / 40, rem = id % 40, rg = rem / 5, c = rem % 5;
      const int row = 64 * half + 8 * rg + ((lane >> 2) & 7);
      voff[i] = (unsigned)(row * 1280 + (32 * c + 8 * ((lane & 3) ^ ((row >> 2) & 3))) * 2);
    }
    const char* wb = (const char*)wo + (size_t)(ksp * 1024) * 1280 + ctile * 320;
    const unsigned dstw = lds0 + wid * 5120;
#define OPIECE(hd, i) glds16_c<(hd) * 128 * 1280>(voff[i], wb, dstw + ((hd) & 3) * 40960 + (i) * 1024)
#define STAGE_O(hd) do { OPIECE(hd, 0); OPIECE(hd, 1); OPIECE(hd, 2); OPIECE(hd, 3); OPIECE(hd, 4); } while (0)
    const char* cb = ws + WS_CTX + ((size_t)((4 * panel + st) * 32 + 8 * ksp) * 8 + 4 * kh) * 1024;
    int boff[2];
    { const int blk = (lane >> 4) & 1, q = (lane & 15) >> 2, p = lane & 3;
#pragma unroll
      for (int t = 0; t < 2; ++t) boff[t] = kh * 20480 + 2560 * t + 64 * (4 * hh + q) + (((2 * blk + (p >> 1)) ^ ((2 * t + hh) & 3)) << 4) + 8 * (p & 1); }
    ldscp bp0 = lds + boff[0], bp1 = lds + boff[1];
    u32x4 xf[3][4];
    f32x16 acc[5] = {};
    bf16x8 aq[6];
#define ARD(U) aq[(U) % 6] = cat8(vtr(bp0 + (((U) / 20) & 3) * 40960 + (((U) % 20) / 5) * 5120 + ((U) % 5) * 512), vtr(bp1 + (((U) / 20) & 3) * 40960 + (((U) % 20) / 5) * 5120 + ((U) % 5) * 512))
    STAGE_O(0); STAGE_O(1); STAGE_O(2);
    if (wid == 0) wait_word(flags + FL_C + 16 * ((panel >> 1) * 4 + ksp), 8u, flags + FL_TMO);
    BAR();
    GLOAD4_SC1(xf[0], vl16, cb); GLOAD4_SC1(xf[1], vl16, cb + 8192);
    WAIT_V(8); BAR();
#pragma unroll
    for (int U = 0; U < 5; ++U) ARD(U);
    static_for<8>([&](auto hh_) {
      constexpr int hd = decltype(hh_)::value;
      if constexpr (hd == 0) { WAITV_TIE4(4, xf[0]); }
      else if constexpr (hd <= 5) { WAITV_TIE4(9, xf[hd % 3]); }
      else if constexpr (hd == 6) { WAITV_TIE4(4, xf[hd % 3]); }
      else { WAITV_TIE4(0, xf[hd % 3]); }
      SBAR(); BAR(); SBAR();
      const char* xn_ = cb + (size_t)(hd + 2) * 8192;
      static_for<20>([&](auto uu_) {
        constexpr int u = decltype(uu_)::value;
        constexpr int U = 20 * hd + u;
        acc[u % 5] = MFMA32(aq[U % 6], __builtin_bit_cast(bf16x8, xf[hd % 3][u / 5]), acc[u % 5]);
        if constexpr (U + 5 < 160) ARD(U + 5);
        if constexpr (hd + 2 < 8) {
          if constexpr (u == 1) GLOAD1_SC1(xf[(hd + 2) % 3][0], vl16, xn_, 0);
          if constexpr (u == 3) GLOAD1_SC1(xf[(hd + 2) % 3][1], vl16, xn_, 1024);
          if constexpr (u == 5) GLOAD1_SC1(xf[(hd + 2) % 3][2], vl16, xn_, 2048);
          if constexpr (u == 7) GLOAD1_SC1(xf[(hd + 2) % 3][3], vl16, xn_, 3072);
        }
        if constexpr (hd + 3 < 8) {
          if constexpr (u == 9) OPIECE(hd + 3, 0);
          if constexpr (u == 11) OPIECE(hd + 3, 1);
          if constexpr (u == 13) OPIECE(hd + 3, 2);
          if constexpr (u == 15) OPIECE(hd + 3, 3);
          if constexpr (u == 17) OPIECE(hd + 3, 4);
        }
        SBAR();
      });
    });
#undef OPIECE
#undef STAGE_O
#undef ARD
    BAR();
    ldsp xw = lds + (kh == 0 ? 49152 : 0) + st * 12288 + lane * 16;
    ldscp xr = lds + (kh == 0 ? 0 : 49152) + st * 12288 + lane * 16;
    if (kh == 0) {
#pragma unroll
      for (int i = 0; i < 2; ++i)
#pragma unroll
        for (int g = 0; g < 4; ++g) *(LAS f32x4*)(xw + (i * 4 + g) * 1024) = (f32x4){acc[3 + i][4 * g], acc[3 + i][4 * g + 1], acc[3 + i][4 * g + 2], acc[3 + i][4 * g + 3]};
    } else {
#pragma unroll
      for (int i = 0; i < 3; ++i)
#pragma unroll
        for (int g = 0; g < 4; ++g) *(LAS f32x4*)(xw + (i * 4 + g) * 1024) = (f32x4){acc[i][4 * g], acc[i][4 * g + 1], acc[i][4 * g + 2], acc[i][4 * g + 3]};
    }
    WAIT_L(0); BAR();
    const char* sb = ws + WS_SLAB + ((size_t)((ksp * 64 + 4 * panel + st) * 20 + 5 * ctile) * 2) * 1024;
#define SLABST(I, T) static_for<2>([&](auto pp_) { \
          constexpr int sp = decltype(pp_)::value; \
          const f32x4 o0 = *(const LAS f32x4*)(xr + ((I) * 4 + 2 * sp) * 1024), o1 = *(const LAS f32x4*)(xr + ((I) * 4 + 2 * sp + 1) * 1024); \
          const u32x4 w = {cvtpk(acc[T][8 * sp] + o0[0], acc[T][8 * sp + 1] + o0[1]), cvtpk(acc[T][8 * sp + 2] + o0[2], acc[T][8 * sp + 3] + o0[3]), \
                           cvtpk(acc[T][8 * sp + 4] + o1[0], acc[T][8 * sp + 5] + o1[1]), cvtpk(acc[T][8 * sp + 6] + o1[2], acc[T][8 * sp + 7] + o1[3])}; \
          gstore_sc1_uc<((T) * 2 + sp) * 1024>(vl16, w, sb); })
    if (kh == 0) { SLABST(0, 0); SLABST(1, 1); SLABST(2, 2); }
    else { SLABST(0, 3); SLABST(1, 4); }
#undef SLABST
    WAIT_V(0); WAIT_L(0); BAR();
    if (wid == 0 && lane == 0) __hip_atomic_fetch_add(flags + FL_P3 + 64 * panel, 1u, __ATOMIC_RELAXED, __HIP_MEMORY_SCOPE_AGENT);
  }
  {
    const int lane = lane_id(), tid = wid * 64 + lane;
    const MlpW wp = mlp_wptr(lds, wid, lane);
    const char* w1b = (const char*)w1 + wt5 * 128;
    const unsigned voffW = mlp_voffw<true>(wid, lane);
    ldsp tr = lds + P4S;
    LAS float* red = (LAS float*)(lds + P4S + 10240);
    ldsp hrow = lds + P4S + 11264;
    const int rr = tid & 7, pc = tid >> 3;
    const int r8 = ksp * 4 + ctile;
    const int s = 128 * panel + 8 * r8 + rr, st = s >> 5;
    int n0[2]; unsigned so[2];
    u32x2 xv[2][2];
#pragma unroll
    for (int i = 0; i < 2; ++i) {
      const int pi = pc + 64 * i; const int pcl = pi < 80 ? pi : 0;
      const int nt = pcl >> 2, sp = (pcl >> 1) & 1, hh = pcl & 1;
      n0[i] = 32 * nt + 16 * sp + 4 * hh;
      so[i] = (unsigned)((((st * 20 + nt) * 2 + sp) * 64 + (s & 31) + 32 * hh) * 16);
      xv[i][0] = *(const u32x2*)(x + (size_t)s * DM + n0[i]); xv[i][1] = *(const u32x2*)(x + (size_t)s * DM + n0[i] + 8);
    }
    asm volatile("" : "+v"(xv[0][0]), "+v"(xv[0][1]), "+v"(xv[1][0]), "+v"(xv[1][1]));
    if (do5) mlp_wstage<true, 0, 16>(lds0, w1b, voffW, wid, 0);
    if (wid == 0) wait_word(flags + FL_P3 + 64 * panel, 16u, flags + FL_TMO);
    BAR();
    u32x4 sv[2][4];
    const char* sl = ws + WS_SLAB;
#pragma unroll
    for (int i = 0; i < 2; ++i)
#pragma unroll
      for (int k = 0; k < 4; ++k) GLOAD1_SC1(sv[i][k], so[i], sl + (size_t)k * (64 * 20 * 2 * 1024), 0);
    WAITV_TIE4(0, sv[0]); WAITV_TIE4(0, sv[1]);
    float hv[2][8];
    unsigned pk[2][4];
    float sum = 0.f, ssq = 0.f;
#pragma unroll
    for (int i = 0; i < 2; ++i) {
      const bool ok = pc + 64 * i < 80;
#pragma unroll
      for (int e = 0; e < 4; ++e) {
        const float lo = (bflo(sv[i][0][e]) + bflo(sv[i][1][e])) + (bflo(sv[i][2][e]) + bflo(sv[i][3][e]));
        const float hi = (bfhi(sv[i][0][e]) + bfhi(sv[i][1][e])) + (bfhi(sv[i][2][e]) + bfhi(sv[i][3][e]));
        const unsigned xw_ = xv[i][e >> 1][e & 1];
        pk[i][e] = cvtpk(lo + bflo(xw_), hi + bfhi(xw_));
        hv[i][2 * e] = ok ? bflo(pk[i][e]) : 0.f; hv[i][2 * e + 1] = ok ? bfhi(pk[i][e]) : 0.f;
        sum += hv[i][2 * e] + hv[i][2 * e + 1];
      }
    }
    sum += __shfl_xor(sum, 8); sum += __shfl_xor(sum, 16); sum += __shfl_xor(sum, 32);
    const float cntw = (wid < 2) ? 128.f : 64.f;
    const float mw = sum / cntw;
#pragma unroll
    for (int i = 0; i < 2; ++i) {
      if (pc + 64 * i < 80) {
#pragma unroll
        for (int e = 0; e < 8; ++e) { const float d = hv[i][e] - mw; ssq += d * d; }
      }
    }
    ssq += __shfl_xor(ssq, 8); ssq += __shfl_xor(ssq, 16); ssq += __shfl_xor(ssq, 32);
    if (lane < 8) { red[(wid * 8 + lane) * 2] = mw; red[(wid * 8 + lane) * 2 + 1] = ssq; }
    WAIT_L(0); BAR();
    float mean = 0.f;
#pragma unroll
    for (int w = 0; w < 8; ++w) mean += red[(w * 8 + rr) * 2] * (w < 2 ? 128.f : 64.f);
    mean *= (1.0f / DM);
    float m2 = 0.f;
#pragma unroll
    for (int w = 0; w < 8; ++w) { const float dm = red[(w * 8 + rr) * 2] - mean; m2 += red[(w * 8 + rr) * 2 + 1] + dm * dm * (w < 2 ? 128.f : 64.f); }
    const float rstd = 1.0f / sqrtf(m2 * (1.0f / DM) + LN_EPS);
#pragma unroll
    for (int i = 0; i < 2; ++i) {
      if (pc + 64 * i < 80) {
        LAS u16* t16 = (LAS u16*)tr;
#pragma unroll
        for (int e = 0; e < 4; ++e) {
          const unsigned p = cvtpk((hv[i][2 * e] - mean) * rstd, (hv[i][2 * e + 1] - mean) * rstd);
          const int n = n0[i] + 8 * (e >> 1) + 2 * (e & 1);
          t16[n * 8 + rr] = (u16)(p & 0xffff); t16[(n + 1) * 8 + rr] = (u16)(p >> 16);
        }
        *(LAS u32x2*)(hrow + rr * 1280 + n0[i] * 2) = (u32x2){pk[i][0], pk[i][1]};
        *(LAS u32x2*)(hrow + rr * 1280 + n0[i] * 2 + 16) = (u32x2){pk[i][2], pk[i][3]};
      }
    }
    WAIT_L(0); BAR();
    const char* hdst = ws + WS_HNF + ((size_t)st * 40 * 64 + ((s >> 3) & 3) * 16) * 16;
    { const int d = tid; const u32x4 tv4 = *(const LAS u32x4*)(tr + d * 16); gstore_sc1_u((unsigned)(((d >> 4) * 64 + (d & 15)) * 16), tv4, hdst); }
    if (tid < 128) { const int d = 512 + tid; const u32x4 tv4 = *(const LAS u32x4*)(tr + d * 16); gstore_sc1_u((unsigned)(((d >> 4) * 64 + (d & 15)) * 16), tv4, hdst); }
    {
      const char* hd2 = ws + WS_H + (size_t)(128 * panel + 8 * r8) * 1280;
      { const u32x4 hv4 = *(const LAS u32x4*)(hrow + tid * 16); gstore_sc1_u((unsigned)(tid * 16), hv4, hd2); }
      if (tid < 128) { const u32x4 hv4 = *(const LAS u32x4*)(hrow + 8192 + tid * 16); gstore_sc1_u((unsigned)(8192 + tid * 16), hv4, hd2); }
    }
    if (do5) {
#pragma unroll
      for (int s4 = 0; s4 < 4; ++s4) mlp_wrd(wp, s4 * 32768, wf + 4 * s4);
    }
    WAIT_V(0); WAIT_L(0); BAR();
    if (tid == 0) __hip_atomic_fetch_add(flags + FL_H + 64 * (b & 7), 1u, __ATOMIC_RELAXED, __HIP_MEMORY_SCOPE_AGENT);
    if (do5) mlp_wstage<true, 16, 16>(lds0, w1b, voffW, wid, 0);
  }
  constexpr int RHI = 81920;
  {
    const int lane = lane_id(), tid = wid * 64 + lane;
    const char* w2b = (const char*)w2 + wt6 * 128;
    const unsigned voffW2 = mlp_voffw<false>(wid, lane);
    if (do5) {
      if (wid == 0) {
        for (unsigned spins = 0;; ++spins) {
          const unsigned v = __hip_atomic_load(flags + FL_H + 64 * (lane & 7), __ATOMIC_RELAXED, __HIP_MEMORY_SCOPE_AGENT);
          if (__all(v >= 32u)) break;
          if ((spins & 63u) == 63u && (spins > (1u << 15) || rfl(__hip_atomic_load(flags + FL_TMO, __ATOMIC_RELAXED, __HIP_MEMORY_SCOPE_AGENT)) != 0u)) { __hip_atomic_store(flags + FL_TMO, 1u, __ATOMIC_RELAXED, __HIP_MEMORY_SCOPE_AGENT); break; }
          __builtin_amdgcn_s_sleep(1);
        }
      }
      WAIT_V(0); WAIT_L(0); BAR();
      f32x4 acc[4][5];
      { const MlpW wp = mlp_wptr(lds, wid, lane); mlp_main<true, 0, 4, 16, 16>(lds, ws + WS_HNF + (size_t)(5 * dg5) * 1024, wid, lane, wp, wf, acc); }
      BAR();
      mlp_park(lds, wid, lane, acc);
      {
        float v[2][8]; u32x4 bv[2];
#pragma unroll
        for (int k2 = 0; k2 < 2; ++k2) {
          const int it = k2 == 0 ? tid : (tid < 128 ? 512 + tid : tid);
          const int d15 = it & 15, gp = (it >> 4) & 3, fr = it >> 6, dt = fr % 5, Tp = fr / 5;
          bv[k2] = *(const u32x4*)(b1 + 64 * wt5 + 32 * Tp + 8 * gp);
          mlp_gather(lds, 2 * Tp + (gp >> 1), dt, 2 * (gp & 1), d15, v[k2]);
        }
#pragma unroll
        for (int k2 = 0; k2 < 2; ++k2) {
          if (k2 == 0 || tid < 128) {
            const int it = k2 == 0 ? tid : 512 + tid;
            const int d15 = it & 15, gp = (it >> 4) & 3, fr = it >> 6, dt = fr % 5, Tp = fr / 5;
            u32x4 o;
#pragma unroll
            for (int e = 0; e < 4; ++e) {
              float z[2] = {v[k2][2 * e] + bflo(bv[k2][e]), v[k2][2 * e + 1] + bfhi(bv[k2][e])};
#pragma unroll
              for (int k = 0; k < 2; ++k) {
                const float xx = z[k];
                const float a = xx * (1.0f + 0.044715f * xx * xx) * (-2.0f * 0.7978845608028654f * 1.4426950408889634f);
                z[k] = xx * __builtin_amdgcn_rcpf(1.0f + __builtin_amdgcn_exp2f(a));
              }
              o[e] = cvtpk(z[0], z[1]);
            }
            gstore_sc1_u((unsigned)((((2 * wt5 + Tp) * 40 + 5 * dg5 + dt) * 64 + gp * 16 + d15) * 16), o, ws + WS_UF);
          }
        }
      }
      mlp_wstage<false, 20, 8>(lds0, w2b, voffW2, wid, RHI);
      WAIT_V(8); WAIT_L(0); BAR();
      if (tid == 0) __hip_atomic_fetch_add(flags + FL_U + 64 * dg5, 1u, __ATOMIC_RELAXED, __HIP_MEMORY_SCOPE_AGENT);
    } else {
      WAIT_L(0); BAR();
      mlp_wstage<false, 20, 8>(lds0, w2b, voffW2, wid, RHI);
    }
  }
  {
    const int lane = lane_id(), tid = wid * 64 + lane;
    bf16x8 wg[8];
    const MlpW wp = mlp_wptr(lds, wid, lane);
    const char* w2b = (const char*)w2 + wt6 * 128;
    const unsigned voffW2 = mlp_voffw<false>(wid, lane);
    mlp_wstage<false, 0, 10>(lds0, w2b, voffW2, wid, 0);
    WAIT_V(10); BAR();
    mlp_wrd(wp, RHI, wg); mlp_wrd(wp, RHI + 32768, wg + 4);
    WAIT_L(0); BAR();
    mlp_wstage<false, 10, 10>(lds0, w2b, voffW2, wid, RHI);
    if (wid == 0) wait_word(flags + FL_U + 64 * dg6, 27u, flags + FL_TMO);
    WAIT_V(0); BAR();
    const char* ab = ws + WS_UF + (size_t)(5 * dg6) * 1024;
    f32x4 acc[4][5];
    mlp_main<false, 5, 7, 0, 8>(lds, ab, wid, lane, wp, wg, acc);
    u32x4 hv[2]; float bb[2]; size_t oo[2];
#pragma unroll
    for (int k2 = 0; k2 < 2; ++k2) {
      const int it = k2 == 0 ? tid : (tid < 128 ? 512 + tid : tid);
      const int sl = it / 10, c = it % 10, srow = 64 * wt6 + sl;
      oo[k2] = (size_t)srow * DM + 80 * dg6 + 8 * c;
      GLOAD1_SC1(hv[k2], (unsigned)(oo[k2] * 2), ws + WS_H, 0);
      bb[k2] = bflo((unsigned)b2[srow]);
    }
    BAR();
    mlp_park(lds, wid, lane, acc);
    float v[2][8];
#pragma unroll
    for (int k2 = 0; k2 < 2; ++k2) {
      const int it = k2 == 0 ? tid : (tid < 128 ? 512 + tid : tid);
      const int sl = it / 10, c = it % 10;
      mlp_gather(lds, sl >> 4, c >> 1, 2 * (c & 1), sl & 15, v[k2]);
    }
    asm volatile("s_waitcnt vmcnt(0)" : "+v"(hv[0]), "+v"(hv[1]) :: "memory");
#pragma unroll
    for (int k2 = 0; k2 < 2; ++k2) {
      if (k2 == 0 || tid < 128) {
        u32x4 ov;
#pragma unroll
        for (int e = 0; e < 4; ++e) ov[e] = cvtpk(v[k2][2 * e] + bb[k2] + bflo(hv[k2][e]), v[k2][2 * e + 1] + bb[k2] + bfhi(hv[k2][e]));
        *(u32x4*)(out + oo[k2]) = ov;
      }
    }
  }
}

constexpr int CODE_BYTES = 73728;
constexpr int qproj_ns(int c, int cls) {
  constexpr int tab[4][10] = {{3, 3, 3, 3, 2, 2, 0, 0, 0, 0}, {1, 2, 2, 2, 3, 3, 3, 0, 0, 0}, {0, 1, 1, 2, 2, 2, 2, 3, 3, 0}, {0, 0, 1, 1, 1, 2, 2, 3, 3, 3}};
  return (c < 0 || c > 9) ? 0 : tab[cls][c];
}
constexpr int qproj_sbase(int c, int cls) { int n = 0; for (int i = 0; i < c; ++i) n += qproj_ns(i, cls); return n; }
constexpr int qproj_nl(int c) { return (c + 2 < 10 ? 4 : 0) + (c + 3 < 10 ? 2 : 0); }
constexpr int qproj_allowed(int c, int cls) {
  return c == 0 ? 6 : (c == 1 ? qproj_nl(0) + qproj_ns(0, cls) : qproj_ns(c - 2, cls) + qproj_nl(c - 1) + qproj_ns(c - 1, cls));
}
__global__ __launch_bounds__(512, 2) void kF1(const u16* __restrict__ ckv, const u16* __restrict__ wuk, const u16* __restrict__ wuv, const u16* __restrict__ wq,
                                              const u16* __restrict__ wo, const u16* __restrict__ w1, const u16* __restrict__ w2,
                                              const u16* __restrict__ x, const u16* __restrict__ b1, const u16* __restrict__ b2, char* __restrict__ ws, u16* __restrict__ out) {
  extern __shared__ __attribute__((aligned(16))) unsigned char smem[];
  const int bidx = blockIdx.x, xcd = bidx & 7, rr_ = bidx >> 3;
  const int h = xcd * 4 + (rr_ >> 3), j = rr_ & 7;
  const int wid = rfl(threadIdx.x >> 6);
  ldsp lds = (ldsp)smem;
  const unsigned lds0 = (unsigned)(uintptr_t)smem;
  unsigned* flags = (unsigned*)(ws + WS_FLAGS);
  const char* code0 = (const char*)__builtin_amdgcn_s_getpc();
  unsigned cpf = 0;
#define CODE_TOUCH() do { const unsigned ln_ = (unsigned)rr_ * 32u + (threadIdx.x & 63u); if (wid == 7 && (threadIdx.x & 63u) < 32u && ln_ * 128u < (unsigned)CODE_BYTES) \
    asm volatile("global_load_dword %0, %1, off" : "+v"(cpf) : "v"(code0 + (size_t)ln_ * 128) : "memory"); } while (0)
  CODE_TOUCH();
  bf16x8 kvp[16];
  {
    const int wr = wid >> 2, wc = wid & 3;
    const int lane = threadIdx.x & 63, l31 = lane & 31, hh = lane >> 5;
    unsigned voffC, voffW;
    { const int row = 8 * wid + (lane >> 3); voffC = (unsigned)(row * 1024 + (((lane & 7) ^ ((row >> 1) & 7)) << 4)); }
    { const int row = 8 * (wid >> 1) + ((lane >> 2) & 7); const int chq = 2 * (wid & 1) + (lane >> 5);
      const int ch = 4 * chq + ((lane & 3) ^ ((row >> 2) & 3)); voffW = (unsigned)(row * 8192 + ch * 16); }
    const char* cbase = (const char*)ckv + (size_t)(256 * j) * 1024;
    const char* wkb = (const char*)wuk + h * 256;
    const char* wvb = (const char*)wuv + h * 256;
    const unsigned dstw = lds0 + wid * 1024;
#define CBUF(b, a) (((b) * 2 + (a)) * 16384)
#define WBUF(b, a) (65536 + ((b) * 2 + (a)) * 16384)
#define STAGE_C(b, a, kt) do { const char* s_ = cbase + (a) * 131072 + (kt) * 128; glds16(voffC, s_, dstw + CBUF(b, a)); glds16(voffC, s_ + 65536, dstw + CBUF(b, a) + 8192); } while (0)
#define STAGE_W(b, a, kt) do { const char* s_ = ((a) ? wvb : wkb) + (size_t)(kt) * 64 * 8192; glds16(voffW, s_, dstw + WBUF(b, a)); glds16(voffW, s_ + 32 * 8192, dstw + WBUF(b, a) + 8192); } while (0)
    int aoff[4], boff[2];
#pragma unroll
    for (int s = 0; s < 4; ++s) aoff[s] = 128 * (wr * 64 + l31) + (((2 * s + hh) ^ ((l31 >> 1) & 7)) << 4);
    { const int blk = (lane >> 4) & 1, q = (lane & 15) >> 2, p = lane & 3;
#pragma unroll
      for (int t = 0; t < 2; ++t) boff[t] = 2048 * hh + 512 * wc + 64 * (4 * t + q) + (((2 * blk + (p >> 1)) ^ ((2 * hh + t) & 3)) << 4) + 8 * (p & 1); }
#define LDA(dst, b, a) do { _Pragma("unroll") for (int m = 0; m < 2; ++m) _Pragma("unroll") for (int s = 0; s < 4; ++s) dst[m][s] = ldsr128(lds + CBUF(b, a) + m * 4096 + aoff[s]); } while (0)
#define LDB(dst, b, a) do { _Pragma("unroll") for (int s = 0; s < 4; ++s) dst[s] = cat8(vtr(lds + WBUF(b, a) + s * 4096 + boff[0]), vtr(lds + WBUF(b, a) + s * 4096 + boff[1])); } while (0)
#define MMAK(ai, At, Bt) do { __builtin_amdgcn_s_setprio(1); _Pragma("unroll") for (int s = 0; s < 4; ++s) _Pragma("unroll") for (int m = 0; m < 2; ++m) \
    accK[ai][m] = MFMA32(Bt[s], At[m][s], accK[ai][m]); __builtin_amdgcn_s_setprio(0); } while (0)
#define MMAV(ai, At, Bt) do { __builtin_amdgcn_s_setprio(1); _Pragma("unroll") for (int s = 0; s < 4; ++s) _Pragma("unroll") for (int m = 0; m < 2; ++m) \
    accV[ai][m] = MFMA32(At[m][s], Bt[s], accV[ai][m]); __builtin_amdgcn_s_setprio(0); } while (0)
    f32x16 accK[2][2] = {}, accV[2][2] = {};
    bf16x8 At[2][4], B0[4], B1[4];
    constexpr int nt = DC / 64;
    STAGE_W(0, 0, 0); STAGE_C(0, 0, 0); STAGE_W(0, 1, 0); STAGE_C(0, 1, 0);
    if (wr == 1) BAR();
    WAIT_V(4); BAR();
    STAGE_W(1, 0, 1); STAGE_C(1, 0, 1); STAGE_W(1, 1, 1);
    WAIT_V(6); BAR();
    unsigned pfd = 0;
    if (wid < 3) {
      const int ln = wid * 64 + lane;
      if (ln < 160) asm volatile("global_load_dword %0, %1, off" : "+v"(pfd) : "v"((const char*)wq + (size_t)(80 * j + (ln >> 1)) * 8192 + h * 256 + (ln & 1) * 128) : "memory");
    }
    for (int t = 0; t < nt - 2; t += 2) {
      LDB(B0, 0, 0); SBAR(); LDA(At, 0, 0); STAGE_C(1, 1, t + 1);
      WAIT_L(8); BAR(); WAIT_L(0); MMAK(0, At, B0); BAR(); SBAR();
      LDB(B1, 0, 1); STAGE_W(0, 0, t + 2);
      BAR(); WAIT_L(0); MMAV(0, At, B1); BAR();
      LDA(At, 0, 1); STAGE_C(0, 0, t + 2);
      BAR(); WAIT_L(0); MMAK(1, At, B0); BAR(); SBAR();
      STAGE_W(0, 1, t + 2);
      WAIT_V(6); BAR(); MMAV(1, At, B1); BAR();
      LDB(B0, 1, 0); SBAR(); LDA(At, 1, 0); STAGE_C(0, 1, t + 2);
      WAIT_L(8); BAR(); WAIT_L(0); MMAK(0, At, B0); BAR(); SBAR();
      LDB(B1, 1, 1); STAGE_W(1, 0, t + 3);
      BAR(); WAIT_L(0); MMAV(0, At, B1); BAR();
      LDA(At, 1, 1); STAGE_C(1, 0, t + 3);
      BAR(); WAIT_L(0); MMAK(1, At, B0); BAR(); SBAR();
      STAGE_W(1, 1, t + 3);
      WAIT_V(6); BAR(); MMAV(1, At, B1); BAR();
    }
    {
      const char* wqb_ = (const char*)wq + h * 256;
      glds16_c<0>(voffW, wqb_, lds0 + 131072 + wid * 1024); glds16_c<32 * 8192>(voffW, wqb_, lds0 + 131072 + 8192 + wid * 1024);
      glds16_c<64 * 8192>(voffW, wqb_, lds0 + 147456 + wid * 1024); glds16_c<96 * 8192>(voffW, wqb_, lds0 + 147456 + 8192 + wid * 1024); }
    { LDB(B0, 0, 0); WAIT_V(0); LDA(At, 0, 0); STAGE_C(1, 1, nt - 1);
      BAR(); WAIT_L(0); MMAK(0, At, B0); BAR();
      LDB(B1, 0, 1); BAR(); WAIT_L(0); MMAV(0, At, B1); BAR();
      LDA(At, 0, 1); WAIT_V(4); BAR(); WAIT_L(0); MMAK(1, At, B0); MMAV(1, At, B1); BAR(); }
    { LDB(B0, 1, 0); LDA(At, 1, 0); WAIT_V(2); BAR(); WAIT_L(0); MMAK(0, At, B0); BAR();
      LDB(B1, 1, 1); WAIT_V(0); BAR(); WAIT_L(0); MMAV(0, At, B1); BAR();
      LDA(At, 1, 1); BAR(); WAIT_L(0); MMAK(1, At, B0); MMAV(1, At, B1); BAR(); }
    if (wr == 0) BAR();
    asm volatile("" :: "v"(pfd), "v"(cpf));
#pragma unroll
    for (int ai = 0; ai < 2; ++ai)
#pragma unroll
      for (int m = 0; m < 2; ++m)
#pragma unroll
        for (int sp = 0; sp < 2; ++sp) {
          kvp[((ai * 2 + m) * 2 + sp) * 2] = pack8(accK[ai][m], sp);
          kvp[((ai * 2 + m) * 2 + sp) * 2 + 1] = pack8(accV[ai][m], sp);
          PIN(kvp[((ai * 2 + m) * 2 + sp) * 2]); PIN(kvp[((ai * 2 + m) * 2 + sp) * 2 + 1]);
        }
#undef CBUF
#undef WBUF
#undef STAGE_C
#undef STAGE_W
#undef LDA
#undef LDB
#undef MMAK
#undef MMAV
  }
  SBAR();
  bf16x8 qr[8];
  unsigned kvmask = 0;
  const int lane = lane_id(), tid = wid * 64 + lane;
  const unsigned vl16 = lane * 16;
  {
    const int hh = lane >> 5;
    const char* Kh = ws + WS_KF + (size_t)h * 64 * 8192;
    const char* Vh = ws + WS_VF + (size_t)h * 128 * 4096;
    const unsigned vt2 = (unsigned)(wid * 2048 + lane * 16);
    const unsigned dt2 = lds0 + wid * 2048;
    const int n1 = j >= 4 ? 16 : 4 * j + 2;
#define TILE(g) ((g) < n1 ? (g) : 16 + (g) - n1)
#define DMA_T(src, ldsoff) do { unsigned keep_; asm volatile("s_mov_b32 %0, m0\n\ts_mov_b32 m0, %3\n\ts_nop 2\n\tglobal_load_lds_dwordx4 %1, %2 sc1\n\tglobal_load_lds_dwordx4 %1, %2 offset:1024 sc1\n\ts_mov_b32 m0, %0" \
    : "=&s"(keep_) : "v"(vt2), "s"(src), "s"(dt2 + (ldsoff)) : "memory"); } while (0)
    const int wr = wid >> 2, wc = wid & 3, cls = j & 3;
    const char* kst = ws + WS_KF + ((size_t)(h * 64 + 8 * j + 2 * wr) * 8 + 2 * wc) * 1024;
    const char* vst = ws + WS_VF + ((size_t)(h * 128 + 2 * (8 * j + 2 * wr)) * 4 + wc) * 1024;
#define KVST(i) gstore_sc1_c<(((i) & 1) ? (((i) >> 3) * 8 + (((i) >> 2) & 1) * 2 + (((i) >> 1) & 1)) * 4096 : (((i) >> 3) * 4 + (((i) >> 2) & 1)) * 8192 + (((i) >> 1) & 1) * 1024)>(vl16, kvp[i], ((i) & 1) ? vst : kst)
    unsigned voffW;
    { const int row = 8 * (wid >> 1) + ((lane >> 2) & 7); const int chq = 2 * (wid & 1) + (lane >> 5);
      const int ch = 4 * chq + ((lane & 3) ^ ((row >> 2) & 3)); voffW = (unsigned)(row * 8192 + ch * 16); }
    const char* wqb = (const char*)wq + h * 256;
    const unsigned dstw = lds0 + L_Q + wid * 1024;
#define STAGE_Q1(c, i) glds16_c<(c) * 64 * 8192 + (i) * 32 * 8192>(voffW, wqb, dstw + (((c) + 2) & 3) * 16384 + (i) * 8192)
    const char* xb = ws + WS_XNF + (size_t)(8 * j + wid) * 40 * 1024;
    int boff[2];
    { const int blk = (lane >> 4) & 1, q = (lane & 15) >> 2, p = lane & 3;
#pragma unroll
      for (int t = 0; t < 2; ++t) boff[t] = L_Q + 2048 * hh + 64 * (4 * t + q) + (((2 * blk + (p >> 1)) ^ ((2 * hh + t) & 3)) << 4) + 8 * (p & 1); }
    u32x4 xf[3][4];
    f32x16 acc[4] = {};
    bf16x8 aq[6];
    unsigned fv = 0;
    ldscp bp0 = lds + boff[0], bp1 = lds + boff[1];
#define ARD(u) aq[(u) % 6] = cat8(vtr(bp0 + ((((u) >> 4) + 2) & 3) * 16384 + (((u) >> 2) & 3) * 4096 + ((u) & 3) * 512), vtr(bp1 + ((((u) >> 4) + 2) & 3) * 16384 + (((u) >> 2) & 3) * 4096 + ((u) & 3) * 512))
    GLOAD4(xf[0], vl16, xb);
    GLOAD4(xf[1], vl16, xb + 4096); STAGE_Q1(2, 0); STAGE_Q1(2, 1);
#pragma unroll
    for (int u = 0; u < 5; ++u) ARD(u);
    static_for<10>([&](auto cc_) {
      constexpr int c = decltype(cc_)::value;
#define QW(K) WAITVN(qproj_allowed(c, K))
      if (cls == 0) QW(0); else if (cls == 1) QW(1); else if (cls == 2) QW(2); else QW(3);
      asm volatile("" : "+v"(xf[c % 3][0]), "+v"(xf[c % 3][1]), "+v"(xf[c % 3][2]), "+v"(xf[c % 3][3]) :: "memory");
#undef QW
      if (c == 9 && wid == 0) {
        asm volatile("s_waitcnt vmcnt(0)" : "+v"(fv) :: "memory");
        kvmask = (unsigned)__builtin_amdgcn_ballot_w64(fv != 0u) & 0xffu;
        if (!(kvmask & 1u)) { wait_word(flags + FL_KV + 16 * (8 * h), 1u, flags + FL_TMO); kvmask |= 1u; }
        if (j == 0 && !(kvmask & 16u)) { wait_word(flags + FL_KV + 16 * (8 * h + 4), 1u, flags + FL_TMO); kvmask |= 16u; }
      }
      SBAR(); BAR();
      if (tid == 0 && ((cls == 0 && c == 8) || (cls == 1 && c == 9)))
        __hip_atomic_store(flags + FL_KV + 16 * (8 * h + j), 1u, __ATOMIC_RELAXED, __HIP_MEMORY_SCOPE_AGENT);
      SBAR();
      const char* xn_ = xb + (c + 2) * 4096;
      static_for<16>([&](auto ii_) {
        constexpr int i = decltype(ii_)::value;
        constexpr int u = 16 * c + i;
        acc[i & 3] = MFMA32(aq[u % 6], __builtin_bit_cast(bf16x8, xf[c % 3][i >> 2]), acc[i & 3]);
        if constexpr (u + 5 < 160) ARD(u + 5);
        if (c + 2 < 10) {
          if (i == 1) GLOAD1(xf[(c + 2) % 3][0], vl16, xn_, 0);
          if (i == 3) GLOAD1(xf[(c + 2) % 3][1], vl16, xn_, 1024);
          if (i == 5) GLOAD1(xf[(c + 2) % 3][2], vl16, xn_, 2048);
          if (i == 7) GLOAD1(xf[(c + 2) % 3][3], vl16, xn_, 3072);
        }
        if constexpr (c + 3 < 10) { if constexpr (i == 8) STAGE_Q1(c + 3, 0); if constexpr (i == 9) STAGE_Q1(c + 3, 1); }
        if constexpr (i >= 10) {
          static_for<4>([&](auto kk_) {
            constexpr int k = decltype(kk_)::value;
            constexpr int cnt = qproj_ns(c, k), base = qproj_sbase(c, k);
            if constexpr (((i - 10) & 1) == 0 && (i - 10) / 2 < cnt) { if (cls == k) KVST(base + (i - 10) / 2); }
          });
        }
        if (c == 8 && i == 8 && wid == 0)
          asm volatile("s_nop 4\n\tglobal_load_dword %0, %1, %2 sc1" : "+v"(fv) : "v"((unsigned)(FL_KV + 16 * (8 * h + (lane & 7))) * 4u), "s"(flags) : "memory");
        if (c == 9) {
          if (i == 0) DMA_T(Kh + (size_t)TILE(0) * 16384, L_K);
          if (i == 2) DMA_T(Vh + (size_t)TILE(0) * 16384, L_V);
          if (i == 4) DMA_T(Kh + (size_t)TILE(1) * 16384, L_K + 16384);
          if (i == 6) DMA_T(Vh + (size_t)TILE(1) * 16384, L_V + 16384);
          if (i == 8) DMA_T(Kh + (size_t)TILE(2) * 16384, L_K + 32768);
          if (i == 9) DMA_T(Kh + (size_t)TILE(3) * 16384, L_K + 49152);
        }
        SBAR();
      });
    });
#pragma unroll
    for (int dt = 0; dt < 4; ++dt) { acc[dt] = acc[dt] * QSCALE; qr[2 * dt] = pack8(acc[dt], 0); qr[2 * dt + 1] = pack8(acc[dt], 1); }
    if (cls == 3) WAIT_V(13); else WAIT_V(10);
    BAR();
    { const char* qst = ws + WS_QF + (size_t)(h * 64 + 8 * j + wid) * 8192;
      static_for<8>([&](auto ss_) { constexpr int s = decltype(ss_)::value; gstore_sc1_c<s * 1024>(vl16, qr[s], qst); }); }
#undef KVST
#undef STAGE_Q1
#undef ARD
#undef TILE
#undef DMA_T
  }
  const PfW pfw = {(const char*)wo, (const char*)w1, (const char*)w2};
  if (wid < 4) attn_main<0>(lds, lds0, ws, flags, h, j, wid, lane, tid, qr, kvmask, pfw);
  else attn_main<1>(lds, lds0, ws, flags, h, j, wid, lane, tid, qr, kvmask, pfw);
  WAIT_V(0); WAIT_L(0); BAR();
  if (tid == 0) __hip_atomic_fetch_add(flags + FL_C + 16 * ((7 - j) * 4 + (h >> 3)), 1u, __ATOMIC_RELAXED, __HIP_MEMORY_SCOPE_AGENT);
  tail_phase(smem, x, wo, w1, b1, w2, b2, ws, out, wid);
}

extern "C" void kernel_launch(void* const* d_in, const int* in_sizes, int n_in, void* d_out, int out_size, void* d_ws, size_t ws_size,
                              hipStream_t stream) {
  const u16* x = (const u16*)d_in[0];
  const u16* ckv = (const u16*)d_in[1];
  const u16* wuk = (const u16*)d_in[3];
  const u16* wuv = (const u16*)d_in[4];
  char* ws = (char*)d_ws;
  const u16* wq = (const u16*)d_in[2];
  const u16* wo = (const u16*)d_in[5];
  const u16* w1 = (const u16*)d_in[7];
  const u16* b1 = (const u16*)d_in[8];
  const u16* w2 = (const u16*)d_in[9];
  const u16* b2 = (const u16*)d_in[10];
  (void)hipFuncSetAttribute((const void*)kF1, hipFuncAttributeMaxDynamicSharedMemorySize, F1_LDS);
  hipLaunchKernelGGL(k0_ln, dim3(256), dim3(512), 0, stream, x, (u16*)(ws + WS_XNF), (unsigned*)(ws + WS_FLAGS));
  hipLaunchKernelGGL(kF1, dim3(256), dim3(512), F1_LDS, stream, ckv, wuk, wuv, wq, wo, w1, w2, x, b1, b2, ws, (u16*)d_out);
}
```
